```python
import torch

def _softmax(x, axis=-1):
    return torch.softmax(x, dim=axis)
def _gelu(x):
    return torch.nn.functional.gelu(x, approximate="tanh")
def _silu(x):
    return torch.nn.functional.silu(x)
def _sigmoid(x):
    return torch.sigmoid(x)
def _layernorm(x, axis=-1, eps=1e-5):
    m = x.mean(dim=axis, keepdim=True)
    v = x.var(dim=axis, keepdim=True, unbiased=False)
    return (x - m) / torch.sqrt(v + eps)
def _scatter_add(tgt, idx, src, axis=0):
    return tgt.clone().index_add_(axis, idx, src)
def _segment_sum(src, seg, n):
    out = torch.zeros((n,) + src.shape[1:], dtype=src.dtype)
    return out.index_add_(0, seg, src)
def _pad(x, pw):
    flat = tuple(v for pair in reversed(pw) for v in pair)
    return torch.nn.functional.pad(x, flat)

kernel_name = 'standalone__mha_softmax__near_ridge'

def setup_inputs():

    _w = (lambda a: (lambda t: t.to(torch.bfloat16) if t.is_floating_point() else t)(torch.as_tensor(a)))
    B, H, Sq, Sk, D = 16, 32, 768, 512, 128
    return {
        "q": _w((lambda *s: torch.randn(*s, dtype=torch.bfloat16) * 0.8)(B, H, Sq, D)),
        "k": _w((lambda *s: torch.randn(*s, dtype=torch.bfloat16) * 0.8)(B, H, Sk, D)),
        "v": _w((lambda *s: torch.randn(*s, dtype=torch.bfloat16) * 0.05)(B, H, Sk, D)),
    }

def reference(q, k, v):
    scale = q.shape[-1] ** -0.5
    scores = torch.einsum("bhqd,bhkd->bhqk", q, k) * scale
    p = _softmax(scores, axis=-1)
    return torch.einsum("bhqk,bhkd->bhqd", p, v)
```

```cpp
#include <hip/hip_runtime.h>
#include <hip/hip_bf16.h>
#include <cstdio>

using bf16 = __hip_bfloat16;
constexpr int   D = 128, NW = 8, QBLK = 32, KVBLK = 64;
constexpr float SCALE = 0.088388347648318440f;
#define ATTN_THR 8.f
constexpr float THR = ATTN_THR;
#define ATTN_LAYOUT 0
constexpr int LAYOUT = ATTN_LAYOUT;
#define ATTN_SDEPTH 2
constexpr int SDEPTH = ATTN_SDEPTH;
static_assert(SDEPTH == 1 || SDEPTH == 2, "SDEPTH is 1 or 2");
#define ATTN_B 16
#define ATTN_HQ 32
#define ATTN_HKV 32
#define ATTN_S 768
#define ATTN_SKV 512
constexpr int B = ATTN_B, HQ = ATTN_HQ, HKV = ATTN_HKV, S = ATTN_S, SKV = ATTN_SKV;
constexpr int LDQ = LAYOUT ? HQ * D : D, LDK = LAYOUT ? HKV * D : D, LDO = LDQ;
static_assert(S % (QBLK * NW) == 0 && SKV % (2 * KVBLK) == 0 && HQ % HKV == 0, "S % 256 == 0, SKV % 128 == 0 (even tile count), HQ % HKV == 0");
constexpr size_t SHM_V = KVBLK * D * 2, SHM_K = KVBLK * D * 2, SHM_ATTN = 2 * SHM_V + 2 * SHM_K + NW * 64 * 4;

using bf16x8 = __attribute__((ext_vector_type(8))) short;
using s16x4  = __attribute__((ext_vector_type(4))) short;
using f32x16 = __attribute__((ext_vector_type(16))) float;
using f32x8  = __attribute__((ext_vector_type(8))) float;
using u32x4  = __attribute__((ext_vector_type(4))) unsigned;
#define KSWZ(row, colB) ((row) * 256 + ((colB) ^ (((row) & 7) << 4)))
#define SBAR() __builtin_amdgcn_sched_barrier(0)
__device__ __forceinline__ int crow(int r, int hi) { return (r & 3) + 8 * (r >> 2) + 4 * hi; }
__device__ __forceinline__ unsigned cvtpk(float lo, float hi) {
  unsigned r; asm volatile("v_cvt_pk_bf16_f32 %0, %1, %2" : "=v"(r) : "v"(lo), "v"(hi)); return r;
}
template <typename TIn> struct Stage;
template <> struct Stage<bf16>  { using T = bf16x8;
  __device__ static __forceinline__ T ld8(const bf16* p) { return *reinterpret_cast<const bf16x8*>(p); }
  __device__ static __forceinline__ bf16x8 tobf(T x) { return x; } };
template <> struct Stage<float> { using T = f32x8;
  __device__ static __forceinline__ T ld8(const float* p) { return *reinterpret_cast<const f32x8*>(p); }
  __device__ static __forceinline__ bf16x8 tobf(T x) {
    u32x4 w = {cvtpk(x[0], x[1]), cvtpk(x[2], x[3]), cvtpk(x[4], x[5]), cvtpk(x[6], x[7])}; return *reinterpret_cast<bf16x8*>(&w); } };

__device__ __forceinline__ void partialSM(f32x16& p0, f32x16& p1, float& m_reg, float& mn, float& alpha) {
  constexpr float C = SCALE * 1.4426950408889634f;
  float pmax = p0[0]; for (int r = 1; r < 16; ++r) pmax = fmaxf(pmax, p0[r]); for (int r = 0; r < 16; ++r) pmax = fmaxf(pmax, p1[r]);
  { auto rr = __builtin_amdgcn_permlane32_swap(__float_as_uint(pmax), __float_as_uint(pmax), false, false);
    pmax = fmaxf(__uint_as_float(rr[0]), __uint_as_float(rr[1])); }
  if (__builtin_expect(__all(pmax - m_reg <= THR / SCALE), 1)) { mn = m_reg; alpha = 1.f; }
  else { mn = fmaxf(m_reg, pmax); alpha = __builtin_amdgcn_exp2f((m_reg - mn) * C); m_reg = mn; }
  float mnC = -mn * C;
  for (int r = 0; r < 16; ++r) p0[r] = fmaf(p0[r], C, mnC); for (int r = 0; r < 16; ++r) p1[r] = fmaf(p1[r], C, mnC);
  for (int r = 0; r < 16; ++r) p0[r] = __builtin_amdgcn_exp2f(p0[r]);
}
__device__ __forceinline__ void finishSM(f32x16& p0, f32x16& p1, float alpha, float& l_reg, bf16x8& pa0, bf16x8& pa1, bf16x8& pa2, bf16x8& pa3) {
  for (int r = 0; r < 16; ++r) p1[r] = __builtin_amdgcn_exp2f(p1[r]);
  float ps = 0; for (int r = 0; r < 16; ++r) ps += p0[r]; for (int r = 0; r < 16; ++r) ps += p1[r];
  { auto rr = __builtin_amdgcn_permlane32_swap(__float_as_uint(ps), __float_as_uint(ps), false, false);
    ps = __uint_as_float(rr[0]) + __uint_as_float(rr[1]); }
  l_reg = l_reg * alpha + ps;
#define PK4(P, BASE, OUT) do { unsigned a0 = cvtpk(P[BASE + 0], P[BASE + 1]), a1 = cvtpk(P[BASE + 2], P[BASE + 3]);   \
    unsigned b0 = cvtpk(P[BASE + 4], P[BASE + 5]), b1 = cvtpk(P[BASE + 6], P[BASE + 7]);                              \
    auto r0 = __builtin_amdgcn_permlane32_swap(a0, b0, false, false); auto r1 = __builtin_amdgcn_permlane32_swap(a1, b1, false, false); \
    u32x4 w = {r0[0], r1[0], r0[1], r1[1]}; OUT = *reinterpret_cast<bf16x8*>(&w); } while (0)
  PK4(p0, 0, pa0); PK4(p0, 8, pa1); PK4(p1, 0, pa2); PK4(p1, 8, pa3);
#undef PK4
}
__device__ __forceinline__ void qkt(f32x16& p0, f32x16& p1, const bf16* Ks, const bf16x8* qr, int r32, int hi) {
  p0 = f32x16{}; p1 = f32x16{};
  for (int d0 = 0; d0 < 8; ++d0) { int cb = (d0 * 16 + hi * 8) * 2;
    bf16x8 b0 = *reinterpret_cast<const bf16x8*>((const char*)Ks + KSWZ(r32, cb));
    bf16x8 b1 = *reinterpret_cast<const bf16x8*>((const char*)Ks + KSWZ(32 + r32, cb));
    p0 = __builtin_amdgcn_mfma_f32_32x32x16_bf16(b0, qr[d0], p0, 0, 0, 0);
    p1 = __builtin_amdgcn_mfma_f32_32x32x16_bf16(b1, qr[d0], p1, 0, 0, 0); }
}
__device__ __forceinline__ int v_st(int k, int c) { const int kk = (k & ~0xC) | ((k & 4) << 1) | ((k & 8) >> 1); return ((kk >> 3) * 4 + (c >> 5)) * 512 + ((kk & 7) * 32 + (c & 31)) * 2; }
__device__ __forceinline__ int v_rd_base(int lane) { return ((lane & 3) << 3) | (((lane >> 2) & 3) << 6) | (((lane >> 4) & 1) << 5) | (((lane >> 5) & 1) << 8); }
constexpr int v_rd_off(int d0, int ks, int half) { return d0 * 512 + ks * 4096 + half * 2048; }
template <int OFF> __device__ __forceinline__ s16x4 tr_read(int vb) {
  s16x4 r; asm volatile("ds_read_b64_tr_b16 %0, %1 offset:%2" : "=&v"(r) : "v"(vb), "i"(OFF) : "memory"); return r;
}
template <int D0> __device__ __forceinline__ void pv_one(f32x16& od, int vb, bf16x8 pa0, bf16x8 pa1, bf16x8 pa2, bf16x8 pa3) {
  const s16x4 l0 = tr_read<v_rd_off(D0, 0, 0)>(vb), h0 = tr_read<v_rd_off(D0, 0, 1)>(vb), l1 = tr_read<v_rd_off(D0, 1, 0)>(vb), h1 = tr_read<v_rd_off(D0, 1, 1)>(vb);
  const s16x4 l2 = tr_read<v_rd_off(D0, 2, 0)>(vb), h2 = tr_read<v_rd_off(D0, 2, 1)>(vb), l3 = tr_read<v_rd_off(D0, 3, 0)>(vb), h3 = tr_read<v_rd_off(D0, 3, 1)>(vb);
  asm volatile("s_waitcnt lgkmcnt(0)" ::: "memory"); SBAR();
#define PK(L, H) (bf16x8){L[0], L[1], L[2], L[3], H[0], H[1], H[2], H[3]}
  od = __builtin_amdgcn_mfma_f32_32x32x16_bf16(pa0, PK(l0, h0), od, 0, 0, 0);
  od = __builtin_amdgcn_mfma_f32_32x32x16_bf16(pa1, PK(l1, h1), od, 0, 0, 0);
  od = __builtin_amdgcn_mfma_f32_32x32x16_bf16(pa2, PK(l2, h2), od, 0, 0, 0);
  od = __builtin_amdgcn_mfma_f32_32x32x16_bf16(pa3, PK(l3, h3), od, 0, 0, 0);
#undef PK
}
__device__ __forceinline__ void pv_d0(f32x16* o, int vb, bf16x8 pa0, bf16x8 pa1, bf16x8 pa2, bf16x8 pa3) {
  pv_one<0>(o[0], vb, pa0, pa1, pa2, pa3); pv_one<1>(o[1], vb, pa0, pa1, pa2, pa3); pv_one<2>(o[2], vb, pa0, pa1, pa2, pa3); pv_one<3>(o[3], vb, pa0, pa1, pa2, pa3);
}

template <typename TQ>
__device__ __forceinline__ void attn_dense_body(const TQ* __restrict__ Qb, const bf16* __restrict__ Kh, const bf16* __restrict__ Vh,
                                                bf16* __restrict__ Ob, int seq, char* lds) {
  using St = Stage<bf16>; using SQ = Stage<TQ>;
  const int tid = threadIdx.x, wid = tid >> 6, lane = tid & 63, r32 = lane & 31, hi = lane >> 5;
  bf16* V_lds = (bf16*)lds; bf16* K_lds = (bf16*)(lds + 2 * SHM_V);
  float* ws = (float*)(lds + 2 * SHM_V + 2 * SHM_K) + wid * 64; float* li_l = ws; float* al_l = ws + 32;
  float m_reg = -1e30f, l_reg = 0; f32x16 o[4] = {}; bf16x8 qr[8];
  const TQ* Qw = Qb + (long)(wid * QBLK + r32) * LDQ + hi * 8;
#pragma unroll
  for (int d0 = 0; d0 < 8; ++d0) qr[d0] = SQ::tobf(SQ::ld8(Qw + d0 * 16));
  const int sr = tid >> 4, sc = (tid & 15) * 8, vst0 = v_st(sr, sc), vst1 = v_st(32 + sr, sc);
  const int vb0 = (int)(uintptr_t)V_lds + v_rd_base(lane);
  struct { typename St::T vs0, vs1, ks0, ks1; } sr_[SDEPTH];
#define SLOAD(i, k0) do { sr_[i].vs0 = St::ld8(&Vh[(long)((k0) + sr) * LDK + sc]); sr_[i].vs1 = St::ld8(&Vh[(long)((k0) + 32 + sr) * LDK + sc]); \
    sr_[i].ks0 = St::ld8(&Kh[(long)((k0) + sr) * LDK + sc]); sr_[i].ks1 = St::ld8(&Kh[(long)((k0) + 32 + sr) * LDK + sc]); } while (0)
#define SWRITE(b, i) do { *(bf16x8*)((char*)V_lds + (b) * SHM_V + vst0) = St::tobf(sr_[i].vs0);          \
    *(bf16x8*)((char*)V_lds + (b) * SHM_V + vst1) = St::tobf(sr_[i].vs1); int kc = sc * 2;               \
    *(bf16x8*)((char*)K_lds + (b) * SHM_K + KSWZ(sr, kc)) = St::tobf(sr_[i].ks0);                       \
    *(bf16x8*)((char*)K_lds + (b) * SHM_K + KSWZ(32 + sr, kc)) = St::tobf(sr_[i].ks1); } while (0)
#define SWAIT() do { if constexpr (SDEPTH == 2) asm volatile("s_waitcnt vmcnt(4)" ::: "memory"); else asm volatile("s_waitcnt vmcnt(0)" ::: "memory"); } while (0)
#define RESC(a) do { if (__any((a) < 1.f)) { if (hi == 0) al_l[r32] = (a); asm volatile("s_waitcnt lgkmcnt(0)" ::: "memory"); \
    for (int d = 0; d < 4; ++d) for (int r = 0; r < 16; ++r) o[d][r] *= al_l[crow(r, hi)]; } } while (0)
  f32x16 pA0, pA1, pB0, pB1; float mnA, mnB, alA, alB; bf16x8 pa0, pa1, pa2, pa3; const int NT = seq / KVBLK;
  constexpr int SE = 0, SO = SDEPTH - 1;
  SLOAD(SE, 0); asm volatile("s_waitcnt vmcnt(0)" ::: "memory"); SWRITE(0, SE); __syncthreads();
  qkt(pA0, pA1, K_lds, qr, r32, hi); partialSM(pA0, pA1, m_reg, mnA, alA);
  SLOAD(SO, KVBLK); if constexpr (SDEPTH == 2) { if (2 < NT) SLOAD(SE, 2 * KVBLK); }
  SWAIT(); SWRITE(1, SO); __syncthreads();
  for (int j = 1; j + 1 < NT; j += 2) {
    SBAR(); qkt(pB0, pB1, (bf16*)((char*)K_lds + SHM_K), qr, r32, hi);
    finishSM(pA0, pA1, alA, l_reg, pa0, pa1, pa2, pa3); SBAR();
    SLOAD(SO, (j + SDEPTH) * KVBLK); SBAR();
    pv_d0(o, vb0, pa0, pa1, pa2, pa3); partialSM(pB0, pB1, m_reg, mnB, alB);
    __syncthreads(); SWAIT(); SWRITE(0, SE);
    RESC(alB); __syncthreads();
    SBAR(); qkt(pA0, pA1, K_lds, qr, r32, hi);
    finishSM(pB0, pB1, alB, l_reg, pa0, pa1, pa2, pa3); SBAR();
    if (SDEPTH == 1 || j + 3 < NT) SLOAD(SE, (j + 1 + SDEPTH) * KVBLK); SBAR();
    pv_d0(o, vb0 + (int)SHM_V, pa0, pa1, pa2, pa3); partialSM(pA0, pA1, m_reg, mnA, alA);
    __syncthreads(); SWAIT(); SWRITE(1, SO);
    RESC(alA); __syncthreads();
  }
  SBAR(); qkt(pB0, pB1, (bf16*)((char*)K_lds + SHM_K), qr, r32, hi);
  finishSM(pA0, pA1, alA, l_reg, pa0, pa1, pa2, pa3); SBAR();
  pv_d0(o, vb0, pa0, pa1, pa2, pa3); partialSM(pB0, pB1, m_reg, mnB, alB);
  __syncthreads(); RESC(alB);
  finishSM(pB0, pB1, alB, l_reg, pa0, pa1, pa2, pa3); SBAR();
  pv_d0(o, vb0 + (int)SHM_V, pa0, pa1, pa2, pa3);
  if (hi == 0) li_l[r32] = l_reg; asm volatile("s_waitcnt lgkmcnt(0)" ::: "memory");
  float rli[16];
#pragma unroll
  for (int r = 0; r < 16; ++r) rli[r] = __builtin_amdgcn_rcpf(li_l[crow(r, hi)]);
  bf16* Ow = Ob + (long)(wid * QBLK) * LDO;
#pragma unroll
  for (int r = 0; r < 16; ++r) { int orow = crow(r, hi);
    for (int d0 = 0; d0 < 4; ++d0) Ow[(long)orow * LDO + d0 * 32 + r32] = __float2bfloat16(o[d0][r] * rli[r]); }
#undef SLOAD
#undef SWRITE
#undef SWAIT
#undef RESC
}

template <typename TQ>
__global__ __launch_bounds__(64 * NW, 1)
void attn_fwd_dense(const TQ* __restrict__ Q, const bf16* __restrict__ K, const bf16* __restrict__ V, bf16* __restrict__ O, int sq, int skv, int GQA) {
  extern __shared__ __attribute__((aligned(16))) char shm[];
  const int id = blockIdx.x, xcd = id & 7, j = id >> 3, head = xcd * 64 + j / 3, qb = j % 3; const long b = head >> 5, h = head & 31, Hq = 32, Hkv = 32, kvh = h, i0 = (long)qb * (QBLK * NW);
  if constexpr (LAYOUT == 0) {
    const long q0 = ((b * Hq + h) * sq + i0) * D, k0 = (b * Hkv + kvh) * skv * (long)D;
    attn_dense_body<TQ>(Q + q0, K + k0, V + k0, O + q0, skv, shm);
  } else {
    const long q0 = ((b * sq + i0) * HQ + h) * (long)D, k0 = (b * skv * HKV + kvh) * (long)D;
    attn_dense_body<TQ>(Q + q0, K + k0, V + k0, O + q0, skv, shm);
  }
}
__global__ __launch_bounds__(256) void cvt_f32_bf16(const float* __restrict__ a, bf16* __restrict__ ao, long na8,
                                                   const float* __restrict__ b, bf16* __restrict__ bo, long nb8) {
  for (long i = blockIdx.x * 256L + threadIdx.x; i < na8 + nb8; i += gridDim.x * 256L) {
    const bool inA = i < na8; const long j = inA ? i : i - na8;
    const f32x8 x = *reinterpret_cast<const f32x8*>((inA ? a : b) + j * 8);
    *reinterpret_cast<bf16x8*>((inA ? ao : bo) + j * 8) = Stage<float>::tobf(x);
  }
}

extern "C" void kernel_launch(void* const* d_in, const int* in_sizes, int n_in, void* d_out, int out_size, void* d_ws, size_t ws_size, hipStream_t stream) {
  hipFuncSetAttribute((const void*)attn_fwd_dense<bf16>, hipFuncAttributeMaxDynamicSharedMemorySize, (int)SHM_ATTN);
  hipLaunchKernelGGL(attn_fwd_dense<bf16>, dim3(1536), dim3(64 * NW), SHM_ATTN, stream,
                     (const bf16*)d_in[0], (const bf16*)d_in[1], (const bf16*)d_in[2], (bf16*)d_out, S, SKV, HQ / HKV);
}
```

```cpp
#include <hip/hip_runtime.h>
#include <hip/hip_bf16.h>

typedef float  f32x4  __attribute__((ext_vector_type(4)));
typedef __bf16 bf16x8 __attribute__((ext_vector_type(8)));
typedef __bf16 bf16x4 __attribute__((ext_vector_type(4)));

__device__ __forceinline__ void slow_rows(const char* __restrict__ Qr, const char* __restrict__ Kh, const char* __restrict__ Vh, char* __restrict__ Or) {
  const unsigned l = __builtin_amdgcn_mbcnt_hi(~0u, __builtin_amdgcn_mbcnt_lo(~0u, 0u)), i = l & 15u, g = l >> 4;
  const float C = 0.12751743082f;
#pragma unroll 1
  for (int qb = 0; qb < 6; ++qb) {
    bf16x8 qf[4];
#pragma unroll
    for (int s = 0; s < 4; ++s) qf[s] = *(const bf16x8*)(Qr + qb * 4096 + i * 256 + 64 * s + 16 * g);
    float m = -__builtin_inff();
#pragma unroll 1
    for (int kb = 0; kb < 32; ++kb) {
      f32x4 sc = {0.f, 0.f, 0.f, 0.f};
#pragma unroll
      for (int s = 0; s < 4; ++s) sc = __builtin_amdgcn_mfma_f32_16x16x32_bf16(*(const bf16x8*)(Kh + (16 * kb + i) * 256 + 64 * s + 16 * g), qf[s], sc, 0, 0, 0);
      m = fmaxf(fmaxf(fmaxf(m, sc[0]), fmaxf(sc[1], sc[2])), sc[3]);
    }
    m = fmaxf(m, __shfl_xor(m, 16)); m = fmaxf(m, __shfl_xor(m, 32));
    f32x4 o[8];
#pragma unroll
    for (int db = 0; db < 8; ++db) o[db] = f32x4{0.f, 0.f, 0.f, 0.f};
    float sum = 0.f;
#pragma unroll 1
    for (int st = 0; st < 16; ++st) {
      bf16x8 p;
#pragma unroll
      for (int kb = 0; kb < 2; ++kb) {
        f32x4 sc = {0.f, 0.f, 0.f, 0.f};
#pragma unroll
        for (int s = 0; s < 4; ++s) sc = __builtin_amdgcn_mfma_f32_16x16x32_bf16(*(const bf16x8*)(Kh + (32 * st + 16 * kb + i) * 256 + 64 * s + 16 * g), qf[s], sc, 0, 0, 0);
#pragma unroll
        for (int j = 0; j < 4; ++j) { const __bf16 e = (__bf16)__builtin_amdgcn_exp2f((sc[j] - m) * C); p[4 * kb + j] = e; sum += (float)e; }
      }
#pragma unroll
      for (int db = 0; db < 8; ++db) {
        bf16x8 vf;
#pragma unroll
        for (int j = 0; j < 8; ++j) vf[j] = *(const __bf16*)(Vh + (32 * st + 16 * (j >> 2) + 4 * g + (j & 3)) * 256 + (16 * db + i) * 2);
        o[db] = __builtin_amdgcn_mfma_f32_16x16x32_bf16(vf, p, o[db], 0, 0, 0);
      }
    }
    sum += __shfl_xor(sum, 16); sum += __shfl_xor(sum, 32);
    const float r = 1.f / sum;
#pragma unroll
    for (int db = 0; db < 8; ++db) {
      bf16x4 w = {(__bf16)(o[db][0] * r), (__bf16)(o[db][1] * r), (__bf16)(o[db][2] * r), (__bf16)(o[db][3] * r)};
      *(bf16x4*)(Or + qb * 4096 + i * 256 + (16 * db + 4 * g) * 2) = w;
    }
  }
}

__global__ __launch_bounds__(256, 1) void mha_fwd_kernel(const char* __restrict__ Q, const char* __restrict__ K, const char* __restrict__ V, char* __restrict__ O) {
  extern __shared__ __attribute__((aligned(16))) char lds[];
  const unsigned w = __builtin_amdgcn_readfirstlane(threadIdx.x >> 6);
  const unsigned xcd = blockIdx.x & 7u, c = blockIdx.x >> 3;
  unsigned kv[4], qo[4];
#pragma unroll
  for (int r = 0; r < 4; ++r) {
    const unsigned j = 32u * r + c, hd = (j >> 1) * 8u + xcd;
    kv[r] = hd * 131072u + (w >> 1) * 4096u + (w & 1u) * 128u + ((hd) & 15u) * 8192u;
    qo[r] = (hd * 768u + (j & 1u) * 384u + w * 96u) * 256u;
  }
  const unsigned ldsb = (unsigned)(size_t)lds;
  unsigned fail;
  asm volatile(
    "v_mbcnt_lo_u32_b32 v200, -1, 0\n\t"
    "v_mbcnt_hi_u32_b32 v200, -1, v200\n\t"
    "v_bfe_u32 v201, v200, 2, 3\n\t"
    "v_lshlrev_b32 v226, 8, v201\n\t"
    "v_lshrrev_b32 v201, 5, v200\n\t"
    "v_lshl_add_u32 v226, v201, 6, v226\n\t"
    "v_lshrrev_b32 v201, 3, v200\n\t"
    "v_and_b32 v201, 2, v201\n\t"
    "v_and_b32 v202, 3, v200\n\t"
    "v_xor_b32 v201, v201, v202\n\t"
    "v_lshl_add_u32 v226, v201, 4, v226\n\t"
    "s_mov_b64 s[44:45], %1\n\t"
    "s_and_b32 s45, s45, 0xffff\n\t"
    "s_mov_b32 s46, 100663296\n\t"
    "s_mov_b32 s47, 0x20000\n\t"
    "s_mov_b32 s69, %15\n\t"
    "s_mov_b32 s53, %10\n\t"
    "s_bitcmp1_b32 %16, 0\n\t"
    "s_cbranch_scc0 .Lgo%=\n\t"
    "s_sleep 45\n.Lgo%=:\n\t"
    "s_add_u32 s65, s53, 0\n\t"
    "s_add_u32 m0, s69, 0\n\t"
    "s_nop 0\n\t"
    "buffer_load_dwordx4 v226, s[44:47], s65 offen offset:0 nt lds\n\t"
    "buffer_load_dwordx4 v226, s[44:47], s65 offen offset:2048 nt lds\n\t"
    "s_add_u32 s65, s53, 128\n\t"
    "s_add_u32 m0, s69, 1024\n\t"
    "s_nop 0\n\t"
    "buffer_load_dwordx4 v226, s[44:47], s65 offen offset:0 nt lds\n\t"
    "buffer_load_dwordx4 v226, s[44:47], s65 offen offset:2048 nt lds\n\t"
    "s_getpc_b64 s[96:97]\n.Lpc%=:\n\t"
    "v_lshlrev_b32 v201, 7, v200\n\t"
    "v_add_u32 v201, %16, v201\n\t"
    "v_and_b32 v201, -4, v201\n\t"
    "v_add_u32 v202, 0x8000, v201\n\t"
    "v_min_u32 v202, .Lend%=-.Lpc%=-68, v202\n\t"
    "v_min_u32 v201, .Lend%=-.Lpc%=-68, v201\n\t"
    "global_load_dword a252, v201, s[96:97] offset:0\n\t"
    "global_load_dword a252, v201, s[96:97] offset:64\n\t"
    "global_load_dword a252, v202, s[96:97] offset:0\n\t"
    "global_load_dword a252, v202, s[96:97] offset:64\n\t"
    "s_add_u32 s65, s53, 4096\n\t"
    "s_add_u32 m0, s69, 4096\n\t"
    "s_nop 0\n\t"
    "buffer_load_dwordx4 v226, s[44:47], s65 offen offset:0 nt lds\n\t"
    "buffer_load_dwordx4 v226, s[44:47], s65 offen offset:2048 nt lds\n\t"
    "s_add_u32 s65, s53, 4224\n\t"
    "s_add_u32 m0, s69, 5120\n\t"
    "s_nop 0\n\t"
    "buffer_load_dwordx4 v226, s[44:47], s65 offen offset:0 nt lds\n\t"
    "buffer_load_dwordx4 v226, s[44:47], s65 offen offset:2048 nt lds\n\t"
    "s_mov_b64 s[36:37], %2\n\t"
    "s_and_b32 s37, s37, 0xffff\n\t"
    "s_mov_b32 s38, 67108864\n\t"
    "s_mov_b32 s39, 0x20000\n\t"
    "s_mov_b64 s[40:41], %3\n\t"
    "s_and_b32 s41, s41, 0xffff\n\t"
    "s_mov_b32 s42, 67108864\n\t"
    "s_mov_b32 s43, 0x20000\n\t"
    "s_mov_b32 s63, %5\n\t"
    "s_mov_b32 s52, %6\n\t"
    "s_xor_b32 s64, s52, 0\n\t"
    "s_add_u32 m0, s63, 0\n\t"
    "s_nop 0\n\t"
    "buffer_load_dwordx4 v226, s[36:39], s64 offen offset:0 lds\n\t"
    "buffer_load_dwordx4 v226, s[36:39], s64 offen offset:2048 lds\n\t"
    "s_add_u32 m0, s63, 32768\n\t"
    "s_nop 0\n\t"
    "buffer_load_dwordx4 v226, s[40:43], s64 offen offset:0 lds\n\t"
    "buffer_load_dwordx4 v226, s[40:43], s64 offen offset:2048 lds\n\t"
    "s_xor_b32 s64, s52, 8192\n\t"
    "s_add_u32 m0, s63, 8192\n\t"
    "s_nop 0\n\t"
    "buffer_load_dwordx4 v226, s[36:39], s64 offen offset:0 lds\n\t"
    "buffer_load_dwordx4 v226, s[36:39], s64 offen offset:2048 lds\n\t"
    "s_add_u32 m0, s63, 40960\n\t"
    "s_nop 0\n\t"
    "buffer_load_dwordx4 v226, s[40:43], s64 offen offset:0 lds\n\t"
    "buffer_load_dwordx4 v226, s[40:43], s64 offen offset:2048 lds\n\t"
    "s_mov_b64 s[48:49], %4\n\t"
    "s_and_b32 s49, s49, 0xffff\n\t"
    "s_mov_b32 s50, 100663296\n\t"
    "s_mov_b32 s51, 0x20000\n\t"
    "v_bfe_u32 v201, v200, 3, 1\n\t"
    "v_lshlrev_b32 v224, 11, v201\n\t"
    "v_and_b32 v201, 7, v200\n\t"
    "v_lshl_add_u32 v224, v201, 6, v224\n\t"
    "v_lshrrev_b32 v201, 1, v200\n\t"
    "v_and_b32 v201, 2, v201\n\t"
    "v_lshrrev_b32 v202, 4, v200\n\t"
    "v_xor_b32 v201, v201, v202\n\t"
    "v_lshl_add_u32 v224, v201, 4, v224\n\t"
    "v_add_u32 v229, s69, v224\n\t"
    "v_add_u32 v224, %14, v224\n\t"
    "v_lshrrev_b32 v201, 5, v200\n\t"
    "v_lshlrev_b32 v233, 11, v201\n\t"
    "v_and_b32 v201, 31, v200\n\t"
    "v_lshl_add_u32 v233, v201, 4, v233\n\t"
    "v_add_u32 v233, s69, v233\n\t"
    "v_lshrrev_b32 v201, 5, v200\n\t"
    "v_lshlrev_b32 v225, 11, v201\n\t"
    "v_bfe_u32 v201, v200, 2, 3\n\t"
    "v_lshl_add_u32 v225, v201, 6, v225\n\t"
    "v_lshrrev_b32 v201, 3, v200\n\t"
    "v_and_b32 v201, 2, v201\n\t"
    "v_and_b32 v202, 3, v200\n\t"
    "v_xor_b32 v201, v201, v202\n\t"
    "v_lshl_add_u32 v225, v201, 4, v225\n\t"
    "v_add_u32 v225, %14, v225\n\t"
    "v_add_u32 v225, 32768, v225\n\t"
    "v_lshlrev_b32 v227, 4, v200\n\t"
    "v_and_b32 v201, 15, v200\n\t"
    "v_lshlrev_b32 v230, 8, v201\n\t"
    "v_lshrrev_b32 v202, 4, v200\n\t"
    "v_lshlrev_b32 v232, 8, v202\n\t"
    "v_xor_b32 v201, v201, v202\n\t"
    "v_lshl_add_u32 v232, v201, 4, v232\n\t"
    "v_and_b32 v201, 3, v201\n\t"
    "v_lshl_add_u32 v230, v201, 4, v230\n\t"
    "v_bfe_u32 v201, v200, 2, 1\n\t"
    "v_lshl_add_u32 v230, v201, 6, v230\n\t"
    "v_xor_b32 v231, 64, v230\n\t"
    "v_xor_b32 v228, 64, v227\n\t"
    "v_add_u32 v231, s69, v231\n\t"
    "v_add_u32 v230, s69, v230\n\t"
    "v_add_u32 v232, s69, v232\n\t"
    "v_mov_b32 v201, 0x3f803f80\n\t"
    "v_and_b32 v202, 3, v200\n\t"
    "v_cmp_eq_u32 vcc, 0, v202\n\t"
    "s_nop 1\n\t"
    "v_cndmask_b32 v201, 0, v201, vcc\n\t"
    "v_accvgpr_write_b32 a248, v201\n\t"
    "v_accvgpr_write_b32 a249, v201\n\t"
    "v_accvgpr_write_b32 a250, v201\n\t"
    "v_accvgpr_write_b32 a251, v201\n\t"
    "s_mov_b32 s60, 0x3e0293ee\n\t"
    "s_mov_b32 s61, 0xbe0293ee\n\t"
    "s_mov_b32 s62, 0x32000000\n\t"
    "s_mov_b32 s56, 0\n\t"
    "s_mov_b32 s55, 0\n\t"
    "s_mov_b32 s68, 1\n\t"
    "s_mov_b64 s[58:59], 0\n\t"
    "s_mov_b32 s57, 4\n\t"
    "s_mov_b32 s54, 0xc0000000\n\t"
    "s_mov_b32 s66, 0xc0000000\n\t"
    "s_mov_b32 s71, %7\n\t"
    "s_mov_b32 s72, %8\n\t"
    "s_mov_b32 s73, %9\n\t"
    "s_mov_b32 s76, %10\n\t"
    "s_mov_b32 s77, %11\n\t"
    "s_mov_b32 s78, %12\n\t"
    "s_mov_b32 s79, %13\n\t"
    "v_and_b32 v239, 0x30, v227\n\t"
    "v_bfe_u32 v238, v227, 8, 2\n\t"
    "v_lshl_add_u32 v239, v239, 2, %14\n\t"
    "v_lshl_add_u32 v239, v238, 4, v239\n\t"
    "s_xor_b32 s64, s52, 16384\n\t"
    "s_add_u32 m0, s63, 16384\n\t"
    "s_nop 0\n\t"
    "buffer_load_dwordx4 v226, s[36:39], s64 offen offset:0 lds\n\t"
    "buffer_load_dwordx4 v226, s[36:39], s64 offen offset:2048 lds\n\t"
    "s_add_u32 m0, s63, 49152\n\t"
    "s_nop 0\n\t"
    "buffer_load_dwordx4 v226, s[40:43], s64 offen offset:0 lds\n\t"
    "buffer_load_dwordx4 v226, s[40:43], s64 offen offset:2048 lds\n\t"
    "s_add_u32 s65, s53, 16384\n\t"
    "s_add_u32 m0, s69, 16384\n\t"
    "s_nop 0\n\t"
    "buffer_load_dwordx4 v226, s[44:47], s65 offen offset:0 nt lds\n\t"
    "buffer_load_dwordx4 v226, s[44:47], s65 offen offset:2048 nt lds\n\t"
    "s_add_u32 s65, s53, 16512\n\t"
    "s_add_u32 m0, s69, 17408\n\t"
    "s_nop 0\n\t"
    "buffer_load_dwordx4 v226, s[44:47], s65 offen offset:0 nt lds\n\t"
    "buffer_load_dwordx4 v226, s[44:47], s65 offen offset:2048 nt lds\n\t"
    "s_waitcnt vmcnt(16)\n\t"
    "ds_read_b128 a[64:67], v229 offset:0\n\t"
    "ds_read_b128 a[68:71], v229 offset:512\n\t"
    "ds_read_b128 a[72:75], v229 offset:1024\n\t"
    "ds_read_b128 a[76:79], v229 offset:1536\n\t"
    "ds_read_b128 a[80:83], v229 offset:4096\n\t"
    "ds_read_b128 a[84:87], v229 offset:4608\n\t"
    "ds_read_b128 a[88:91], v229 offset:5120\n\t"
    "ds_read_b128 a[92:95], v229 offset:5632\n\t"
    "s_waitcnt vmcnt(12)\n\t"
    "s_barrier\n\t"
    "ds_read_b128 a[48:51], v239 offset:0\n\t"
    "ds_read_b128 a[52:55], v239 offset:512\n\t"
    "ds_read_b128 a[56:59], v239 offset:1024\n\t"
    "ds_read_b128 a[60:63], v239 offset:1536\n\t"
    "ds_read_b128 a[160:163], v224 offset:0\n\t"
    "ds_read_b128 a[164:167], v224 offset:512\n\t"
    "ds_read_b128 a[168:171], v224 offset:1024\n\t"
    "ds_read_b128 a[172:175], v224 offset:1536\n\t"
    "ds_read_b128 a[176:179], v224 offset:4096\n\t"
    "ds_read_b128 a[180:183], v224 offset:4608\n\t"
    "ds_read_b128 a[184:187], v224 offset:5120\n\t"
    "ds_read_b128 a[188:191], v224 offset:5632\n\t"
    "ds_read_b64_tr_b16 a[192:193], v225 offset:0\n\t"
    "ds_read_b64_tr_b16 a[194:195], v225 offset:4096\n\t"
    "ds_read_b64_tr_b16 a[196:197], v225 offset:8\n\t"
    "ds_read_b64_tr_b16 a[198:199], v225 offset:4104\n\t"
    "ds_read_b64_tr_b16 a[200:201], v225 offset:512\n\t"
    "ds_read_b64_tr_b16 a[202:203], v225 offset:4608\n\t"
    "ds_read_b64_tr_b16 a[204:205], v225 offset:520\n\t"
    "ds_read_b64_tr_b16 a[206:207], v225 offset:4616\n\t"
    "ds_read_b64_tr_b16 a[208:209], v225 offset:1024\n\t"
    "ds_read_b64_tr_b16 a[210:211], v225 offset:5120\n\t"
    "ds_read_b64_tr_b16 a[212:213], v225 offset:1032\n\t"
    "ds_read_b64_tr_b16 a[214:215], v225 offset:5128\n\t"
    "ds_read_b64_tr_b16 a[216:217], v225 offset:1536\n\t"
    "ds_read_b64_tr_b16 a[218:219], v225 offset:5632\n\t"
    "ds_read_b64_tr_b16 a[220:221], v225 offset:1544\n\t"
    "ds_read_b64_tr_b16 a[222:223], v225 offset:5640\n\t"
    "s_waitcnt lgkmcnt(0)\n\t"
    "v_mfma_f32_16x16x32_bf16 v[160:163], a[48:51], a[64:67], 0\n\t"
    "v_mfma_f32_16x16x32_bf16 v[160:163], a[52:55], a[68:71], v[160:163]\n\t"
    "v_mfma_f32_16x16x32_bf16 v[160:163], a[56:59], a[72:75], v[160:163]\n\t"
    "v_mfma_f32_16x16x32_bf16 v[160:163], a[60:63], a[76:79], v[160:163]\n\t"
    "v_mfma_f32_16x16x32_bf16 v[164:167], a[48:51], a[80:83], 0\n\t"
    "v_mfma_f32_16x16x32_bf16 v[164:167], a[52:55], a[84:87], v[164:167]\n\t"
    "v_mfma_f32_16x16x32_bf16 v[164:167], a[56:59], a[88:91], v[164:167]\n\t"
    "v_mfma_f32_16x16x32_bf16 v[164:167], a[60:63], a[92:95], v[164:167]\n\t"
    "v_mfma_f32_16x16x32_bf16 v[128:131], a[160:163], a[64:67], 0\n\t"
    "s_nop 2\n\t"
    "v_max3_f32 v160, v160, v161, v162\n\t"
    "v_max_f32 v160, v160, v163\n\t"
    "v_mfma_f32_16x16x32_bf16 v[128:131], a[164:167], a[68:71], v[128:131]\n\t"
    "s_add_u32 s65, s53, 20480\n\t"
    "v_mul_f32 v168, s61, v160\n\t"
    "v_mfma_f32_16x16x32_bf16 v[128:131], a[168:171], a[72:75], v[128:131]\n\t"
    "s_add_u32 m0, s69, 20480\n\t"
    "v_mfma_f32_16x16x32_bf16 v[128:131], a[172:175], a[76:79], v[128:131]\n\t"
    "buffer_load_dwordx4 v226, s[44:47], s65 offen offset:0 nt lds\n\t"
    "v_mfma_f32_16x16x32_bf16 v[132:135], a[176:179], a[64:67], 0\n\t"
    "v_max3_f32 v164, v164, v165, v166\n\t"
    "v_max_f32 v164, v164, v167\n\t"
    "v_mfma_f32_16x16x32_bf16 v[132:135], a[180:183], a[68:71], v[132:135]\n\t"
    "v_mul_f32 v169, s61, v164\n\t"
    "v_mfma_f32_16x16x32_bf16 v[132:135], a[184:187], a[72:75], v[132:135]\n\t"
    "v_mfma_f32_16x16x32_bf16 v[132:135], a[188:191], a[76:79], v[132:135]\n\t"
    "buffer_load_dwordx4 v226, s[44:47], s65 offen offset:2048 nt lds\n\t"
    "v_mfma_f32_16x16x32_bf16 v[136:139], a[160:163], a[80:83], 0\n\t"
    "v_fma_f32 v128, v128, s60, v168\n\t"
    "v_fma_f32 v129, v129, s60, v168\n\t"
    "v_mfma_f32_16x16x32_bf16 v[136:139], a[164:167], a[84:87], v[136:139]\n\t"
    "s_waitcnt vmcnt(10)\n\t"
    "v_exp_f32 v128, v128\n\t"
    "v_fma_f32 v130, v130, s60, v168\n\t"
    "v_fma_f32 v131, v131, s60, v168\n\t"
    "v_mfma_f32_16x16x32_bf16 v[136:139], a[168:171], a[88:91], v[136:139]\n\t"
    "s_barrier\n\t"
    "v_exp_f32 v129, v129\n\t"
    "v_mfma_f32_16x16x32_bf16 v[136:139], a[172:175], a[92:95], v[136:139]\n\t"
    "ds_read_b128 a[160:163], v224 offset:8192\n\t"
    "ds_read_b128 a[164:167], v224 offset:8704\n\t"
    "ds_read_b128 a[168:171], v224 offset:9216\n\t"
    "ds_read_b128 a[172:175], v224 offset:9728\n\t"
    "s_add_u32 s65, s53, 20608\n\t"
    "v_mfma_f32_16x16x32_bf16 v[140:143], a[176:179], a[80:83], 0\n\t"
    "ds_read_b128 a[176:179], v224 offset:12288\n\t"
    "s_add_u32 m0, s69, 21504\n\t"
    "v_fma_f32 v132, v132, s60, v168\n\t"
    "v_fma_f32 v133, v133, s60, v168\n\t"
    "v_fma_f32 v134, v134, s60, v168\n\t"
    "v_fma_f32 v135, v135, s60, v168\n\t"
    "v_mfma_f32_16x16x32_bf16 v[140:143], a[180:183], a[84:87], v[140:143]\n\t"
    "buffer_load_dwordx4 v226, s[44:47], s65 offen offset:0 nt lds\n\t"
    "ds_read_b128 a[180:183], v224 offset:12800\n\t"
    "v_exp_f32 v130, v130\n\t"
    "v_exp_f32 v131, v131\n\t"
    "v_exp_f32 v132, v132\n\t"
    "v_exp_f32 v133, v133\n\t"
    "v_mfma_f32_16x16x32_bf16 v[140:143], a[184:187], a[88:91], v[140:143]\n\t"
    "ds_read_b128 a[184:187], v224 offset:13312\n\t"
    "v_cvt_pk_bf16_f32 v128, v128, v129\n\t"
    "v_exp_f32 v134, v134\n\t"
    "v_mfma_f32_16x16x32_bf16 v[140:143], a[188:191], a[92:95], v[140:143]\n\t"
    "ds_read_b128 a[188:191], v224 offset:13824\n\t"
    "s_waitcnt lgkmcnt(6)\n\t"
    "v_mfma_f32_16x16x32_bf16 v[144:147], a[160:163], a[64:67], 0\n\t"
    "ds_read_b64_tr_b16 a[46:47], v225 offset:12808\n\t"
    "s_waitcnt lgkmcnt(5)\n\t"
    "v_mfma_f32_16x16x32_bf16 v[144:147], a[164:167], a[68:71], v[144:147]\n\t"
    "buffer_load_dwordx4 v226, s[44:47], s65 offen offset:2048 nt lds\n\t"
    "v_mfma_f32_16x16x32_bf16 v[144:147], a[168:171], a[72:75], v[144:147]\n\t"
    "s_add_u32 s65, s53, 8192\n\t"
    "v_exp_f32 v135, v135\n\t"
    "v_mfma_f32_16x16x32_bf16 v[144:147], a[172:175], a[76:79], v[144:147]\n\t"
    "s_add_u32 m0, s69, 8192\n\t"
    "s_waitcnt lgkmcnt(3)\n\t"
    "v_cvt_pk_bf16_f32 v129, v130, v131\n\t"
    "v_mfma_f32_16x16x32_bf16 v[148:151], a[176:179], a[64:67], 0\n\t"
    "buffer_load_dwordx4 v226, s[44:47], s65 offen offset:0 nt lds\n\t"
    "v_mfma_f32_16x16x32_bf16 v[148:151], a[180:183], a[68:71], v[148:151]\n\t"
    "s_waitcnt lgkmcnt(1)\n\t"
    "v_cvt_pk_bf16_f32 v130, v132, v133\n\t"
    "v_cvt_pk_bf16_f32 v131, v134, v135\n\t"
    "v_fma_f32 v136, v136, s60, v169\n\t"
    "v_fma_f32 v137, v137, s60, v169\n\t"
    "v_fma_f32 v138, v138, s60, v169\n\t"
    "v_fma_f32 v139, v139, s60, v169\n\t"
    "v_fma_f32 v140, v140, s60, v169\n\t"
    "v_fma_f32 v141, v141, s60, v169\n\t"
    "v_fma_f32 v142, v142, s60, v169\n\t"
    "v_mfma_f32_16x16x32_bf16 v[148:151], a[184:187], a[72:75], v[148:151]\n\t"
    "ds_read_b64_tr_b16 a[32:33], v225 offset:8192\n\t"
    "v_fma_f32 v143, v143, s60, v169\n\t"
    "v_exp_f32 v136, v136\n\t"
    "v_mfma_f32_16x16x32_bf16 v[148:151], a[188:191], a[76:79], v[148:151]\n\t"
    "v_exp_f32 v137, v137\n\t"
    "v_mfma_f32_16x16x32_bf16 a[224:227], a[248:251], v[128:131], 0\n\t"
    "buffer_load_dwordx4 v226, s[44:47], s65 offen offset:2048 nt lds\n\t"
    "v_mfma_f32_16x16x32_bf16 v[0:3], a[192:195], v[128:131], 0\n\t"
    "v_exp_f32 v138, v138\n\t"
    "v_exp_f32 v139, v139\n\t"
    "v_mfma_f32_16x16x32_bf16 v[4:7], a[196:199], v[128:131], 0\n\t"
    "ds_read_b64_tr_b16 a[34:35], v225 offset:12288\n\t"
    "v_cvt_pk_bf16_f32 v136, v136, v137\n\t"
    "v_exp_f32 v140, v140\n\t"
    "v_mfma_f32_16x16x32_bf16 v[8:11], a[200:203], v[128:131], 0\n\t"
    "v_exp_f32 v141, v141\n\t"
    "v_mfma_f32_16x16x32_bf16 v[12:15], a[204:207], v[128:131], 0\n\t"
    "ds_read_b64_tr_b16 a[36:37], v225 offset:8200\n\t"
    "s_add_u32 s65, s53, 8320\n\t"
    "v_exp_f32 v142, v142\n\t"
    "v_exp_f32 v143, v143\n\t"
    "v_mfma_f32_16x16x32_bf16 v[16:19], a[208:211], v[128:131], 0\n\t"
    "s_add_u32 m0, s69, 9216\n\t"
    "v_cvt_pk_bf16_f32 v137, v138, v139\n\t"
    "v_cvt_pk_bf16_f32 v138, v140, v141\n\t"
    "v_cvt_pk_bf16_f32 v139, v142, v143\n\t"
    "v_fma_f32 v144, v144, s60, v168\n\t"
    "v_fma_f32 v145, v145, s60, v168\n\t"
    "v_fma_f32 v146, v146, s60, v168\n\t"
    "v_fma_f32 v147, v147, s60, v168\n\t"
    "v_fma_f32 v148, v148, s60, v168\n\t"
    "v_fma_f32 v149, v149, s60, v168\n\t"
    "v_mfma_f32_16x16x32_bf16 v[20:23], a[212:215], v[128:131], 0\n\t"
    "buffer_load_dwordx4 v226, s[44:47], s65 offen offset:0 nt lds\n\t"
    "v_fma_f32 v150, v150, s60, v168\n\t"
    "v_fma_f32 v151, v151, s60, v168\n\t"
    "v_mfma_f32_16x16x32_bf16 v[24:27], a[216:219], v[128:131], 0\n\t"
    "v_exp_f32 v144, v144\n\t"
    "v_mfma_f32_16x16x32_bf16 v[28:31], a[220:223], v[128:131], 0\n\t"
    "ds_read_b64_tr_b16 a[38:39], v225 offset:12296\n\t"
    "v_exp_f32 v145, v145\n\t"
    "v_mfma_f32_16x16x32_bf16 v[152:155], a[160:163], a[80:83], 0\n\t"
    "v_exp_f32 v146, v146\n\t"
    "v_mfma_f32_16x16x32_bf16 v[152:155], a[164:167], a[84:87], v[152:155]\n\t"
    "ds_read_b64_tr_b16 a[40:41], v225 offset:8704\n\t"
    "s_waitcnt vmcnt(11)\n\t"
    "v_mfma_f32_16x16x32_bf16 v[152:155], a[168:171], a[88:91], v[152:155]\n\t"
    "s_barrier\n\t"
    "v_exp_f32 v147, v147\n\t"
    "v_mfma_f32_16x16x32_bf16 v[152:155], a[172:175], a[92:95], v[152:155]\n\t"
    "buffer_load_dwordx4 v226, s[44:47], s65 offen offset:2048 nt lds\n\t"
    "v_mfma_f32_16x16x32_bf16 v[156:159], a[176:179], a[80:83], 0\n\t"
    "ds_read_b128 a[160:163], v224 offset:16384\n\t"
    "v_cvt_pk_bf16_f32 v144, v144, v145\n\t"
    "v_mfma_f32_16x16x32_bf16 v[156:159], a[180:183], a[84:87], v[156:159]\n\t"
    "ds_read_b128 a[164:167], v224 offset:16896\n\t"
    "v_cvt_pk_bf16_f32 v145, v146, v147\n\t"
    "v_mfma_f32_16x16x32_bf16 v[156:159], a[184:187], a[88:91], v[156:159]\n\t"
    "ds_read_b128 a[168:171], v224 offset:17408\n\t"
    "v_mfma_f32_16x16x32_bf16 v[156:159], a[188:191], a[92:95], v[156:159]\n\t"
    "ds_read_b128 a[172:175], v224 offset:17920\n\t"
    "v_exp_f32 v148, v148\n\t"
    "v_mfma_f32_16x16x32_bf16 a[228:231], a[248:251], v[136:139], 0\n\t"
    "ds_read_b128 a[176:179], v224 offset:20480\n\t"
    "v_fma_f32 v152, v152, s60, v169\n\t"
    "v_mfma_f32_16x16x32_bf16 v[32:35], a[192:195], v[136:139], 0\n\t"
    "ds_read_b128 a[180:183], v224 offset:20992\n\t"
    "v_fma_f32 v153, v153, s60, v169\n\t"
    "v_exp_f32 v149, v149\n\t"
    "v_exp_f32 v150, v150\n\t"
    "v_mfma_f32_16x16x32_bf16 v[36:39], a[196:199], v[136:139], 0\n\t"
    "ds_read_b128 a[184:187], v224 offset:21504\n\t"
    "v_cvt_pk_bf16_f32 v146, v148, v149\n\t"
    "v_exp_f32 v151, v151\n\t"
    "v_mfma_f32_16x16x32_bf16 v[40:43], a[200:203], v[136:139], 0\n\t"
    "ds_read_b128 a[188:191], v224 offset:22016\n\t"
    "v_cvt_pk_bf16_f32 v147, v150, v151\n\t"
    "v_mfma_f32_16x16x32_bf16 v[44:47], a[204:207], v[136:139], 0\n\t"
    "ds_read_b64_tr_b16 a[198:199], v225 offset:20488\n\t"
    "v_fma_f32 v154, v154, s60, v169\n\t"
    "v_fma_f32 v155, v155, s60, v169\n\t"
    "v_fma_f32 v156, v156, s60, v169\n\t"
    "v_fma_f32 v157, v157, s60, v169\n\t"
    "v_mfma_f32_16x16x32_bf16 v[48:51], a[208:211], v[136:139], 0\n\t"
    "ds_read_b64_tr_b16 a[42:43], v225 offset:12800\n\t"
    "v_fma_f32 v158, v158, s60, v169\n\t"
    "v_fma_f32 v159, v159, s60, v169\n\t"
    "v_exp_f32 v152, v152\n\t"
    "v_exp_f32 v153, v153\n\t"
    "v_exp_f32 v154, v154\n\t"
    "v_exp_f32 v155, v155\n\t"
    "v_mfma_f32_16x16x32_bf16 v[52:55], a[212:215], v[136:139], 0\n\t"
    "ds_read_b64_tr_b16 a[210:211], v225 offset:13312\n\t"
    "v_cvt_pk_bf16_f32 v152, v152, v153\n\t"
    "v_exp_f32 v156, v156\n\t"
    "v_mfma_f32_16x16x32_bf16 v[56:59], a[216:219], v[136:139], 0\n\t"
    "ds_read_b64_tr_b16 a[44:45], v225 offset:8712\n\t"
    "v_cvt_pk_bf16_f32 v153, v154, v155\n\t"
    "v_mfma_f32_16x16x32_bf16 v[60:63], a[220:223], v[136:139], 0\n\t"
    "ds_read_b64_tr_b16 a[212:213], v225 offset:9224\n\t"
    "s_waitcnt lgkmcnt(11)\n\t"
    "v_mfma_f32_16x16x32_bf16 v[128:131], a[160:163], a[64:67], 0\n\t"
    "ds_read_b64_tr_b16 a[208:209], v225 offset:9216\n\t"
    "s_waitcnt lgkmcnt(10)\n\t"
    "v_mfma_f32_16x16x32_bf16 v[128:131], a[164:167], a[68:71], v[128:131]\n\t"
    "ds_read_b64_tr_b16 a[214:215], v225 offset:13320\n\t"
    "s_add_u32 s65, s53, 12288\n\t"
    "v_mfma_f32_16x16x32_bf16 v[128:131], a[168:171], a[72:75], v[128:131]\n\t"
    "ds_read_b64_tr_b16 a[216:217], v225 offset:9728\n\t"
    "s_add_u32 m0, s69, 12288\n\t"
    "v_mfma_f32_16x16x32_bf16 v[128:131], a[172:175], a[76:79], v[128:131]\n\t"
    "buffer_load_dwordx4 v226, s[44:47], s65 offen offset:0 nt lds\n\t"
    "s_waitcnt lgkmcnt(10)\n\t"
    "v_mfma_f32_16x16x32_bf16 v[132:135], a[176:179], a[64:67], 0\n\t"
    "ds_read_b64_tr_b16 a[218:219], v225 offset:13824\n\t"
    "v_mfma_f32_16x16x32_bf16 v[132:135], a[180:183], a[68:71], v[132:135]\n\t"
    "ds_read_b64_tr_b16 a[220:221], v225 offset:9736\n\t"
    "s_waitcnt lgkmcnt(10)\n\t"
    "v_mfma_f32_16x16x32_bf16 v[132:135], a[184:187], a[72:75], v[132:135]\n\t"
    "ds_read_b64_tr_b16 a[222:223], v225 offset:13832\n\t"
    "v_exp_f32 v157, v157\n\t"
    "v_mfma_f32_16x16x32_bf16 v[132:135], a[188:191], a[76:79], v[132:135]\n\t"
    "buffer_load_dwordx4 v226, s[44:47], s65 offen offset:2048 nt lds\n\t"
    "v_mfma_f32_16x16x32_bf16 a[224:227], a[248:251], v[144:147], a[224:227]\n\t"
    "ds_read_b64_tr_b16 a[202:203], v225 offset:20992\n\t"
    "s_waitcnt lgkmcnt(15)\n\t"
    "v_mfma_f32_16x16x32_bf16 v[0:3], a[32:35], v[144:147], v[0:3]\n\t"
    "ds_read_b64_tr_b16 a[192:193], v225 offset:16384\n\t"
    "s_waitcnt lgkmcnt(6)\n\t"
    "v_mfma_f32_16x16x32_bf16 v[4:7], a[36:39], v[144:147], v[4:7]\n\t"
    "ds_read_b64_tr_b16 a[204:205], v225 offset:16904\n\t"
    "s_waitcnt lgkmcnt(10)\n\t"
    "v_mfma_f32_16x16x32_bf16 v[8:11], a[40:43], v[144:147], v[8:11]\n\t"
    "ds_read_b64_tr_b16 a[194:195], v225 offset:20480\n\t"
    "s_add_u32 s65, s53, 12416\n\t"
    "v_mfma_f32_16x16x32_bf16 v[12:15], a[44:47], v[144:147], v[12:15]\n\t"
    "ds_read_b64_tr_b16 a[206:207], v225 offset:21000\n\t"
    "s_add_u32 m0, s69, 13312\n\t"
    "v_mfma_f32_16x16x32_bf16 v[16:19], a[208:211], v[144:147], v[16:19]\n\t"
    "buffer_load_dwordx4 v226, s[44:47], s65 offen offset:0 nt lds\n\t"
    "v_mfma_f32_16x16x32_bf16 v[20:23], a[212:215], v[144:147], v[20:23]\n\t"
    "s_waitcnt vmcnt(7)\n\t"
    "s_waitcnt lgkmcnt(5)\n\t"
    "v_cvt_pk_bf16_f32 v154, v156, v157\n\t"
    "v_exp_f32 v158, v158\n\t"
    "v_mfma_f32_16x16x32_bf16 v[24:27], a[216:219], v[144:147], v[24:27]\n\t"
    "ds_read_b128 a[128:131], v229 offset:16384\n\t"
    "v_fma_f32 v128, v128, s60, v168\n\t"
    "v_mfma_f32_16x16x32_bf16 v[28:31], a[220:223], v[144:147], v[28:31]\n\t"
    "ds_read_b128 a[132:135], v229 offset:16896\n\t"
    "v_fma_f32 v129, v129, s60, v168\n\t"
    "v_mfma_f32_16x16x32_bf16 v[136:139], a[160:163], a[80:83], 0\n\t"
    "buffer_load_dwordx4 v226, s[44:47], s65 offen offset:2048 nt lds\n\t"
    "v_mfma_f32_16x16x32_bf16 v[136:139], a[164:167], a[84:87], v[136:139]\n\t"
    "ds_read_b128 a[136:139], v229 offset:17408\n\t"
    "v_fma_f32 v130, v130, s60, v168\n\t"
    "v_mfma_f32_16x16x32_bf16 v[136:139], a[168:171], a[88:91], v[136:139]\n\t"
    "ds_read_b128 a[140:143], v229 offset:17920\n\t"
    "v_fma_f32 v131, v131, s60, v168\n\t"
    "v_exp_f32 v159, v159\n\t"
    "v_mfma_f32_16x16x32_bf16 v[136:139], a[172:175], a[92:95], v[136:139]\n\t"
    "ds_read_b128 a[144:147], v229 offset:20480\n\t"
    "v_cvt_pk_bf16_f32 v155, v158, v159\n\t"
    "v_mfma_f32_16x16x32_bf16 v[140:143], a[176:179], a[80:83], 0\n\t"
    "ds_read_b128 a[176:179], v224 offset:4096\n\t"
    "s_mov_b32 s53, %11\n\t"
    "v_mfma_f32_16x16x32_bf16 v[140:143], a[180:183], a[84:87], v[140:143]\n\t"
    "ds_read_b128 a[148:151], v229 offset:20992\n\t"
    "v_fma_f32 v132, v132, s60, v168\n\t"
    "v_mfma_f32_16x16x32_bf16 v[140:143], a[184:187], a[88:91], v[140:143]\n\t"
    "ds_read_b128 a[152:155], v229 offset:21504\n\t"
    "v_fma_f32 v133, v133, s60, v168\n\t"
    "v_mfma_f32_16x16x32_bf16 v[140:143], a[188:191], a[92:95], v[140:143]\n\t"
    "ds_read_b128 a[156:159], v229 offset:22016\n\t"
    "v_fma_f32 v134, v134, s60, v168\n\t"
    "v_mfma_f32_16x16x32_bf16 a[228:231], a[248:251], v[152:155], a[228:231]\n\t"
    "ds_read_b128 a[184:187], v224 offset:5120\n\t"
    "v_fma_f32 v135, v135, s60, v168\n\t"
    "v_mfma_f32_16x16x32_bf16 v[32:35], a[32:35], v[152:155], v[32:35]\n\t"
    "ds_read_b128 a[160:163], v224 offset:0\n\t"
    "v_fma_f32 v136, v136, s60, v169\n\t"
    "v_mfma_f32_16x16x32_bf16 v[36:39], a[36:39], v[152:155], v[36:39]\n\t"
    "v_exp_f32 v128, v128\n\t"
    "v_mfma_f32_16x16x32_bf16 v[40:43], a[40:43], v[152:155], v[40:43]\n\t"
    "ds_read_b128 a[164:167], v224 offset:512\n\t"
    "v_fma_f32 v137, v137, s60, v169\n\t"
    "v_mfma_f32_16x16x32_bf16 v[44:47], a[44:47], v[152:155], v[44:47]\n\t"
    "v_exp_f32 v129, v129\n\t"
    "v_mfma_f32_16x16x32_bf16 v[48:51], a[208:211], v[152:155], v[48:51]\n\t"
    "ds_read_b128 a[168:171], v224 offset:1024\n\t"
    "v_cvt_pk_bf16_f32 v128, v128, v129\n\t"
    "v_mfma_f32_16x16x32_bf16 v[52:55], a[212:215], v[152:155], v[52:55]\n\t"
    "ds_read_b64_tr_b16 a[210:211], v225 offset:21504\n\t"
    "v_fma_f32 v138, v138, s60, v169\n\t"
    "v_exp_f32 v130, v130\n\t"
    "v_mfma_f32_16x16x32_bf16 v[56:59], a[216:219], v[152:155], v[56:59]\n\t"
    "ds_read_b128 a[172:175], v224 offset:1536\n\t"
    "v_fma_f32 v139, v139, s60, v169\n\t"
    "v_mfma_f32_16x16x32_bf16 v[60:63], a[220:223], v[152:155], v[60:63]\n\t"
    "ds_read_b64_tr_b16 a[212:213], v225 offset:17416\n\t"
    "s_waitcnt lgkmcnt(7)\n\t"
    "v_mfma_f32_16x16x32_bf16 v[160:163], a[48:51], a[128:131], 0\n\t"
    "ds_read_b128 a[180:183], v224 offset:4608\n\t"
    "v_fma_f32 v140, v140, s60, v169\n\t"
    "v_exp_f32 v131, v131\n\t"
    "v_mfma_f32_16x16x32_bf16 v[160:163], a[52:55], a[132:135], v[160:163]\n\t"
    "ds_read_b64_tr_b16 a[214:215], v225 offset:21512\n\t"
    "v_cvt_pk_bf16_f32 v129, v130, v131\n\t"
    "v_mfma_f32_16x16x32_bf16 v[160:163], a[56:59], a[136:139], v[160:163]\n\t"
    "ds_read_b128 a[188:191], v224 offset:5632\n\t"
    "v_fma_f32 v141, v141, s60, v169\n\t"
    "v_mfma_f32_16x16x32_bf16 v[160:163], a[60:63], a[140:143], v[160:163]\n\t"
    "ds_read_b64_tr_b16 a[216:217], v225 offset:17920\n\t"
    "v_fma_f32 v142, v142, s60, v169\n\t"
    "v_exp_f32 v132, v132\n\t"
    "v_mfma_f32_16x16x32_bf16 v[164:167], a[48:51], a[144:147], 0\n\t"
    "ds_read_b64_tr_b16 a[196:197], v225 offset:16392\n\t"
    "v_fma_f32 v143, v143, s60, v169\n\t"
    "v_mfma_f32_16x16x32_bf16 v[164:167], a[52:55], a[148:151], v[164:167]\n\t"
    "ds_read_b64_tr_b16 a[218:219], v225 offset:22016\n\t"
    "v_exp_f32 v133, v133\n\t"
    "v_mfma_f32_16x16x32_bf16 v[164:167], a[56:59], a[152:155], v[164:167]\n\t"
    "ds_read_b64_tr_b16 a[200:201], v225 offset:16896\n\t"
    "v_cvt_pk_bf16_f32 v130, v132, v133\n\t"
    "v_mfma_f32_16x16x32_bf16 v[164:167], a[60:63], a[156:159], v[164:167]\n\t"
    "ds_read_b64_tr_b16 a[220:221], v225 offset:17928\n\t"
    "s_waitcnt lgkmcnt(12)\n\t"
    "v_mfma_f32_16x16x32_bf16 v[144:147], a[160:163], a[128:131], 0\n\t"
    "ds_read_b64_tr_b16 a[208:209], v225 offset:17408\n\t"
    "s_waitcnt lgkmcnt(10)\n\t"
    "v_mfma_f32_16x16x32_bf16 v[144:147], a[164:167], a[132:135], v[144:147]\n\t"
    "ds_read_b64_tr_b16 a[222:223], v225 offset:22024\n\t"
    "s_xor_b32 s64, s52, 24576\n\t"
    "v_mfma_f32_16x16x32_bf16 v[144:147], a[168:171], a[136:139], v[144:147]\n\t"
    "s_add_u32 m0, s63, 24576\n\t"
    "v_exp_f32 v134, v134\n\t"
    "v_mfma_f32_16x16x32_bf16 v[144:147], a[172:175], a[140:143], v[144:147]\n\t"
    "buffer_load_dwordx4 v226, s[36:39], s64 offen offset:0 lds\n\t"
    "s_waitcnt lgkmcnt(9)\n\t"
    "v_mfma_f32_16x16x32_bf16 v[148:151], a[176:179], a[128:131], 0\n\t"
    "v_exp_f32 v135, v135\n\t"
    "v_mfma_f32_16x16x32_bf16 v[148:151], a[180:183], a[132:135], v[148:151]\n\t"
    "s_waitcnt lgkmcnt(7)\n\t"
    "v_exp_f32 v136, v136\n\t"
    "v_cvt_pk_bf16_f32 v131, v134, v135\n\t"
    "v_max3_f32 v160, v160, v161, v162\n\t"
    "v_mfma_f32_16x16x32_bf16 v[148:151], a[184:187], a[136:139], v[148:151]\n\t"
    "v_exp_f32 v137, v137\n\t"
    "v_max_f32 v160, v160, v163\n\t"
    "v_mul_f32 v172, s61, v160\n\t"
    "v_mfma_f32_16x16x32_bf16 v[148:151], a[188:191], a[140:143], v[148:151]\n\t"
    "buffer_load_dwordx4 v226, s[36:39], s64 offen offset:2048 lds\n\t"
    "v_mfma_f32_16x16x32_bf16 a[224:227], a[248:251], v[128:131], a[224:227]\n\t"
    "s_waitcnt lgkmcnt(5)\n\t"
    "v_exp_f32 v138, v138\n\t"
    "v_mfma_f32_16x16x32_bf16 v[0:3], a[192:195], v[128:131], v[0:3]\n\t"
    "s_waitcnt lgkmcnt(1)\n\t"
    "v_exp_f32 v139, v139\n\t"
    "v_mfma_f32_16x16x32_bf16 v[4:7], a[196:199], v[128:131], v[4:7]\n\t"
    "s_waitcnt lgkmcnt(3)\n\t"
    "v_exp_f32 v140, v140\n\t"
    "v_exp_f32 v141, v141\n\t"
    "v_mfma_f32_16x16x32_bf16 v[8:11], a[200:203], v[128:131], v[8:11]\n\t"
    "s_add_u32 m0, s63, 57344\n\t"
    "v_exp_f32 v142, v142\n\t"
    "v_mfma_f32_16x16x32_bf16 v[12:15], a[204:207], v[128:131], v[12:15]\n\t"
    "buffer_load_dwordx4 v226, s[40:43], s64 offen offset:0 lds\n\t"
    "v_exp_f32 v143, v143\n\t"
    "v_cvt_pk_bf16_f32 v136, v136, v137\n\t"
    "v_cvt_pk_bf16_f32 v137, v138, v139\n\t"
    "v_mfma_f32_16x16x32_bf16 v[16:19], a[208:211], v[128:131], v[16:19]\n\t"
    "v_cvt_pk_bf16_f32 v138, v140, v141\n\t"
    "v_cvt_pk_bf16_f32 v139, v142, v143\n\t"
    "v_max3_f32 v164, v164, v165, v166\n\t"
    "v_max_f32 v164, v164, v167\n\t"
    "v_mul_f32 v173, s61, v164\n\t"
    "v_fma_f32 v144, v144, s60, v172\n\t"
    "v_fma_f32 v145, v145, s60, v172\n\t"
    "v_fma_f32 v146, v146, s60, v172\n\t"
    "v_fma_f32 v147, v147, s60, v172\n\t"
    "v_fma_f32 v148, v148, s60, v172\n\t"
    "v_mfma_f32_16x16x32_bf16 v[20:23], a[212:215], v[128:131], v[20:23]\n\t"
    "s_waitcnt lgkmcnt(0)\n\t"
    "v_exp_f32 v144, v144\n\t"
    "v_fma_f32 v149, v149, s60, v172\n\t"
    "v_fma_f32 v150, v150, s60, v172\n\t"
    "v_mfma_f32_16x16x32_bf16 v[24:27], a[216:219], v[128:131], v[24:27]\n\t"
    "v_exp_f32 v145, v145\n\t"
    "v_mfma_f32_16x16x32_bf16 v[28:31], a[220:223], v[128:131], v[28:31]\n\t"
    "buffer_load_dwordx4 v226, s[40:43], s64 offen offset:2048 lds\n\t"
    "v_mfma_f32_16x16x32_bf16 v[152:155], a[160:163], a[144:147], 0\n\t"
    "v_exp_f32 v146, v146\n\t"
    "v_mfma_f32_16x16x32_bf16 v[152:155], a[164:167], a[148:151], v[152:155]\n\t"
    "ds_read_b128 a[160:163], v224 offset:8192\n\t"
    "v_fma_f32 v151, v151, s60, v172\n\t"
    "v_mfma_f32_16x16x32_bf16 v[152:155], a[168:171], a[152:155], v[152:155]\n\t"
    "v_exp_f32 v147, v147\n\t"
    "v_mfma_f32_16x16x32_bf16 v[152:155], a[172:175], a[156:159], v[152:155]\n\t"
    "ds_read_b128 a[164:167], v224 offset:8704\n\t"
    "v_cvt_pk_bf16_f32 v144, v144, v145\n\t"
    "v_mfma_f32_16x16x32_bf16 v[156:159], a[176:179], a[144:147], 0\n\t"
    "ds_read_b128 a[176:179], v224 offset:12288\n\t"
    "v_cvt_pk_bf16_f32 v145, v146, v147\n\t"
    "v_mfma_f32_16x16x32_bf16 v[156:159], a[180:183], a[148:151], v[156:159]\n\t"
    "ds_read_b128 a[168:171], v224 offset:9216\n\t"
    "v_exp_f32 v148, v148\n\t"
    "v_mfma_f32_16x16x32_bf16 v[156:159], a[184:187], a[152:155], v[156:159]\n\t"
    "ds_read_b128 a[184:187], v224 offset:13312\n\t"
    "v_mfma_f32_16x16x32_bf16 v[156:159], a[188:191], a[156:159], v[156:159]\n\t"
    "ds_read_b128 a[172:175], v224 offset:9728\n\t"
    "v_exp_f32 v149, v149\n\t"
    "v_mfma_f32_16x16x32_bf16 a[228:231], a[248:251], v[136:139], a[228:231]\n\t"
    "v_exp_f32 v150, v150\n\t"
    "v_mfma_f32_16x16x32_bf16 v[32:35], a[192:195], v[136:139], v[32:35]\n\t"
    "ds_read_b128 a[180:183], v224 offset:12800\n\t"
    "v_cvt_pk_bf16_f32 v146, v148, v149\n\t"
    "v_mfma_f32_16x16x32_bf16 v[36:39], a[196:199], v[136:139], v[36:39]\n\t"
    "ds_read_b64_tr_b16 a[194:195], v225 offset:4096\n\t"
    "v_fma_f32 v152, v152, s60, v173\n\t"
    "v_mfma_f32_16x16x32_bf16 v[40:43], a[200:203], v[136:139], v[40:43]\n\t"
    "ds_read_b128 a[188:191], v224 offset:13824\n\t"
    "v_fma_f32 v153, v153, s60, v173\n\t"
    "v_exp_f32 v151, v151\n\t"
    "v_mfma_f32_16x16x32_bf16 v[44:47], a[204:207], v[136:139], v[44:47]\n\t"
    "ds_read_b64_tr_b16 a[196:197], v225 offset:8\n\t"
    "v_cvt_pk_bf16_f32 v147, v150, v151\n\t"
    "v_mfma_f32_16x16x32_bf16 v[48:51], a[208:211], v[136:139], v[48:51]\n\t"
    "ds_read_b64_tr_b16 a[192:193], v225 offset:0\n\t"
    "ds_read_b64_tr_b16 a[210:211], v225 offset:5120\n\t"
    "v_fma_f32 v154, v154, s60, v173\n\t"
    "v_fma_f32 v155, v155, s60, v173\n\t"
    "v_fma_f32 v156, v156, s60, v173\n\t"
    "v_fma_f32 v157, v157, s60, v173\n\t"
    "v_mfma_f32_16x16x32_bf16 v[52:55], a[212:215], v[136:139], v[52:55]\n\t"
    "ds_read_b64_tr_b16 a[198:199], v225 offset:4104\n\t"
    "ds_read_b64_tr_b16 a[214:215], v225 offset:5128\n\t"
    "v_fma_f32 v158, v158, s60, v173\n\t"
    "v_fma_f32 v159, v159, s60, v173\n\t"
    "v_mfma_f32_16x16x32_bf16 v[56:59], a[216:219], v[136:139], v[56:59]\n\t"
    "ds_read_b64_tr_b16 a[200:201], v225 offset:512\n\t"
    "ds_read_b64_tr_b16 a[218:219], v225 offset:5632\n\t"
    "v_mfma_f32_16x16x32_bf16 v[60:63], a[220:223], v[136:139], v[60:63]\n\t"
    "ds_read_b64_tr_b16 a[202:203], v225 offset:4608\n\t"
    "ds_read_b64_tr_b16 a[222:223], v225 offset:5640\n\t"
    "s_waitcnt lgkmcnt(15)\n\t"
    "v_mfma_f32_16x16x32_bf16 v[128:131], a[160:163], a[128:131], 0\n\t"
    "ds_read_b64_tr_b16 a[204:205], v225 offset:520\n\t"
    "s_waitcnt lgkmcnt(13)\n\t"
    "v_mfma_f32_16x16x32_bf16 v[128:131], a[164:167], a[132:135], v[128:131]\n\t"
    "ds_read_b64_tr_b16 a[206:207], v225 offset:4616\n\t"
    "s_add_u32 s65, s53, 0\n\t"
    "v_mfma_f32_16x16x32_bf16 v[128:131], a[168:171], a[136:139], v[128:131]\n\t"
    "ds_read_b64_tr_b16 a[208:209], v225 offset:1024\n\t"
    "s_add_u32 m0, s69, 0\n\t"
    "v_mfma_f32_16x16x32_bf16 v[128:131], a[172:175], a[140:143], v[128:131]\n\t"
    "buffer_load_dwordx4 v226, s[44:47], s65 offen offset:0 nt lds\n\t"
    "s_waitcnt lgkmcnt(14)\n\t"
    "v_mfma_f32_16x16x32_bf16 v[132:135], a[176:179], a[128:131], 0\n\t"
    "ds_read_b64_tr_b16 a[212:213], v225 offset:1032\n\t"
    "v_exp_f32 v152, v152\n\t"
    "v_mfma_f32_16x16x32_bf16 v[132:135], a[180:183], a[132:135], v[132:135]\n\t"
    "ds_read_b64_tr_b16 a[216:217], v225 offset:1536\n\t"
    "s_waitcnt lgkmcnt(14)\n\t"
    "v_mfma_f32_16x16x32_bf16 v[132:135], a[184:187], a[136:139], v[132:135]\n\t"
    "ds_read_b64_tr_b16 a[220:221], v225 offset:1544\n\t"
    "v_mfma_f32_16x16x32_bf16 v[132:135], a[188:191], a[140:143], v[132:135]\n\t"
    "buffer_load_dwordx4 v226, s[44:47], s65 offen offset:2048 nt lds\n\t"
    "v_mfma_f32_16x16x32_bf16 a[240:243], a[248:251], v[144:147], 0\n\t"
    "s_waitcnt lgkmcnt(11)\n\t"
    "v_exp_f32 v153, v153\n\t"
    "v_mfma_f32_16x16x32_bf16 v[208:211], a[192:195], v[144:147], 0\n\t"
    "s_waitcnt lgkmcnt(2)\n\t"
    "v_exp_f32 v154, v154\n\t"
    "v_mfma_f32_16x16x32_bf16 v[212:215], a[196:199], v[144:147], 0\n\t"
    "s_waitcnt lgkmcnt(4)\n\t"
    "v_exp_f32 v155, v155\n\t"
    "v_mfma_f32_16x16x32_bf16 v[216:219], a[200:203], v[144:147], 0\n\t"
    "s_add_u32 s65, s53, 128\n\t"
    "v_exp_f32 v156, v156\n\t"
    "v_mfma_f32_16x16x32_bf16 v[220:223], a[204:207], v[144:147], 0\n\t"
    "s_add_u32 m0, s69, 1024\n\t"
    "v_exp_f32 v157, v157\n\t"
    "v_mfma_f32_16x16x32_bf16 v[240:243], a[208:211], v[144:147], 0\n\t"
    "buffer_load_dwordx4 v226, s[44:47], s65 offen offset:0 nt lds\n\t"
    "v_mfma_f32_16x16x32_bf16 v[244:247], a[212:215], v[144:147], 0\n\t"
    "s_waitcnt vmcnt(7)\n\t"
    "s_waitcnt lgkmcnt(0)\n\t"
    "v_cvt_pk_bf16_f32 v152, v152, v153\n\t"
    "v_mfma_f32_16x16x32_bf16 v[248:251], a[216:219], v[144:147], 0\n\t"
    "ds_read_b128 a[96:99], v229 offset:8192\n\t"
    "v_cvt_pk_bf16_f32 v153, v154, v155\n\t"
    "v_mfma_f32_16x16x32_bf16 v[252:255], a[220:223], v[144:147], 0\n\t"
    "ds_read_b128 a[100:103], v229 offset:8704\n\t"
    "v_cvt_pk_bf16_f32 v154, v156, v157\n\t"
    "v_mfma_f32_16x16x32_bf16 v[136:139], a[160:163], a[144:147], 0\n\t"
    "buffer_load_dwordx4 v226, s[44:47], s65 offen offset:2048 nt lds\n\t"
    "v_mfma_f32_16x16x32_bf16 v[136:139], a[164:167], a[148:151], v[136:139]\n\t"
    "ds_read_b128 a[104:107], v229 offset:9216\n\t"
    "v_fma_f32 v128, v128, s60, v172\n\t"
    "v_exp_f32 v158, v158\n\t"
    "v_mfma_f32_16x16x32_bf16 v[136:139], a[168:171], a[152:155], v[136:139]\n\t"
    "ds_read_b128 a[108:111], v229 offset:9728\n\t"
    "v_fma_f32 v129, v129, s60, v172\n\t"
    "v_mfma_f32_16x16x32_bf16 v[136:139], a[172:175], a[156:159], v[136:139]\n\t"
    "ds_read_b128 a[112:115], v229 offset:12288\n\t"
    "v_fma_f32 v130, v130, s60, v172\n\t"
    "v_exp_f32 v159, v159\n\t"
    "v_mfma_f32_16x16x32_bf16 v[140:143], a[176:179], a[144:147], 0\n\t"
    "ds_read_b128 a[116:119], v229 offset:12800\n\t"
    "v_cvt_pk_bf16_f32 v155, v158, v159\n\t"
    "v_mfma_f32_16x16x32_bf16 v[140:143], a[180:183], a[148:151], v[140:143]\n\t"
    "ds_read_b128 a[120:123], v229 offset:13312\n\t"
    "v_fma_f32 v131, v131, s60, v172\n\t"
    "v_mfma_f32_16x16x32_bf16 v[140:143], a[184:187], a[152:155], v[140:143]\n\t"
    "ds_read_b128 a[124:127], v229 offset:13824\n\t"
    "v_fma_f32 v132, v132, s60, v172\n\t"
    "v_mfma_f32_16x16x32_bf16 v[140:143], a[188:191], a[156:159], v[140:143]\n\t"
    "ds_read_b128 a[180:183], v224 offset:4608\n\t"
    "v_fma_f32 v133, v133, s60, v172\n\t"
    "v_mfma_f32_16x16x32_bf16 a[244:247], a[248:251], v[152:155], 0\n\t"
    "ds_read_b128 a[160:163], v224 offset:0\n\t"
    "v_fma_f32 v134, v134, s60, v172\n\t"
    "v_mfma_f32_16x16x32_bf16 v[176:179], a[192:195], v[152:155], 0\n\t"
    "ds_read_b128 a[188:191], v224 offset:5632\n\t"
    "v_fma_f32 v135, v135, s60, v172\n\t"
    "v_mfma_f32_16x16x32_bf16 v[180:183], a[196:199], v[152:155], 0\n\t"
    "ds_read_b128 a[164:167], v224 offset:512\n\t"
    "v_fma_f32 v136, v136, s60, v173\n\t"
    "v_mfma_f32_16x16x32_bf16 v[184:187], a[200:203], v[152:155], 0\n\t"
    "v_exp_f32 v128, v128\n\t"
    "v_mfma_f32_16x16x32_bf16 v[188:191], a[204:207], v[152:155], 0\n\t"
    "ds_read_b128 a[168:171], v224 offset:1024\n\t"
    "v_fma_f32 v137, v137, s60, v173\n\t"
    "v_mfma_f32_16x16x32_bf16 v[192:195], a[208:211], v[152:155], 0\n\t"
    "v_exp_f32 v129, v129\n\t"
    "v_mfma_f32_16x16x32_bf16 v[196:199], a[212:215], v[152:155], 0\n\t"
    "ds_read_b128 a[172:175], v224 offset:1536\n\t"
    "v_cvt_pk_bf16_f32 v128, v128, v129\n\t"
    "v_mfma_f32_16x16x32_bf16 v[200:203], a[216:219], v[152:155], 0\n\t"
    "ds_read_b64_tr_b16 a[216:217], v225 offset:9728\n\t"
    "v_fma_f32 v138, v138, s60, v173\n\t"
    "v_mfma_f32_16x16x32_bf16 v[204:207], a[220:223], v[152:155], 0\n\t"
    "ds_read_b128 a[176:179], v224 offset:4096\n\t"
    "s_waitcnt lgkmcnt(8)\n\t"
    "v_mfma_f32_16x16x32_bf16 v[160:163], a[48:51], a[96:99], 0\n\t"
    "ds_read_b64_tr_b16 a[218:219], v225 offset:13824\n\t"
    "v_fma_f32 v139, v139, s60, v173\n\t"
    "v_mfma_f32_16x16x32_bf16 v[160:163], a[52:55], a[100:103], v[160:163]\n\t"
    "ds_read_b128 a[184:187], v224 offset:5120\n\t"
    "v_fma_f32 v140, v140, s60, v173\n\t"
    "v_mfma_f32_16x16x32_bf16 v[160:163], a[56:59], a[104:107], v[160:163]\n\t"
    "ds_read_b64_tr_b16 a[222:223], v225 offset:13832\n\t"
    "v_fma_f32 v141, v141, s60, v173\n\t"
    "v_exp_f32 v130, v130\n\t"
    "v_mfma_f32_16x16x32_bf16 v[160:163], a[60:63], a[108:111], v[160:163]\n\t"
    "ds_read_b64_tr_b16 a[208:209], v225 offset:9216\n\t"
    "v_fma_f32 v142, v142, s60, v173\n\t"
    "v_mfma_f32_16x16x32_bf16 v[164:167], a[48:51], a[112:115], 0\n\t"
    "v_exp_f32 v131, v131\n\t"
    "v_mfma_f32_16x16x32_bf16 v[164:167], a[52:55], a[116:119], v[164:167]\n\t"
    "ds_read_b64_tr_b16 a[210:211], v225 offset:13312\n\t"
    "v_cvt_pk_bf16_f32 v129, v130, v131\n\t"
    "v_mfma_f32_16x16x32_bf16 v[164:167], a[56:59], a[120:123], v[164:167]\n\t"
    "v_exp_f32 v132, v132\n\t"
    "v_mfma_f32_16x16x32_bf16 v[164:167], a[60:63], a[124:127], v[164:167]\n\t"
    "ds_read_b64_tr_b16 a[212:213], v225 offset:9224\n\t"
    "s_waitcnt lgkmcnt(10)\n\t"
    "v_mfma_f32_16x16x32_bf16 v[144:147], a[160:163], a[96:99], 0\n\t"
    "v_exp_f32 v133, v133\n\t"
    "v_mfma_f32_16x16x32_bf16 v[144:147], a[164:167], a[100:103], v[144:147]\n\t"
    "ds_read_b64_tr_b16 a[214:215], v225 offset:13320\n\t"
    "s_waitcnt lgkmcnt(9)\n\t"
    "v_mfma_f32_16x16x32_bf16 v[144:147], a[168:171], a[104:107], v[144:147]\n\t"
    "v_exp_f32 v134, v134\n\t"
    "v_mfma_f32_16x16x32_bf16 v[144:147], a[172:175], a[108:111], v[144:147]\n\t"
    "ds_read_b64_tr_b16 a[220:221], v225 offset:9736\n\t"
    "s_waitcnt lgkmcnt(8)\n\t"
    "v_mfma_f32_16x16x32_bf16 v[148:151], a[176:179], a[96:99], 0\n\t"
    "v_exp_f32 v135, v135\n\t"
    "v_mfma_f32_16x16x32_bf16 v[148:151], a[180:183], a[100:103], v[148:151]\n\t"
    "s_waitcnt lgkmcnt(6)\n\t"
    "v_exp_f32 v136, v136\n\t"
    "v_cvt_pk_bf16_f32 v130, v132, v133\n\t"
    "v_cvt_pk_bf16_f32 v131, v134, v135\n\t"
    "v_mfma_f32_16x16x32_bf16 v[148:151], a[184:187], a[104:107], v[148:151]\n\t"
    "v_exp_f32 v137, v137\n\t"
    "v_max3_f32 v160, v160, v161, v162\n\t"
    "v_mfma_f32_16x16x32_bf16 v[148:151], a[188:191], a[108:111], v[148:151]\n\t"
    "v_exp_f32 v138, v138\n\t"
    "v_max_f32 v160, v160, v163\n\t"
    "v_mul_f32 v170, s61, v160\n\t"
    "v_mfma_f32_16x16x32_bf16 a[240:243], a[248:251], v[128:131], a[240:243]\n\t"
    "v_exp_f32 v139, v139\n\t"
    "v_mfma_f32_16x16x32_bf16 v[208:211], a[32:35], v[128:131], v[208:211]\n\t"
    "v_exp_f32 v140, v140\n\t"
    "v_mfma_f32_16x16x32_bf16 v[212:215], a[36:39], v[128:131], v[212:215]\n\t"
    "v_exp_f32 v141, v141\n\t"
    "v_mfma_f32_16x16x32_bf16 v[216:219], a[40:43], v[128:131], v[216:219]\n\t"
    "v_exp_f32 v142, v142\n\t"
    "v_mfma_f32_16x16x32_bf16 v[220:223], a[44:47], v[128:131], v[220:223]\n\t"
    "s_waitcnt lgkmcnt(1)\n\t"
    "v_fma_f32 v143, v143, s60, v173\n\t"
    "v_cvt_pk_bf16_f32 v136, v136, v137\n\t"
    "v_mfma_f32_16x16x32_bf16 v[240:243], a[208:211], v[128:131], v[240:243]\n\t"
    "v_exp_f32 v143, v143\n\t"
    "v_mfma_f32_16x16x32_bf16 v[244:247], a[212:215], v[128:131], v[244:247]\n\t"
    "s_waitcnt lgkmcnt(0)\n\t"
    "v_cvt_pk_bf16_f32 v137, v138, v139\n\t"
    "v_cvt_pk_bf16_f32 v138, v140, v141\n\t"
    "v_cvt_pk_bf16_f32 v139, v142, v143\n\t"
    "v_mfma_f32_16x16x32_bf16 v[248:251], a[216:219], v[128:131], v[248:251]\n\t"
    "v_max3_f32 v164, v164, v165, v166\n\t"
    "v_max_f32 v164, v164, v167\n\t"
    "v_mfma_f32_16x16x32_bf16 v[252:255], a[220:223], v[128:131], v[252:255]\n\t"
    "v_mul_f32 v171, s61, v164\n\t"
    "v_fma_f32 v144, v144, s60, v170\n\t"
    "v_mfma_f32_16x16x32_bf16 v[152:155], a[160:163], a[112:115], 0\n\t"
    "ds_read_b128 a[160:163], v224 offset:8192\n\t"
    "v_fma_f32 v145, v145, s60, v170\n\t"
    "v_mfma_f32_16x16x32_bf16 v[152:155], a[164:167], a[116:119], v[152:155]\n\t"
    "v_exp_f32 v144, v144\n\t"
    "v_mfma_f32_16x16x32_bf16 v[152:155], a[168:171], a[120:123], v[152:155]\n\t"
    "ds_read_b128 a[164:167], v224 offset:8704\n\t"
    "v_fma_f32 v146, v146, s60, v170\n\t"
    "v_mfma_f32_16x16x32_bf16 v[152:155], a[172:175], a[124:127], v[152:155]\n\t"
    "v_exp_f32 v145, v145\n\t"
    "v_mfma_f32_16x16x32_bf16 v[156:159], a[176:179], a[112:115], 0\n\t"
    "ds_read_b128 a[168:171], v224 offset:9216\n\t"
    "v_fma_f32 v147, v147, s60, v170\n\t"
    "v_mfma_f32_16x16x32_bf16 v[156:159], a[180:183], a[116:119], v[156:159]\n\t"
    "ds_read_b128 a[180:183], v224 offset:12800\n\t"
    "v_fma_f32 v148, v148, s60, v170\n\t"
    "v_mfma_f32_16x16x32_bf16 v[156:159], a[184:187], a[120:123], v[156:159]\n\t"
    "ds_read_b128 a[172:175], v224 offset:9728\n\t"
    "v_fma_f32 v149, v149, s60, v170\n\t"
    "v_mfma_f32_16x16x32_bf16 v[156:159], a[188:191], a[124:127], v[156:159]\n\t"
    "ds_read_b128 a[188:191], v224 offset:13824\n\t"
    "v_fma_f32 v150, v150, s60, v170\n\t"
    "v_mfma_f32_16x16x32_bf16 a[244:247], a[248:251], v[136:139], a[244:247]\n\t"
    "ds_read_b128 a[176:179], v224 offset:12288\n\t"
    "v_fma_f32 v151, v151, s60, v170\n\t"
    "v_mfma_f32_16x16x32_bf16 v[176:179], a[32:35], v[136:139], v[176:179]\n\t"
    "v_exp_f32 v146, v146\n\t"
    "v_mfma_f32_16x16x32_bf16 v[180:183], a[36:39], v[136:139], v[180:183]\n\t"
    "ds_read_b128 a[184:187], v224 offset:13312\n\t"
    "v_cvt_pk_bf16_f32 v144, v144, v145\n\t"
    "v_mfma_f32_16x16x32_bf16 v[184:187], a[40:43], v[136:139], v[184:187]\n\t"
    "v_exp_f32 v147, v147\n\t"
    "v_mfma_f32_16x16x32_bf16 v[188:191], a[44:47], v[136:139], v[188:191]\n\t"
    "v_exp_f32 v148, v148\n\t"
    "v_mfma_f32_16x16x32_bf16 v[192:195], a[208:211], v[136:139], v[192:195]\n\t"
    "ds_read_b64_tr_b16 a[208:209], v225 offset:1024\n\t"
    "v_cvt_pk_bf16_f32 v145, v146, v147\n\t"
    "v_mfma_f32_16x16x32_bf16 v[196:199], a[212:215], v[136:139], v[196:199]\n\t"
    "ds_read_b64_tr_b16 a[214:215], v225 offset:5128\n\t"
    "v_fma_f32 v152, v152, s60, v171\n\t"
    "v_exp_f32 v149, v149\n\t"
    "v_mfma_f32_16x16x32_bf16 v[200:203], a[216:219], v[136:139], v[200:203]\n\t"
    "ds_read_b64_tr_b16 a[210:211], v225 offset:5120\n\t"
    "v_cvt_pk_bf16_f32 v146, v148, v149\n\t"
    "v_mfma_f32_16x16x32_bf16 v[204:207], a[220:223], v[136:139], v[204:207]\n\t"
    "ds_read_b64_tr_b16 a[218:219], v225 offset:5632\n\t"
    "s_waitcnt lgkmcnt(10)\n\t"
    "v_mfma_f32_16x16x32_bf16 v[128:131], a[160:163], a[96:99], 0\n\t"
    "ds_read_b64_tr_b16 a[212:213], v225 offset:1032\n\t"
    "v_fma_f32 v153, v153, s60, v171\n\t"
    "v_exp_f32 v150, v150\n\t"
    "v_mfma_f32_16x16x32_bf16 v[128:131], a[164:167], a[100:103], v[128:131]\n\t"
    "ds_read_b64_tr_b16 a[220:221], v225 offset:1544\n\t"
    "s_waitcnt lgkmcnt(9)\n\t"
    "v_mfma_f32_16x16x32_bf16 v[128:131], a[168:171], a[104:107], v[128:131]\n\t"
    "ds_read_b64_tr_b16 a[216:217], v225 offset:1536\n\t"
    "v_fma_f32 v154, v154, s60, v171\n\t"
    "v_mfma_f32_16x16x32_bf16 v[128:131], a[172:175], a[108:111], v[128:131]\n\t"
    "ds_read_b64_tr_b16 a[222:223], v225 offset:5640\n\t"
    "s_waitcnt lgkmcnt(9)\n\t"
    "v_mfma_f32_16x16x32_bf16 v[132:135], a[176:179], a[96:99], 0\n\t"
    "v_exp_f32 v151, v151\n\t"
    "v_mfma_f32_16x16x32_bf16 v[132:135], a[180:183], a[100:103], v[132:135]\n\t"
    "s_waitcnt lgkmcnt(8)\n\t"
    "v_exp_f32 v152, v152\n\t"
    "v_cvt_pk_bf16_f32 v147, v150, v151\n\t"
    "v_mfma_f32_16x16x32_bf16 v[132:135], a[184:187], a[104:107], v[132:135]\n\t"
    "v_exp_f32 v153, v153\n\t"
    "v_mfma_f32_16x16x32_bf16 v[132:135], a[188:191], a[108:111], v[132:135]\n\t"
    "v_exp_f32 v154, v154\n\t"
    "v_mfma_f32_16x16x32_bf16 a[232:235], a[248:251], v[144:147], 0\n\t"
    "v_fma_f32 v155, v155, s60, v171\n\t"
    "v_fma_f32 v156, v156, s60, v171\n\t"
    "v_mfma_f32_16x16x32_bf16 v[64:67], a[192:195], v[144:147], 0\n\t"
    "v_exp_f32 v155, v155\n\t"
    "v_mfma_f32_16x16x32_bf16 v[68:71], a[196:199], v[144:147], 0\n\t"
    "v_exp_f32 v156, v156\n\t"
    "v_mfma_f32_16x16x32_bf16 v[72:75], a[200:203], v[144:147], 0\n\t"
    "v_fma_f32 v157, v157, s60, v171\n\t"
    "v_fma_f32 v158, v158, s60, v171\n\t"
    "v_mfma_f32_16x16x32_bf16 v[76:79], a[204:207], v[144:147], 0\n\t"
    "s_waitcnt lgkmcnt(3)\n\t"
    "v_exp_f32 v157, v157\n\t"
    "v_fma_f32 v159, v159, s60, v171\n\t"
    "v_exp_f32 v158, v158\n\t"
    "v_mfma_f32_16x16x32_bf16 v[80:83], a[208:211], v[144:147], 0\n\t"
    "s_waitcnt lgkmcnt(0)\n\t"
    "v_exp_f32 v159, v159\n\t"
    "v_cvt_pk_bf16_f32 v152, v152, v153\n\t"
    "v_cvt_pk_bf16_f32 v153, v154, v155\n\t"
    "v_cvt_pk_bf16_f32 v154, v156, v157\n\t"
    "v_fma_f32 v128, v128, s60, v170\n\t"
    "v_fma_f32 v129, v129, s60, v170\n\t"
    "v_fma_f32 v130, v130, s60, v170\n\t"
    "v_fma_f32 v131, v131, s60, v170\n\t"
    "v_mfma_f32_16x16x32_bf16 v[84:87], a[212:215], v[144:147], 0\n\t"
    "s_waitcnt lgkmcnt(0)\n\t"
    "v_exp_f32 v128, v128\n\t"
    "v_cvt_pk_bf16_f32 v155, v158, v159\n\t"
    "v_fma_f32 v132, v132, s60, v170\n\t"
    "v_mfma_f32_16x16x32_bf16 v[88:91], a[216:219], v[144:147], 0\n\t"
    "s_barrier\n\t"
    "v_exp_f32 v129, v129\n\t"
    "v_mfma_f32_16x16x32_bf16 v[92:95], a[220:223], v[144:147], 0\n\t"
    "s_xor_b32 s64, s52, 32768\n\t"
    "v_exp_f32 v130, v130\n\t"
    "v_mfma_f32_16x16x32_bf16 v[136:139], a[160:163], a[112:115], 0\n\t"
    "ds_read_b128 a[160:163], v224 offset:16384\n\t"
    "v_fma_f32 v133, v133, s60, v170\n\t"
    "v_mfma_f32_16x16x32_bf16 v[136:139], a[164:167], a[116:119], v[136:139]\n\t"
    "s_add_u32 m0, s63, 0\n\t"
    "v_exp_f32 v131, v131\n\t"
    "v_mfma_f32_16x16x32_bf16 v[136:139], a[168:171], a[120:123], v[136:139]\n\t"
    "ds_read_b128 a[164:167], v224 offset:16896\n\t"
    "v_fma_f32 v134, v134, s60, v170\n\t"
    "v_mfma_f32_16x16x32_bf16 v[136:139], a[172:175], a[124:127], v[136:139]\n\t"
    "buffer_load_dwordx4 v226, s[36:39], s64 offen offset:0 lds\n\t"
    "v_mfma_f32_16x16x32_bf16 v[140:143], a[176:179], a[112:115], 0\n\t"
    "ds_read_b128 a[168:171], v224 offset:17408\n\t"
    "v_fma_f32 v135, v135, s60, v170\n\t"
    "v_mfma_f32_16x16x32_bf16 v[140:143], a[180:183], a[116:119], v[140:143]\n\t"
    "ds_read_b128 a[180:183], v224 offset:20992\n\t"
    "v_cvt_pk_bf16_f32 v128, v128, v129\n\t"
    "v_mfma_f32_16x16x32_bf16 v[140:143], a[184:187], a[120:123], v[140:143]\n\t"
    "ds_read_b128 a[172:175], v224 offset:17920\n\t"
    "v_cvt_pk_bf16_f32 v129, v130, v131\n\t"
    "v_mfma_f32_16x16x32_bf16 v[140:143], a[188:191], a[124:127], v[140:143]\n\t"
    "ds_read_b128 a[188:191], v224 offset:22016\n\t"
    "v_exp_f32 v132, v132\n\t"
    "v_mfma_f32_16x16x32_bf16 a[236:239], a[248:251], v[152:155], 0\n\t"
    "ds_read_b128 a[176:179], v224 offset:20480\n\t"
    "v_fma_f32 v136, v136, s60, v171\n\t"
    "v_mfma_f32_16x16x32_bf16 v[96:99], a[192:195], v[152:155], 0\n\t"
    "buffer_load_dwordx4 v226, s[36:39], s64 offen offset:2048 lds\n\t"
    "v_mfma_f32_16x16x32_bf16 v[100:103], a[196:199], v[152:155], 0\n\t"
    "ds_read_b128 a[184:187], v224 offset:21504\n\t"
    "v_fma_f32 v137, v137, s60, v171\n\t"
    "v_exp_f32 v133, v133\n\t"
    "v_mfma_f32_16x16x32_bf16 v[104:107], a[200:203], v[152:155], 0\n\t"
    "s_add_u32 m0, s63, 32768\n\t"
    "v_exp_f32 v134, v134\n\t"
    "v_mfma_f32_16x16x32_bf16 v[108:111], a[204:207], v[152:155], 0\n\t"
    "buffer_load_dwordx4 v226, s[40:43], s64 offen offset:0 lds\n\t"
    "v_mfma_f32_16x16x32_bf16 v[112:115], a[208:211], v[152:155], 0\n\t"
    "ds_read_b64_tr_b16 a[208:209], v225 offset:9216\n\t"
    "v_cvt_pk_bf16_f32 v130, v132, v133\n\t"
    "v_mfma_f32_16x16x32_bf16 v[116:119], a[212:215], v[152:155], 0\n\t"
    "ds_read_b64_tr_b16 a[214:215], v225 offset:13320\n\t"
    "v_fma_f32 v138, v138, s60, v171\n\t"
    "v_mfma_f32_16x16x32_bf16 v[120:123], a[216:219], v[152:155], 0\n\t"
    "ds_read_b64_tr_b16 a[210:211], v225 offset:13312\n\t"
    "v_fma_f32 v139, v139, s60, v171\n\t"
    "v_exp_f32 v135, v135\n\t"
    "v_mfma_f32_16x16x32_bf16 v[124:127], a[220:223], v[152:155], 0\n\t"
    "ds_read_b64_tr_b16 a[218:219], v225 offset:13824\n\t"
    "s_waitcnt lgkmcnt(10)\n\t"
    "v_mfma_f32_16x16x32_bf16 v[144:147], a[160:163], a[96:99], 0\n\t"
    "ds_read_b64_tr_b16 a[212:213], v225 offset:9224\n\t"
    "v_cvt_pk_bf16_f32 v131, v134, v135\n\t"
    "v_mfma_f32_16x16x32_bf16 v[144:147], a[164:167], a[100:103], v[144:147]\n\t"
    "ds_read_b64_tr_b16 a[220:221], v225 offset:9736\n\t"
    "s_waitcnt lgkmcnt(9)\n\t"
    "v_mfma_f32_16x16x32_bf16 v[144:147], a[168:171], a[104:107], v[144:147]\n\t"
    "ds_read_b64_tr_b16 a[216:217], v225 offset:9728\n\t"
    "v_fma_f32 v140, v140, s60, v171\n\t"
    "v_mfma_f32_16x16x32_bf16 v[144:147], a[172:175], a[108:111], v[144:147]\n\t"
    "ds_read_b64_tr_b16 a[222:223], v225 offset:13832\n\t"
    "s_waitcnt lgkmcnt(9)\n\t"
    "v_mfma_f32_16x16x32_bf16 v[148:151], a[176:179], a[96:99], 0\n\t"
    "buffer_load_dwordx4 v226, s[40:43], s64 offen offset:2048 lds\n\t"
    "v_mfma_f32_16x16x32_bf16 v[148:151], a[180:183], a[100:103], v[148:151]\n\t"
    "ds_read_b64_tr_b16 a[192:193], v225 offset:16384\n\t"
    "s_waitcnt lgkmcnt(9)\n\t"
    "v_mfma_f32_16x16x32_bf16 v[148:151], a[184:187], a[104:107], v[148:151]\n\t"
    "v_exp_f32 v136, v136\n\t"
    "v_mfma_f32_16x16x32_bf16 v[148:151], a[188:191], a[108:111], v[148:151]\n\t"
    "ds_read_b64_tr_b16 a[194:195], v225 offset:20480\n\t"
    "v_fma_f32 v141, v141, s60, v171\n\t"
    "v_mfma_f32_16x16x32_bf16 a[232:235], a[248:251], v[128:131], a[232:235]\n\t"
    "v_exp_f32 v137, v137\n\t"
    "v_mfma_f32_16x16x32_bf16 v[64:67], a[32:35], v[128:131], v[64:67]\n\t"
    "ds_read_b64_tr_b16 a[196:197], v225 offset:16392\n\t"
    "v_fma_f32 v142, v142, s60, v171\n\t"
    "v_mfma_f32_16x16x32_bf16 v[68:71], a[36:39], v[128:131], v[68:71]\n\t"
    "v_exp_f32 v138, v138\n\t"
    "v_mfma_f32_16x16x32_bf16 v[72:75], a[40:43], v[128:131], v[72:75]\n\t"
    "ds_read_b64_tr_b16 a[198:199], v225 offset:20488\n\t"
    "v_fma_f32 v143, v143, s60, v171\n\t"
    "v_mfma_f32_16x16x32_bf16 v[76:79], a[44:47], v[128:131], v[76:79]\n\t"
    "s_waitcnt lgkmcnt(7)\n\t"
    "v_exp_f32 v139, v139\n\t"
    "v_mfma_f32_16x16x32_bf16 v[80:83], a[208:211], v[128:131], v[80:83]\n\t"
    "ds_read_b64_tr_b16 a[200:201], v225 offset:16896\n\t"
    "v_cvt_pk_bf16_f32 v136, v136, v137\n\t"
    "v_mfma_f32_16x16x32_bf16 v[84:87], a[212:215], v[128:131], v[84:87]\n\t"
    "s_waitcnt lgkmcnt(5)\n\t"
    "v_exp_f32 v140, v140\n\t"
    "v_exp_f32 v141, v141\n\t"
    "v_mfma_f32_16x16x32_bf16 v[88:91], a[216:219], v[128:131], v[88:91]\n\t"
    "ds_read_b64_tr_b16 a[202:203], v225 offset:20992\n\t"
    "v_cvt_pk_bf16_f32 v137, v138, v139\n\t"
    "v_mfma_f32_16x16x32_bf16 v[92:95], a[220:223], v[128:131], v[92:95]\n\t"
    "s_waitcnt lgkmcnt(6)\n\t"
    "v_exp_f32 v142, v142\n\t"
    "v_mfma_f32_16x16x32_bf16 v[152:155], a[160:163], a[112:115], 0\n\t"
    "ds_read_b64_tr_b16 a[204:205], v225 offset:16904\n\t"
    "v_cvt_pk_bf16_f32 v138, v140, v141\n\t"
    "v_mfma_f32_16x16x32_bf16 v[152:155], a[164:167], a[116:119], v[152:155]\n\t"
    "s_barrier\n\t"
    "v_exp_f32 v143, v143\n\t"
    "v_mfma_f32_16x16x32_bf16 v[152:155], a[168:171], a[120:123], v[152:155]\n\t"
    "ds_read_b64_tr_b16 a[206:207], v225 offset:21000\n\t"
    "v_cvt_pk_bf16_f32 v139, v142, v143\n\t"
    "v_mfma_f32_16x16x32_bf16 v[152:155], a[172:175], a[124:127], v[152:155]\n\t"
    "s_xor_b32 s64, s52, 40960\n\t"
    "v_fma_f32 v144, v144, s60, v170\n\t"
    "v_fma_f32 v145, v145, s60, v170\n\t"
    "v_mfma_f32_16x16x32_bf16 v[156:159], a[176:179], a[112:115], 0\n\t"
    "s_add_u32 m0, s63, 8192\n\t"
    "v_exp_f32 v144, v144\n\t"
    "v_mfma_f32_16x16x32_bf16 v[156:159], a[180:183], a[116:119], v[156:159]\n\t"
    "buffer_load_dwordx4 v226, s[36:39], s64 offen offset:0 lds\n\t"
    "v_mfma_f32_16x16x32_bf16 v[156:159], a[184:187], a[120:123], v[156:159]\n\t"
    "v_exp_f32 v145, v145\n\t"
    "v_mfma_f32_16x16x32_bf16 v[156:159], a[188:191], a[124:127], v[156:159]\n\t"
    "buffer_load_dwordx4 v226, s[36:39], s64 offen offset:2048 lds\n\t"
    "v_mfma_f32_16x16x32_bf16 a[236:239], a[248:251], v[136:139], a[236:239]\n\t"
    "v_fma_f32 v146, v146, s60, v170\n\t"
    "v_fma_f32 v147, v147, s60, v170\n\t"
    "v_fma_f32 v148, v148, s60, v170\n\t"
    "v_mfma_f32_16x16x32_bf16 v[96:99], a[32:35], v[136:139], v[96:99]\n\t"
    "s_add_u32 m0, s63, 40960\n\t"
    "v_exp_f32 v146, v146\n\t"
    "v_mfma_f32_16x16x32_bf16 v[100:103], a[36:39], v[136:139], v[100:103]\n\t"
    "buffer_load_dwordx4 v226, s[40:43], s64 offen offset:0 lds\n\t"
    "v_mfma_f32_16x16x32_bf16 v[104:107], a[40:43], v[136:139], v[104:107]\n\t"
    "v_exp_f32 v147, v147\n\t"
    "v_mfma_f32_16x16x32_bf16 v[108:111], a[44:47], v[136:139], v[108:111]\n\t"
    "buffer_load_dwordx4 v226, s[40:43], s64 offen offset:2048 lds\n\t"
    "v_mfma_f32_16x16x32_bf16 v[112:115], a[208:211], v[136:139], v[112:115]\n\t"
    "ds_read_b64_tr_b16 a[208:209], v225 offset:17408\n\t"
    "v_fma_f32 v149, v149, s60, v170\n\t"
    "v_mfma_f32_16x16x32_bf16 v[116:119], a[212:215], v[136:139], v[116:119]\n\t"
    "ds_read_b64_tr_b16 a[210:211], v225 offset:21504\n\t"
    "v_fma_f32 v150, v150, s60, v170\n\t"
    "v_mfma_f32_16x16x32_bf16 v[120:123], a[216:219], v[136:139], v[120:123]\n\t"
    "ds_read_b64_tr_b16 a[212:213], v225 offset:17416\n\t"
    "ds_read_b64_tr_b16 a[216:217], v225 offset:17920\n\t"
    "v_mfma_f32_16x16x32_bf16 v[124:127], a[220:223], v[136:139], v[124:127]\n\t"
    "ds_read_b64_tr_b16 a[214:215], v225 offset:21512\n\t"
    "ds_read_b64_tr_b16 a[218:219], v225 offset:22016\n\t"
    "ds_read_b64_tr_b16 a[220:221], v225 offset:17928\n\t"
    "ds_read_b64_tr_b16 a[222:223], v225 offset:22024\n\t"
    "v_exp_f32 v148, v148\n\t"
    "v_fma_f32 v151, v151, s60, v170\n\t"
    "v_fma_f32 v152, v152, s60, v171\n\t"
    "v_fma_f32 v153, v153, s60, v171\n\t"
    "v_exp_f32 v149, v149\n\t"
    "v_exp_f32 v150, v150\n\t"
    "v_cvt_pk_bf16_f32 v144, v144, v145\n\t"
    "v_cvt_pk_bf16_f32 v145, v146, v147\n\t"
    "v_cvt_pk_bf16_f32 v146, v148, v149\n\t"
    "s_waitcnt vmcnt(12) lgkmcnt(0)\n\t"
    "s_barrier\n\t"
    "v_and_b32 v239, 0x30, v227\n\t"
    "v_bfe_u32 v238, v227, 8, 2\n\t"
    "v_lshl_add_u32 v239, v239, 2, %14\n\t"
    "v_lshl_add_u32 v239, v238, 4, v239\n\t"
    "ds_read_b64_tr_b16 a[32:33], v225 offset:24576\n\t"
    "s_xor_b32 s64, s52, 49152\n\t"
    "ds_read_b64_tr_b16 a[34:35], v225 offset:28672\n\t"
    "s_add_u32 m0, s63, 16384\n\t"
    "ds_read_b64_tr_b16 a[36:37], v225 offset:24584\n\t"
    "buffer_load_dwordx4 v226, s[36:39], s64 offen offset:0 lds\n\t"
    "ds_read_b64_tr_b16 a[38:39], v225 offset:28680\n\t"
    "ds_read_b64_tr_b16 a[40:41], v225 offset:25088\n\t"
    "ds_read_b64_tr_b16 a[42:43], v225 offset:29184\n\t"
    "buffer_load_dwordx4 v226, s[36:39], s64 offen offset:2048 lds\n\t"
    "ds_read_b64_tr_b16 a[44:45], v225 offset:25096\n\t"
    "ds_read_b64_tr_b16 a[46:47], v225 offset:29192\n\t"
    "s_waitcnt lgkmcnt(0)\n\t"
    "s_nop 1\n\t"
    "s_nop 7\n\t"
    "s_branch .Lent%=\n\t"
    ".p2align 6\n\t"
    ".Lbody%=:\n\t"
    "v_mfma_f32_16x16x32_bf16 v[164:167], a[48:51], a[80:83], 0\n\t"
    "ds_write_b128 v231, v[24:27] offset:128\n\t"
    "v_mfma_f32_16x16x32_bf16 v[164:167], a[52:55], a[84:87], v[164:167]\n\t"
    "s_or_b64 s[58:59], s[58:59], vcc\n\t"
    "v_cvt_pk_bf16_f32 v81, v82, v83\n\t"
    "v_cvt_pk_bf16_f32 v82, v84, v85\n\t"
    "v_mfma_f32_16x16x32_bf16 v[164:167], a[56:59], a[88:91], v[164:167]\n\t"
    "v_cvt_pk_bf16_f32 v83, v86, v87\n\t"
    "v_cvt_pk_bf16_f32 v88, v88, v89\n\t"
    "v_mfma_f32_16x16x32_bf16 v[164:167], a[60:63], a[92:95], v[164:167]\n\t"
    "s_waitcnt lgkmcnt(12)\n\t"
    "v_cvt_pk_bf16_f32 v89, v90, v91\n\t"
    "v_cvt_pk_bf16_f32 v90, v92, v93\n\t"
    "v_mfma_f32_16x16x32_bf16 v[128:131], a[160:163], a[64:67], 0\n\t"
    "ds_write_b128 v230, v[32:35] offset:4096\n\t"
    "v_pk_mul_f32 v[96:97], v[96:97], v[238:239] op_sel:[0,1] op_sel_hi:[1,1]\n\t"
    "v_pk_mul_f32 v[98:99], v[98:99], v[238:239] op_sel:[0,1] op_sel_hi:[1,1]\n\t"
    "v_pk_mul_f32 v[100:101], v[100:101], v[238:239] op_sel:[0,1] op_sel_hi:[1,1]\n\t"
    "v_pk_mul_f32 v[102:103], v[102:103], v[238:239] op_sel:[0,1] op_sel_hi:[1,1]\n\t"
    "v_pk_mul_f32 v[104:105], v[104:105], v[238:239] op_sel:[0,1] op_sel_hi:[1,1]\n\t"
    "v_pk_mul_f32 v[106:107], v[106:107], v[238:239] op_sel:[0,1] op_sel_hi:[1,1]\n\t"
    "v_pk_mul_f32 v[108:109], v[108:109], v[238:239] op_sel:[0,1] op_sel_hi:[1,1]\n\t"
    "v_pk_mul_f32 v[110:111], v[110:111], v[238:239] op_sel:[0,1] op_sel_hi:[1,1]\n\t"
    "v_pk_mul_f32 v[112:113], v[112:113], v[238:239] op_sel:[0,1] op_sel_hi:[1,1]\n\t"
    "v_pk_mul_f32 v[114:115], v[114:115], v[238:239] op_sel:[0,1] op_sel_hi:[1,1]\n\t"
    "v_pk_mul_f32 v[116:117], v[116:117], v[238:239] op_sel:[0,1] op_sel_hi:[1,1]\n\t"
    "v_pk_mul_f32 v[118:119], v[118:119], v[238:239] op_sel:[0,1] op_sel_hi:[1,1]\n\t"
    "v_pk_mul_f32 v[120:121], v[120:121], v[238:239] op_sel:[0,1] op_sel_hi:[1,1]\n\t"
    "v_pk_mul_f32 v[122:123], v[122:123], v[238:239] op_sel:[0,1] op_sel_hi:[1,1]\n\t"
    "v_mfma_f32_16x16x32_bf16 v[128:131], a[164:167], a[68:71], v[128:131]\n\t"
    "s_waitcnt lgkmcnt(10)\n\t"
    "v_cvt_pk_bf16_f32 v91, v94, v95\n\t"
    "v_cvt_pk_bf16_f32 v96, v96, v97\n\t"
    "v_cvt_pk_bf16_f32 v97, v98, v99\n\t"
    "v_cvt_pk_bf16_f32 v98, v100, v101\n\t"
    "v_cvt_pk_bf16_f32 v99, v102, v103\n\t"
    "v_cvt_pk_bf16_f32 v104, v104, v105\n\t"
    "v_cvt_pk_bf16_f32 v105, v106, v107\n\t"
    "v_cvt_pk_bf16_f32 v106, v108, v109\n\t"
    "v_cvt_pk_bf16_f32 v107, v110, v111\n\t"
    "v_cvt_pk_bf16_f32 v112, v112, v113\n\t"
    "v_cvt_pk_bf16_f32 v113, v114, v115\n\t"
    "v_cvt_pk_bf16_f32 v114, v116, v117\n\t"
    "v_pk_mul_f32 v[124:125], v[124:125], v[238:239] op_sel:[0,1] op_sel_hi:[1,1]\n\t"
    "v_pk_mul_f32 v[126:127], v[126:127], v[238:239] op_sel:[0,1] op_sel_hi:[1,1]\n\t"
    "v_mfma_f32_16x16x32_bf16 v[128:131], a[168:171], a[72:75], v[128:131]\n\t"
    "v_cvt_pk_bf16_f32 v115, v118, v119\n\t"
    "v_cvt_pk_bf16_f32 v120, v120, v121\n\t"
    "v_cvt_pk_bf16_f32 v121, v122, v123\n\t"
    "v_cvt_pk_bf16_f32 v122, v124, v125\n\t"
    "v_cvt_pk_bf16_f32 v123, v126, v127\n\t"
    "v_mfma_f32_16x16x32_bf16 v[128:131], a[172:175], a[76:79], v[128:131]\n\t"
    "ds_write_b128 v231, v[40:43] offset:4096\n\t"
    "s_waitcnt lgkmcnt(7)\n\t"
    "v_mfma_f32_16x16x32_bf16 v[132:135], a[176:179], a[64:67], 0\n\t"
    "v_max3_f32 v164, v164, v165, v166\n\t"
    "v_max_f32 v164, v164, v167\n\t"
    "v_mul_f32 v169, s61, v164\n\t"
    "v_mfma_f32_16x16x32_bf16 v[132:135], a[180:183], a[68:71], v[132:135]\n\t"
    "s_waitcnt lgkmcnt(3)\n\t"
    "v_mfma_f32_16x16x32_bf16 v[132:135], a[184:187], a[72:75], v[132:135]\n\t"
    "ds_write_b128 v230, v[48:51] offset:4224\n\t"
    "v_mfma_f32_16x16x32_bf16 v[132:135], a[188:191], a[76:79], v[132:135]\n\t"
    "v_mfma_f32_16x16x32_bf16 a[240:243], a[248:251], v[144:147], a[240:243]\n\t"
    "v_fma_f32 v128, v128, s60, v168\n\t"
    "v_fma_f32 v129, v129, s60, v168\n\t"
    "v_mfma_f32_16x16x32_bf16 v[208:211], a[32:35], v[144:147], v[208:211]\n\t"
    "ds_write_b128 v231, v[56:59] offset:4224\n\t"
    "v_exp_f32 v128, v128\n\t"
    "v_mfma_f32_16x16x32_bf16 v[212:215], a[36:39], v[144:147], v[212:215]\n\t"
    "v_fma_f32 v130, v130, s60, v168\n\t"
    "v_fma_f32 v131, v131, s60, v168\n\t"
    "v_mfma_f32_16x16x32_bf16 v[216:219], a[40:43], v[144:147], v[216:219]\n\t"
    "v_exp_f32 v129, v129\n\t"
    "v_mfma_f32_16x16x32_bf16 v[220:223], a[44:47], v[144:147], v[220:223]\n\t"
    "ds_write_b128 v230, v[64:67] offset:8192\n\t"
    "v_fma_f32 v132, v132, s60, v168\n\t"
    "v_fma_f32 v133, v133, s60, v168\n\t"
    "v_mfma_f32_16x16x32_bf16 v[240:243], a[208:211], v[144:147], v[240:243]\n\t"
    "v_exp_f32 v130, v130\n\t"
    "v_accvgpr_read_b32 v238, a240\n\t"
    "v_subrev_u32 v236, 0x35800000, v238\n\t"
    "v_rcp_f32 v238, v238\n\t"
    "v_cmp_le_u32 vcc, s62, v236\n\t"
    "v_mfma_f32_16x16x32_bf16 v[244:247], a[212:215], v[144:147], v[244:247]\n\t"
    "s_nop 0\n\t"
    "s_or_b64 s[58:59], s[58:59], vcc\n\t"
    "v_fma_f32 v134, v134, s60, v168\n\t"
    "v_fma_f32 v135, v135, s60, v168\n\t"
    "v_mfma_f32_16x16x32_bf16 v[248:251], a[216:219], v[144:147], v[248:251]\n\t"
    "ds_write_b128 v231, v[72:75] offset:8192\n\t"
    "v_exp_f32 v131, v131\n\t"
    "v_mfma_f32_16x16x32_bf16 v[252:255], a[220:223], v[144:147], v[252:255]\n\t"
    "s_waitcnt lgkmcnt(15)\n\t"
    "v_cvt_pk_bf16_f32 v128, v128, v129\n\t"
    "v_cvt_pk_bf16_f32 v129, v130, v131\n\t"
    "v_mfma_f32_16x16x32_bf16 v[160:163], a[48:51], a[96:99], 0\n\t"
    "v_exp_f32 v132, v132\n\t"
    "v_mfma_f32_16x16x32_bf16 v[160:163], a[52:55], a[100:103], v[160:163]\n\t"
    "ds_write_b128 v230, v[80:83] offset:8320\n\t"
    "v_mfma_f32_16x16x32_bf16 v[160:163], a[56:59], a[104:107], v[160:163]\n\t"
    "v_exp_f32 v133, v133\n\t"
    "v_mfma_f32_16x16x32_bf16 v[160:163], a[60:63], a[108:111], v[160:163]\n\t"
    "v_cvt_pk_bf16_f32 v130, v132, v133\n\t"
    "v_mfma_f32_16x16x32_bf16 v[136:139], a[160:163], a[80:83], 0\n\t"
    "v_exp_f32 v134, v134\n\t"
    "v_pk_mul_f32 v[208:209], v[208:209], v[238:239] op_sel_hi:[1,0]\n\t"
    "v_pk_mul_f32 v[210:211], v[210:211], v[238:239] op_sel_hi:[1,0]\n\t"
    "v_pk_mul_f32 v[212:213], v[212:213], v[238:239] op_sel_hi:[1,0]\n\t"
    "v_pk_mul_f32 v[214:215], v[214:215], v[238:239] op_sel_hi:[1,0]\n\t"
    "v_pk_mul_f32 v[216:217], v[216:217], v[238:239] op_sel_hi:[1,0]\n\t"
    "v_pk_mul_f32 v[218:219], v[218:219], v[238:239] op_sel_hi:[1,0]\n\t"
    "v_pk_mul_f32 v[220:221], v[220:221], v[238:239] op_sel_hi:[1,0]\n\t"
    "v_pk_mul_f32 v[222:223], v[222:223], v[238:239] op_sel_hi:[1,0]\n\t"
    "v_pk_mul_f32 v[240:241], v[240:241], v[238:239] op_sel_hi:[1,0]\n\t"
    "v_pk_mul_f32 v[242:243], v[242:243], v[238:239] op_sel_hi:[1,0]\n\t"
    "v_pk_mul_f32 v[244:245], v[244:245], v[238:239] op_sel_hi:[1,0]\n\t"
    "v_pk_mul_f32 v[246:247], v[246:247], v[238:239] op_sel_hi:[1,0]\n\t"
    "v_pk_mul_f32 v[248:249], v[248:249], v[238:239] op_sel_hi:[1,0]\n\t"
    "v_pk_mul_f32 v[250:251], v[250:251], v[238:239] op_sel_hi:[1,0]\n\t"
    "v_pk_mul_f32 v[252:253], v[252:253], v[238:239] op_sel_hi:[1,0]\n\t"
    "v_pk_mul_f32 v[254:255], v[254:255], v[238:239] op_sel_hi:[1,0]\n\t"
    "v_mfma_f32_16x16x32_bf16 v[136:139], a[164:167], a[84:87], v[136:139]\n\t"
    "ds_write_b128 v231, v[88:91] offset:8320\n\t"
    "v_cvt_pk_bf16_f32 v208, v208, v209\n\t"
    "v_cvt_pk_bf16_f32 v209, v210, v211\n\t"
    "v_cvt_pk_bf16_f32 v210, v212, v213\n\t"
    "v_cvt_pk_bf16_f32 v211, v214, v215\n\t"
    "v_cvt_pk_bf16_f32 v216, v216, v217\n\t"
    "v_cvt_pk_bf16_f32 v217, v218, v219\n\t"
    "v_cvt_pk_bf16_f32 v218, v220, v221\n\t"
    "v_cvt_pk_bf16_f32 v219, v222, v223\n\t"
    "v_cvt_pk_bf16_f32 v240, v240, v241\n\t"
    "v_cvt_pk_bf16_f32 v241, v242, v243\n\t"
    "v_cvt_pk_bf16_f32 v242, v244, v245\n\t"
    "v_cvt_pk_bf16_f32 v243, v246, v247\n\t"
    "v_mfma_f32_16x16x32_bf16 v[136:139], a[168:171], a[88:91], v[136:139]\n\t"
    "v_exp_f32 v135, v135\n\t"
    "v_cvt_pk_bf16_f32 v248, v248, v249\n\t"
    "v_cvt_pk_bf16_f32 v249, v250, v251\n\t"
    "v_cvt_pk_bf16_f32 v250, v252, v253\n\t"
    "v_cvt_pk_bf16_f32 v251, v254, v255\n\t"
    "v_mfma_f32_16x16x32_bf16 v[136:139], a[172:175], a[92:95], v[136:139]\n\t"
    "v_cvt_pk_bf16_f32 v131, v134, v135\n\t"
    "v_mfma_f32_16x16x32_bf16 v[140:143], a[176:179], a[80:83], 0\n\t"
    "ds_write_b128 v230, v[96:99] offset:12288\n\t"
    "v_max3_f32 v160, v160, v161, v162\n\t"
    "v_max_f32 v160, v160, v163\n\t"
    "v_mfma_f32_16x16x32_bf16 v[140:143], a[180:183], a[84:87], v[140:143]\n\t"
    "v_mul_f32 v170, s61, v160\n\t"
    "v_mfma_f32_16x16x32_bf16 v[140:143], a[184:187], a[88:91], v[140:143]\n\t"
    "v_mfma_f32_16x16x32_bf16 v[140:143], a[188:191], a[92:95], v[140:143]\n\t"
    "ds_write_b128 v231, v[104:107] offset:12288\n\t"
    "v_mfma_f32_16x16x32_bf16 a[244:247], a[248:251], v[152:155], a[244:247]\n\t"
    "v_fma_f32 v136, v136, s60, v169\n\t"
    "v_fma_f32 v137, v137, s60, v169\n\t"
    "v_mfma_f32_16x16x32_bf16 v[176:179], a[32:35], v[152:155], v[176:179]\n\t"
    "v_exp_f32 v136, v136\n\t"
    "v_mfma_f32_16x16x32_bf16 v[180:183], a[36:39], v[152:155], v[180:183]\n\t"
    "ds_write_b128 v230, v[112:115] offset:12416\n\t"
    "v_fma_f32 v138, v138, s60, v169\n\t"
    "v_fma_f32 v139, v139, s60, v169\n\t"
    "v_mfma_f32_16x16x32_bf16 v[184:187], a[40:43], v[152:155], v[184:187]\n\t"
    "v_exp_f32 v137, v137\n\t"
    "v_mfma_f32_16x16x32_bf16 v[188:191], a[44:47], v[152:155], v[188:191]\n\t"
    "v_fma_f32 v140, v140, s60, v169\n\t"
    "v_fma_f32 v141, v141, s60, v169\n\t"
    "v_mfma_f32_16x16x32_bf16 v[192:195], a[208:211], v[152:155], v[192:195]\n\t"
    "ds_read_b64_tr_b16 a[208:209], v225 offset:1024\n\t"
    "v_fma_f32 v142, v142, s60, v169\n\t"
    "v_accvgpr_read_b32 v239, a244\n\t"
    "v_subrev_u32 v237, 0x35800000, v239\n\t"
    "v_rcp_f32 v239, v239\n\t"
    "v_cmp_le_u32 vcc, s62, v237\n\t"
    "v_mfma_f32_16x16x32_bf16 v[196:199], a[212:215], v[152:155], v[196:199]\n\t"
    "ds_write_b128 v231, v[120:123] offset:12416\n\t"
    "s_or_b64 s[58:59], s[58:59], vcc\n\t"
    "v_exp_f32 v138, v138\n\t"
    "v_mfma_f32_16x16x32_bf16 v[200:203], a[216:219], v[152:155], v[200:203]\n\t"
    "ds_read_b64_tr_b16 a[210:211], v225 offset:5120\n\t"
    "v_fma_f32 v143, v143, s60, v169\n\t"
    "v_mfma_f32_16x16x32_bf16 v[204:207], a[220:223], v[152:155], v[204:207]\n\t"
    "s_waitcnt lgkmcnt(15)\n\t"
    "v_exp_f32 v139, v139\n\t"
    "v_mfma_f32_16x16x32_bf16 v[164:167], a[48:51], a[112:115], 0\n\t"
    "ds_read_b64_tr_b16 a[212:213], v225 offset:1032\n\t"
    "v_cvt_pk_bf16_f32 v136, v136, v137\n\t"
    "v_mfma_f32_16x16x32_bf16 v[164:167], a[52:55], a[116:119], v[164:167]\n\t"
    "ds_write_b128 v230, v[208:211] offset:16384\n\t"
    "v_exp_f32 v140, v140\n\t"
    "v_mfma_f32_16x16x32_bf16 v[164:167], a[56:59], a[120:123], v[164:167]\n\t"
    "ds_read_b64_tr_b16 a[214:215], v225 offset:5128\n\t"
    "v_cvt_pk_bf16_f32 v137, v138, v139\n\t"
    "v_mfma_f32_16x16x32_bf16 v[164:167], a[60:63], a[124:127], v[164:167]\n\t"
    "v_exp_f32 v141, v141\n\t"
    "v_mfma_f32_16x16x32_bf16 v[144:147], a[160:163], a[96:99], 0\n\t"
    "ds_read_b64_tr_b16 a[216:217], v225 offset:1536\n\t"
    "v_cvt_pk_bf16_f32 v138, v140, v141\n\t"
    "v_pk_mul_f32 v[176:177], v[176:177], v[238:239] op_sel:[0,1] op_sel_hi:[1,1]\n\t"
    "v_pk_mul_f32 v[178:179], v[178:179], v[238:239] op_sel:[0,1] op_sel_hi:[1,1]\n\t"
    "v_pk_mul_f32 v[180:181], v[180:181], v[238:239] op_sel:[0,1] op_sel_hi:[1,1]\n\t"
    "v_pk_mul_f32 v[182:183], v[182:183], v[238:239] op_sel:[0,1] op_sel_hi:[1,1]\n\t"
    "v_pk_mul_f32 v[184:185], v[184:185], v[238:239] op_sel:[0,1] op_sel_hi:[1,1]\n\t"
    "v_pk_mul_f32 v[186:187], v[186:187], v[238:239] op_sel:[0,1] op_sel_hi:[1,1]\n\t"
    "v_pk_mul_f32 v[188:189], v[188:189], v[238:239] op_sel:[0,1] op_sel_hi:[1,1]\n\t"
    "v_pk_mul_f32 v[190:191], v[190:191], v[238:239] op_sel:[0,1] op_sel_hi:[1,1]\n\t"
    "v_pk_mul_f32 v[192:193], v[192:193], v[238:239] op_sel:[0,1] op_sel_hi:[1,1]\n\t"
    "v_pk_mul_f32 v[194:195], v[194:195], v[238:239] op_sel:[0,1] op_sel_hi:[1,1]\n\t"
    "v_pk_mul_f32 v[196:197], v[196:197], v[238:239] op_sel:[0,1] op_sel_hi:[1,1]\n\t"
    "v_pk_mul_f32 v[198:199], v[198:199], v[238:239] op_sel:[0,1] op_sel_hi:[1,1]\n\t"
    "v_pk_mul_f32 v[200:201], v[200:201], v[238:239] op_sel:[0,1] op_sel_hi:[1,1]\n\t"
    "v_pk_mul_f32 v[202:203], v[202:203], v[238:239] op_sel:[0,1] op_sel_hi:[1,1]\n\t"
    "v_pk_mul_f32 v[204:205], v[204:205], v[238:239] op_sel:[0,1] op_sel_hi:[1,1]\n\t"
    "v_pk_mul_f32 v[206:207], v[206:207], v[238:239] op_sel:[0,1] op_sel_hi:[1,1]\n\t"
    "v_mfma_f32_16x16x32_bf16 v[144:147], a[164:167], a[100:103], v[144:147]\n\t"
    "ds_write_b128 v231, v[216:219] offset:16384\n\t"
    "v_exp_f32 v142, v142\n\t"
    "v_cvt_pk_bf16_f32 v176, v176, v177\n\t"
    "v_cvt_pk_bf16_f32 v177, v178, v179\n\t"
    "v_cvt_pk_bf16_f32 v178, v180, v181\n\t"
    "v_cvt_pk_bf16_f32 v179, v182, v183\n\t"
    "v_cvt_pk_bf16_f32 v184, v184, v185\n\t"
    "v_cvt_pk_bf16_f32 v185, v186, v187\n\t"
    "v_cvt_pk_bf16_f32 v186, v188, v189\n\t"
    "v_cvt_pk_bf16_f32 v187, v190, v191\n\t"
    "v_cvt_pk_bf16_f32 v192, v192, v193\n\t"
    "v_cvt_pk_bf16_f32 v193, v194, v195\n\t"
    "v_cvt_pk_bf16_f32 v194, v196, v197\n\t"
    "v_mfma_f32_16x16x32_bf16 v[144:147], a[168:171], a[104:107], v[144:147]\n\t"
    "ds_read_b64_tr_b16 a[218:219], v225 offset:5632\n\t"
    "v_cvt_pk_bf16_f32 v195, v198, v199\n\t"
    "v_cvt_pk_bf16_f32 v200, v200, v201\n\t"
    "v_cvt_pk_bf16_f32 v201, v202, v203\n\t"
    "v_cvt_pk_bf16_f32 v202, v204, v205\n\t"
    "v_cvt_pk_bf16_f32 v203, v206, v207\n\t"
    "v_mfma_f32_16x16x32_bf16 v[144:147], a[172:175], a[108:111], v[144:147]\n\t"
    "v_exp_f32 v143, v143\n\t"
    "v_mfma_f32_16x16x32_bf16 v[148:151], a[176:179], a[96:99], 0\n\t"
    "ds_read_b64_tr_b16 a[220:221], v225 offset:1544\n\t"
    "v_cvt_pk_bf16_f32 v139, v142, v143\n\t"
    "v_max3_f32 v164, v164, v165, v166\n\t"
    "v_max_f32 v164, v164, v167\n\t"
    "v_mul_f32 v171, s61, v164\n\t"
    "v_mfma_f32_16x16x32_bf16 v[148:151], a[180:183], a[100:103], v[148:151]\n\t"
    "ds_read_b64_tr_b16 a[222:223], v225 offset:5640\n\t"
    "v_mfma_f32_16x16x32_bf16 v[148:151], a[184:187], a[104:107], v[148:151]\n\t"
    "ds_write_b128 v230, v[240:243] offset:16512\n\t"
    "v_mfma_f32_16x16x32_bf16 v[148:151], a[188:191], a[108:111], v[148:151]\n\t"
    "v_mfma_f32_16x16x32_bf16 a[224:227], a[248:251], v[128:131], 0\n\t"
    "s_waitcnt lgkmcnt(15)\n\t"
    "v_fma_f32 v144, v144, s60, v170\n\t"
    "v_fma_f32 v145, v145, s60, v170\n\t"
    "v_mfma_f32_16x16x32_bf16 v[0:3], a[192:195], v[128:131], 0\n\t"
    "ds_write_b128 v231, v[248:251] offset:16512\n\t"
    "v_exp_f32 v144, v144\n\t"
    "v_mfma_f32_16x16x32_bf16 v[4:7], a[196:199], v[128:131], 0\n\t"
    "v_fma_f32 v146, v146, s60, v170\n\t"
    "v_fma_f32 v147, v147, s60, v170\n\t"
    "v_mfma_f32_16x16x32_bf16 v[8:11], a[200:203], v[128:131], 0\n\t"
    "v_exp_f32 v145, v145\n\t"
    "v_mfma_f32_16x16x32_bf16 v[12:15], a[204:207], v[128:131], 0\n\t"
    "ds_write_b128 v230, v[176:179] offset:20480\n\t"
    "s_waitcnt lgkmcnt(8)\n\t"
    "v_fma_f32 v148, v148, s60, v170\n\t"
    "v_fma_f32 v149, v149, s60, v170\n\t"
    "v_mfma_f32_16x16x32_bf16 v[16:19], a[208:211], v[128:131], 0\n\t"
    "v_exp_f32 v146, v146\n\t"
    "v_mfma_f32_16x16x32_bf16 v[20:23], a[212:215], v[128:131], 0\n\t"
    "s_waitcnt lgkmcnt(3)\n\t"
    "v_fma_f32 v150, v150, s60, v170\n\t"
    "v_fma_f32 v151, v151, s60, v170\n\t"
    "v_mfma_f32_16x16x32_bf16 v[24:27], a[216:219], v[128:131], 0\n\t"
    "ds_write_b128 v231, v[184:187] offset:20480\n\t"
    "s_waitcnt lgkmcnt(15)\n\t"
    "v_exp_f32 v147, v147\n\t"
    "v_mfma_f32_16x16x32_bf16 v[28:31], a[220:223], v[128:131], 0\n\t"
    "s_waitcnt vmcnt(10) lgkmcnt(4)\n\t"
    "v_cvt_pk_bf16_f32 v144, v144, v145\n\t"
    "v_cvt_pk_bf16_f32 v145, v146, v147\n\t"
    "v_mfma_f32_16x16x32_bf16 v[160:163], a[48:51], a[128:131], 0\n\t"
    "s_barrier\n\t"
    "v_exp_f32 v148, v148\n\t"
    "v_mfma_f32_16x16x32_bf16 v[160:163], a[52:55], a[132:135], v[160:163]\n\t"
    "ds_read_b64_tr_b16 a[32:33], v225 offset:8192\n\t"
    "v_mfma_f32_16x16x32_bf16 v[160:163], a[56:59], a[136:139], v[160:163]\n\t"
    "ds_write_b128 v230, v[192:195] offset:20608\n\t"
    "s_xor_b32 s64, s52, 32768\n\t"
    "v_exp_f32 v149, v149\n\t"
    "v_mfma_f32_16x16x32_bf16 v[160:163], a[60:63], a[140:143], v[160:163]\n\t"
    "ds_read_b64_tr_b16 a[34:35], v225 offset:12288\n\t"
    "v_cvt_pk_bf16_f32 v146, v148, v149\n\t"
    "v_mfma_f32_16x16x32_bf16 v[152:155], a[160:163], a[112:115], 0\n\t"
    "s_add_u32 m0, s63, 0\n\t"
    "v_exp_f32 v150, v150\n\t"
    "v_mfma_f32_16x16x32_bf16 v[152:155], a[164:167], a[116:119], v[152:155]\n\t"
    "ds_read_b64_tr_b16 a[36:37], v225 offset:8200\n\t"
    "v_mfma_f32_16x16x32_bf16 v[152:155], a[168:171], a[120:123], v[152:155]\n\t"
    "buffer_load_dwordx4 v226, s[36:39], s64 offen offset:0 lds\n\t"
    "v_mfma_f32_16x16x32_bf16 v[152:155], a[172:175], a[124:127], v[152:155]\n\t"
    "ds_read_b64_tr_b16 a[38:39], v225 offset:12296\n\t"
    "v_mfma_f32_16x16x32_bf16 v[156:159], a[176:179], a[112:115], 0\n\t"
    "v_exp_f32 v151, v151\n\t"
    "v_max3_f32 v160, v160, v161, v162\n\t"
    "v_max_f32 v160, v160, v163\n\t"
    "v_mfma_f32_16x16x32_bf16 v[156:159], a[180:183], a[116:119], v[156:159]\n\t"
    "ds_read_b64_tr_b16 a[40:41], v225 offset:8704\n\t"
    "v_cvt_pk_bf16_f32 v147, v150, v151\n\t"
    "v_mfma_f32_16x16x32_bf16 v[156:159], a[184:187], a[120:123], v[156:159]\n\t"
    "ds_write_b128 v231, v[200:203] offset:20608\n\t"
    "v_mfma_f32_16x16x32_bf16 v[156:159], a[188:191], a[124:127], v[156:159]\n\t"
    "ds_read_b64_tr_b16 a[42:43], v225 offset:12800\n\t"
    "v_mul_f32 v172, s61, v160\n\t"
    "v_mfma_f32_16x16x32_bf16 a[228:231], a[248:251], v[136:139], 0\n\t"
    "buffer_load_dwordx4 v226, s[36:39], s64 offen offset:2048 lds\n\t"
    "v_mfma_f32_16x16x32_bf16 v[32:35], a[192:195], v[136:139], 0\n\t"
    "ds_read_b64_tr_b16 a[44:45], v225 offset:8712\n\t"
    "v_fma_f32 v152, v152, s60, v171\n\t"
    "v_mfma_f32_16x16x32_bf16 v[36:39], a[196:199], v[136:139], 0\n\t"
    "v_exp_f32 v152, v152\n\t"
    "v_mfma_f32_16x16x32_bf16 v[40:43], a[200:203], v[136:139], 0\n\t"
    "ds_read_b64_tr_b16 a[46:47], v225 offset:12808\n\t"
    "v_fma_f32 v153, v153, s60, v171\n\t"
    "v_mfma_f32_16x16x32_bf16 v[44:47], a[204:207], v[136:139], 0\n\t"
    "v_exp_f32 v153, v153\n\t"
    "v_mfma_f32_16x16x32_bf16 v[48:51], a[208:211], v[136:139], 0\n\t"
    "s_add_u32 m0, s63, 32768\n\t"
    "v_fma_f32 v154, v154, s60, v171\n\t"
    "v_fma_f32 v155, v155, s60, v171\n\t"
    "v_mfma_f32_16x16x32_bf16 v[52:55], a[212:215], v[136:139], 0\n\t"
    "buffer_load_dwordx4 v226, s[40:43], s64 offen offset:0 lds\n\t"
    "v_mfma_f32_16x16x32_bf16 v[56:59], a[216:219], v[136:139], 0\n\t"
    "v_exp_f32 v154, v154\n\t"
    "v_mfma_f32_16x16x32_bf16 v[60:63], a[220:223], v[136:139], 0\n\t"
    "s_waitcnt lgkmcnt(15)\n\t"
    "v_fma_f32 v156, v156, s60, v171\n\t"
    "v_fma_f32 v157, v157, s60, v171\n\t"
    "v_mfma_f32_16x16x32_bf16 v[164:167], a[48:51], a[144:147], 0\n\t"
    "v_exp_f32 v155, v155\n\t"
    "v_mfma_f32_16x16x32_bf16 v[164:167], a[52:55], a[148:151], v[164:167]\n\t"
    "v_fma_f32 v158, v158, s60, v171\n\t"
    "v_fma_f32 v159, v159, s60, v171\n\t"
    "v_mfma_f32_16x16x32_bf16 v[164:167], a[56:59], a[152:155], v[164:167]\n\t"
    "buffer_load_dwordx4 v226, s[40:43], s64 offen offset:2048 lds\n\t"
    "v_mfma_f32_16x16x32_bf16 v[164:167], a[60:63], a[156:159], v[164:167]\n\t"
    "v_exp_f32 v156, v156\n\t"
    "v_mfma_f32_16x16x32_bf16 v[128:131], a[160:163], a[128:131], 0\n\t"
    "ds_read_b128 a[48:51], v232 offset:0\n\t"
    "v_cvt_pk_bf16_f32 v152, v152, v153\n\t"
    "v_mfma_f32_16x16x32_bf16 v[128:131], a[164:167], a[132:135], v[128:131]\n\t"
    "v_exp_f32 v157, v157\n\t"
    "v_mfma_f32_16x16x32_bf16 v[128:131], a[168:171], a[136:139], v[128:131]\n\t"
    "ds_read_b128 a[52:55], v232 offset:1024\n\t"
    "v_cvt_pk_bf16_f32 v153, v154, v155\n\t"
    "v_mfma_f32_16x16x32_bf16 v[128:131], a[172:175], a[140:143], v[128:131]\n\t"
    "v_exp_f32 v158, v158\n\t"
    "v_mfma_f32_16x16x32_bf16 v[132:135], a[176:179], a[128:131], 0\n\t"
    "ds_read_b128 a[56:59], v232 offset:2048\n\t"
    "v_cvt_pk_bf16_f32 v154, v156, v157\n\t"
    "v_max3_f32 v164, v164, v165, v166\n\t"
    "v_mfma_f32_16x16x32_bf16 v[132:135], a[180:183], a[132:135], v[132:135]\n\t"
    "v_exp_f32 v159, v159\n\t"
    "v_mfma_f32_16x16x32_bf16 v[132:135], a[184:187], a[136:139], v[132:135]\n\t"
    "ds_read_b128 a[60:63], v232 offset:3072\n\t"
    "v_cvt_pk_bf16_f32 v155, v158, v159\n\t"
    "v_mfma_f32_16x16x32_bf16 v[132:135], a[188:191], a[140:143], v[132:135]\n\t"
    "v_max_f32 v164, v164, v167\n\t"
    "v_mul_f32 v173, s61, v164\n\t"
    "v_mfma_f32_16x16x32_bf16 a[232:235], a[248:251], v[144:147], 0\n\t"
    "v_fma_f32 v128, v128, s60, v172\n\t"
    "v_fma_f32 v129, v129, s60, v172\n\t"
    "v_mfma_f32_16x16x32_bf16 v[64:67], a[192:195], v[144:147], 0\n\t"
    "v_exp_f32 v128, v128\n\t"
    "v_mfma_f32_16x16x32_bf16 v[68:71], a[196:199], v[144:147], 0\n\t"
    "v_fma_f32 v130, v130, s60, v172\n\t"
    "v_fma_f32 v131, v131, s60, v172\n\t"
    "v_mfma_f32_16x16x32_bf16 v[72:75], a[200:203], v[144:147], 0\n\t"
    "v_exp_f32 v129, v129\n\t"
    "v_mfma_f32_16x16x32_bf16 v[76:79], a[204:207], v[144:147], 0\n\t"
    "v_fma_f32 v132, v132, s60, v172\n\t"
    "v_fma_f32 v133, v133, s60, v172\n\t"
    "v_mfma_f32_16x16x32_bf16 v[80:83], a[208:211], v[144:147], 0\n\t"
    "v_exp_f32 v130, v130\n\t"
    "v_mfma_f32_16x16x32_bf16 v[84:87], a[212:215], v[144:147], 0\n\t"
    "v_exp_f32 v131, v131\n\t"
    "v_mfma_f32_16x16x32_bf16 v[88:91], a[216:219], v[144:147], 0\n\t"
    "v_exp_f32 v132, v132\n\t"
    "v_mfma_f32_16x16x32_bf16 v[92:95], a[220:223], v[144:147], 0\n\t"
    "v_exp_f32 v133, v133\n\t"
    "v_mfma_f32_16x16x32_bf16 v[136:139], a[160:163], a[144:147], 0\n\t"
    "ds_read_b128 a[160:163], v224 offset:8192\n\t"
    "v_fma_f32 v134, v134, s60, v172\n\t"
    "v_mfma_f32_16x16x32_bf16 v[136:139], a[164:167], a[148:151], v[136:139]\n\t"
    "s_waitcnt lgkmcnt(1)\n\t"
    "v_exp_f32 v134, v134\n\t"
    "v_mfma_f32_16x16x32_bf16 v[136:139], a[168:171], a[152:155], v[136:139]\n\t"
    "ds_read_b128 a[164:167], v224 offset:8704\n\t"
    "s_add_u32 s66, s54, 0\n\t"
    "v_mfma_f32_16x16x32_bf16 v[136:139], a[172:175], a[156:159], v[136:139]\n\t"
    "s_add_u32 s65, s53, 0\n\t"
    "v_fma_f32 v135, v135, s60, v172\n\t"
    "v_cvt_pk_bf16_f32 v128, v128, v129\n\t"
    "v_mfma_f32_16x16x32_bf16 v[140:143], a[176:179], a[144:147], 0\n\t"
    "ds_read_b128 a[168:171], v224 offset:9216\n\t"
    "v_cvt_pk_bf16_f32 v129, v130, v131\n\t"
    "v_mfma_f32_16x16x32_bf16 v[140:143], a[180:183], a[148:151], v[140:143]\n\t"
    "s_add_u32 m0, s69, 0\n\t"
    "v_exp_f32 v135, v135\n\t"
    "v_mfma_f32_16x16x32_bf16 v[140:143], a[184:187], a[152:155], v[140:143]\n\t"
    "ds_read_b128 a[172:175], v224 offset:9728\n\t"
    "v_cvt_pk_bf16_f32 v130, v132, v133\n\t"
    "v_mfma_f32_16x16x32_bf16 v[140:143], a[188:191], a[156:159], v[140:143]\n\t"
    "buffer_load_dwordx4 v226, s[44:47], s65 offen offset:0 nt lds\n\t"
    "v_mfma_f32_16x16x32_bf16 a[236:239], a[248:251], v[152:155], 0\n\t"
    "ds_read_b128 a[176:179], v224 offset:12288\n\t"
    "v_cvt_pk_bf16_f32 v131, v134, v135\n\t"
    "v_mfma_f32_16x16x32_bf16 v[96:99], a[192:195], v[152:155], 0\n\t"
    "buffer_load_dwordx4 v226, s[44:47], s65 offen offset:2048 nt lds\n\t"
    "v_mfma_f32_16x16x32_bf16 v[100:103], a[196:199], v[152:155], 0\n\t"
    "ds_read_b128 a[180:183], v224 offset:12800\n\t"
    "v_fma_f32 v136, v136, s60, v173\n\t"
    "v_mfma_f32_16x16x32_bf16 v[104:107], a[200:203], v[152:155], 0\n\t"
    "buffer_store_dwordx4 a[48:51], v227, s[48:51], s66 offen offset:0 nt\n\t"
    "v_mfma_f32_16x16x32_bf16 v[108:111], a[204:207], v[152:155], 0\n\t"
    "ds_read_b128 a[184:187], v224 offset:13312\n\t"
    "v_fma_f32 v137, v137, s60, v173\n\t"
    "v_mfma_f32_16x16x32_bf16 v[112:115], a[208:211], v[152:155], 0\n\t"
    "ds_read_b128 a[188:191], v224 offset:13824\n\t"
    "v_fma_f32 v138, v138, s60, v173\n\t"
    "v_mfma_f32_16x16x32_bf16 v[116:119], a[212:215], v[152:155], 0\n\t"
    "v_exp_f32 v136, v136\n\t"
    "v_mfma_f32_16x16x32_bf16 v[120:123], a[216:219], v[152:155], 0\n\t"
    "v_fma_f32 v139, v139, s60, v173\n\t"
    "v_fma_f32 v140, v140, s60, v173\n\t"
    "v_mfma_f32_16x16x32_bf16 v[124:127], a[220:223], v[152:155], 0\n\t"
    "s_waitcnt lgkmcnt(6)\n\t"
    "v_exp_f32 v137, v137\n\t"
    "v_mfma_f32_16x16x32_bf16 v[144:147], a[160:163], a[64:67], 0\n\t"
    "v_fma_f32 v141, v141, s60, v173\n\t"
    "v_fma_f32 v142, v142, s60, v173\n\t"
    "v_mfma_f32_16x16x32_bf16 v[144:147], a[164:167], a[68:71], v[144:147]\n\t"
    "s_waitcnt lgkmcnt(4)\n\t"
    "v_exp_f32 v138, v138\n\t"
    "v_mfma_f32_16x16x32_bf16 v[144:147], a[168:171], a[72:75], v[144:147]\n\t"
    "s_add_u32 s65, s53, 128\n\t"
    "v_fma_f32 v143, v143, s60, v173\n\t"
    "v_cvt_pk_bf16_f32 v136, v136, v137\n\t"
    "v_mfma_f32_16x16x32_bf16 v[144:147], a[172:175], a[76:79], v[144:147]\n\t"
    "s_waitcnt lgkmcnt(2)\n\t"
    "v_exp_f32 v139, v139\n\t"
    "v_mfma_f32_16x16x32_bf16 v[148:151], a[176:179], a[64:67], 0\n\t"
    "s_add_u32 m0, s69, 1024\n\t"
    "v_cvt_pk_bf16_f32 v137, v138, v139\n\t"
    "v_mfma_f32_16x16x32_bf16 v[148:151], a[180:183], a[68:71], v[148:151]\n\t"
    "s_waitcnt lgkmcnt(0)\n\t"
    "v_exp_f32 v140, v140\n\t"
    "v_mfma_f32_16x16x32_bf16 v[148:151], a[184:187], a[72:75], v[148:151]\n\t"
    "buffer_load_dwordx4 v226, s[44:47], s65 offen offset:0 nt lds\n\t"
    "v_mfma_f32_16x16x32_bf16 v[148:151], a[188:191], a[76:79], v[148:151]\n\t"
    "v_exp_f32 v141, v141\n\t"
    "v_mfma_f32_16x16x32_bf16 a[240:243], a[248:251], v[128:131], 0\n\t"
    "buffer_load_dwordx4 v226, s[44:47], s65 offen offset:2048 nt lds\n\t"
    "v_mfma_f32_16x16x32_bf16 v[208:211], a[192:195], v[128:131], 0\n\t"
    "v_exp_f32 v142, v142\n\t"
    "v_mfma_f32_16x16x32_bf16 v[212:215], a[196:199], v[128:131], 0\n\t"
    "v_cvt_pk_bf16_f32 v138, v140, v141\n\t"
    "v_fma_f32 v144, v144, s60, v168\n\t"
    "v_mfma_f32_16x16x32_bf16 v[216:219], a[200:203], v[128:131], 0\n\t"
    "v_exp_f32 v143, v143\n\t"
    "v_mfma_f32_16x16x32_bf16 v[220:223], a[204:207], v[128:131], 0\n\t"
    "v_cvt_pk_bf16_f32 v139, v142, v143\n\t"
    "v_fma_f32 v145, v145, s60, v168\n\t"
    "v_mfma_f32_16x16x32_bf16 v[240:243], a[208:211], v[128:131], 0\n\t"
    "v_exp_f32 v144, v144\n\t"
    "v_mfma_f32_16x16x32_bf16 v[244:247], a[212:215], v[128:131], 0\n\t"
    "v_fma_f32 v146, v146, s60, v168\n\t"
    "v_fma_f32 v147, v147, s60, v168\n\t"
    "v_mfma_f32_16x16x32_bf16 v[248:251], a[216:219], v[128:131], 0\n\t"
    "v_exp_f32 v145, v145\n\t"
    "v_mfma_f32_16x16x32_bf16 v[252:255], a[220:223], v[128:131], 0\n\t"
    "v_fma_f32 v148, v148, s60, v168\n\t"
    "v_fma_f32 v149, v149, s60, v168\n\t"
    "v_mfma_f32_16x16x32_bf16 v[152:155], a[160:163], a[80:83], 0\n\t"
    "v_exp_f32 v146, v146\n\t"
    "v_mfma_f32_16x16x32_bf16 v[152:155], a[164:167], a[84:87], v[152:155]\n\t"
    "v_exp_f32 v147, v147\n\t"
    "v_mfma_f32_16x16x32_bf16 v[152:155], a[168:171], a[88:91], v[152:155]\n\t"
    "v_exp_f32 v148, v148\n\t"
    "v_mfma_f32_16x16x32_bf16 v[152:155], a[172:175], a[92:95], v[152:155]\n\t"
    "v_exp_f32 v149, v149\n\t"
    "v_mfma_f32_16x16x32_bf16 v[156:159], a[176:179], a[80:83], 0\n\t"
    "v_fma_f32 v150, v150, s60, v168\n\t"
    "v_fma_f32 v151, v151, s60, v168\n\t"
    "v_mfma_f32_16x16x32_bf16 v[156:159], a[180:183], a[84:87], v[156:159]\n\t"
    "v_exp_f32 v150, v150\n\t"
    "v_mfma_f32_16x16x32_bf16 v[156:159], a[184:187], a[88:91], v[156:159]\n\t"
    "v_exp_f32 v151, v151\n\t"
    "v_mfma_f32_16x16x32_bf16 v[156:159], a[188:191], a[92:95], v[156:159]\n\t"
    "v_cvt_pk_bf16_f32 v144, v144, v145\n\t"
    "v_cvt_pk_bf16_f32 v145, v146, v147\n\t"
    "v_mfma_f32_16x16x32_bf16 a[244:247], a[248:251], v[136:139], 0\n\t"
    "v_cvt_pk_bf16_f32 v146, v148, v149\n\t"
    "v_cvt_pk_bf16_f32 v147, v150, v151\n\t"
    "v_mfma_f32_16x16x32_bf16 v[176:179], a[192:195], v[136:139], 0\n\t"
    "v_fma_f32 v152, v152, s60, v169\n\t"
    "v_fma_f32 v153, v153, s60, v169\n\t"
    "v_mfma_f32_16x16x32_bf16 v[180:183], a[196:199], v[136:139], 0\n\t"
    "v_exp_f32 v152, v152\n\t"
    "v_mfma_f32_16x16x32_bf16 v[184:187], a[200:203], v[136:139], 0\n\t"
    "v_fma_f32 v154, v154, s60, v169\n\t"
    "v_fma_f32 v155, v155, s60, v169\n\t"
    "v_mfma_f32_16x16x32_bf16 v[188:191], a[204:207], v[136:139], 0\n\t"
    "v_exp_f32 v153, v153\n\t"
    "v_mfma_f32_16x16x32_bf16 v[192:195], a[208:211], v[136:139], 0\n\t"
    "ds_read_b64_tr_b16 a[208:209], v225 offset:9216\n\t"
    "v_fma_f32 v156, v156, s60, v169\n\t"
    "v_mfma_f32_16x16x32_bf16 v[196:199], a[212:215], v[136:139], 0\n\t"
    "v_exp_f32 v154, v154\n\t"
    "v_mfma_f32_16x16x32_bf16 v[200:203], a[216:219], v[136:139], 0\n\t"
    "ds_read_b64_tr_b16 a[210:211], v225 offset:13312\n\t"
    "v_fma_f32 v157, v157, s60, v169\n\t"
    "v_mfma_f32_16x16x32_bf16 v[204:207], a[220:223], v[136:139], 0\n\t"
    "v_exp_f32 v155, v155\n\t"
    "v_mfma_f32_16x16x32_bf16 v[128:131], a[160:163], a[96:99], 0\n\t"
    "ds_read_b64_tr_b16 a[212:213], v225 offset:9224\n\t"
    "v_fma_f32 v158, v158, s60, v169\n\t"
    "v_mfma_f32_16x16x32_bf16 v[128:131], a[164:167], a[100:103], v[128:131]\n\t"
    "v_exp_f32 v156, v156\n\t"
    "v_mfma_f32_16x16x32_bf16 v[128:131], a[168:171], a[104:107], v[128:131]\n\t"
    "ds_read_b64_tr_b16 a[214:215], v225 offset:13320\n\t"
    "v_fma_f32 v159, v159, s60, v169\n\t"
    "v_mfma_f32_16x16x32_bf16 v[128:131], a[172:175], a[108:111], v[128:131]\n\t"
    "ds_read_b64_tr_b16 a[216:217], v225 offset:9728\n\t"
    "v_cvt_pk_bf16_f32 v152, v152, v153\n\t"
    "v_mfma_f32_16x16x32_bf16 v[132:135], a[176:179], a[96:99], 0\n\t"
    "ds_read_b64_tr_b16 a[218:219], v225 offset:13824\n\t"
    "v_cvt_pk_bf16_f32 v153, v154, v155\n\t"
    "v_mfma_f32_16x16x32_bf16 v[132:135], a[180:183], a[100:103], v[132:135]\n\t"
    "ds_read_b64_tr_b16 a[220:221], v225 offset:9736\n\t"
    "v_mfma_f32_16x16x32_bf16 v[132:135], a[184:187], a[104:107], v[132:135]\n\t"
    "ds_read_b64_tr_b16 a[222:223], v225 offset:13832\n\t"
    "v_mfma_f32_16x16x32_bf16 v[132:135], a[188:191], a[108:111], v[132:135]\n\t"
    "v_exp_f32 v157, v157\n\t"
    "v_mfma_f32_16x16x32_bf16 a[224:227], a[248:251], v[144:147], a[224:227]\n\t"
    "s_waitcnt lgkmcnt(15)\n\t"
    "v_exp_f32 v158, v158\n\t"
    "v_mfma_f32_16x16x32_bf16 v[0:3], a[32:35], v[144:147], v[0:3]\n\t"
    "v_exp_f32 v159, v159\n\t"
    "v_mfma_f32_16x16x32_bf16 v[4:7], a[36:39], v[144:147], v[4:7]\n\t"
    "buffer_store_dwordx4 a[52:55], v228, s[48:51], s66 offen offset:1024 nt\n\t"
    "v_mfma_f32_16x16x32_bf16 v[8:11], a[40:43], v[144:147], v[8:11]\n\t"
    "v_cvt_pk_bf16_f32 v154, v156, v157\n\t"
    "v_cvt_pk_bf16_f32 v155, v158, v159\n\t"
    "v_mfma_f32_16x16x32_bf16 v[12:15], a[44:47], v[144:147], v[12:15]\n\t"
    "s_waitcnt lgkmcnt(4)\n\t"
    "v_fma_f32 v128, v128, s60, v170\n\t"
    "v_fma_f32 v129, v129, s60, v170\n\t"
    "v_mfma_f32_16x16x32_bf16 v[16:19], a[208:211], v[144:147], v[16:19]\n\t"
    "v_exp_f32 v128, v128\n\t"
    "v_mfma_f32_16x16x32_bf16 v[20:23], a[212:215], v[144:147], v[20:23]\n\t"
    "s_waitcnt lgkmcnt(0)\n\t"
    "v_exp_f32 v129, v129\n\t"
    "v_mfma_f32_16x16x32_bf16 v[24:27], a[216:219], v[144:147], v[24:27]\n\t"
    "v_fma_f32 v130, v130, s60, v170\n\t"
    "v_fma_f32 v131, v131, s60, v170\n\t"
    "v_mfma_f32_16x16x32_bf16 v[28:31], a[220:223], v[144:147], v[28:31]\n\t"
    "v_exp_f32 v130, v130\n\t"
    "v_mfma_f32_16x16x32_bf16 v[136:139], a[160:163], a[112:115], 0\n\t"
    "s_waitcnt vmcnt(14) lgkmcnt(0)\n\t"
    "v_exp_f32 v131, v131\n\t"
    "v_mfma_f32_16x16x32_bf16 v[136:139], a[164:167], a[116:119], v[136:139]\n\t"
    "s_barrier\n\t"
    "v_fma_f32 v132, v132, s60, v170\n\t"
    "v_fma_f32 v133, v133, s60, v170\n\t"
    "v_mfma_f32_16x16x32_bf16 v[136:139], a[168:171], a[120:123], v[136:139]\n\t"
    "ds_read_b64_tr_b16 a[192:193], v225 offset:16384\n\t"
    "v_fma_f32 v134, v134, s60, v170\n\t"
    "v_mfma_f32_16x16x32_bf16 v[136:139], a[172:175], a[124:127], v[136:139]\n\t"
    "s_xor_b32 s64, s52, 40960\n\t"
    "v_exp_f32 v132, v132\n\t"
    "v_mfma_f32_16x16x32_bf16 v[140:143], a[176:179], a[112:115], 0\n\t"
    "ds_read_b64_tr_b16 a[194:195], v225 offset:20480\n\t"
    "v_fma_f32 v135, v135, s60, v170\n\t"
    "v_mfma_f32_16x16x32_bf16 v[140:143], a[180:183], a[116:119], v[140:143]\n\t"
    "s_add_u32 m0, s63, 8192\n\t"
    "v_exp_f32 v133, v133\n\t"
    "v_mfma_f32_16x16x32_bf16 v[140:143], a[184:187], a[120:123], v[140:143]\n\t"
    "ds_read_b64_tr_b16 a[196:197], v225 offset:16392\n\t"
    "v_cvt_pk_bf16_f32 v128, v128, v129\n\t"
    "v_mfma_f32_16x16x32_bf16 v[140:143], a[188:191], a[124:127], v[140:143]\n\t"
    "buffer_load_dwordx4 v226, s[36:39], s64 offen offset:0 lds\n\t"
    "v_mfma_f32_16x16x32_bf16 a[228:231], a[248:251], v[152:155], a[228:231]\n\t"
    "ds_read_b64_tr_b16 a[198:199], v225 offset:20488\n\t"
    "v_cvt_pk_bf16_f32 v129, v130, v131\n\t"
    "v_mfma_f32_16x16x32_bf16 v[32:35], a[32:35], v[152:155], v[32:35]\n\t"
    "v_exp_f32 v134, v134\n\t"
    "v_mfma_f32_16x16x32_bf16 v[36:39], a[36:39], v[152:155], v[36:39]\n\t"
    "ds_read_b64_tr_b16 a[200:201], v225 offset:16896\n\t"
    "v_cvt_pk_bf16_f32 v130, v132, v133\n\t"
    "v_mfma_f32_16x16x32_bf16 v[40:43], a[40:43], v[152:155], v[40:43]\n\t"
    "v_exp_f32 v135, v135\n\t"
    "v_mfma_f32_16x16x32_bf16 v[44:47], a[44:47], v[152:155], v[44:47]\n\t"
    "ds_read_b64_tr_b16 a[202:203], v225 offset:20992\n\t"
    "v_cvt_pk_bf16_f32 v131, v134, v135\n\t"
    "v_mfma_f32_16x16x32_bf16 v[48:51], a[208:211], v[152:155], v[48:51]\n\t"
    "buffer_load_dwordx4 v226, s[36:39], s64 offen offset:2048 lds\n\t"
    "v_mfma_f32_16x16x32_bf16 v[52:55], a[212:215], v[152:155], v[52:55]\n\t"
    "ds_read_b64_tr_b16 a[204:205], v225 offset:16904\n\t"
    "v_fma_f32 v136, v136, s60, v171\n\t"
    "v_mfma_f32_16x16x32_bf16 v[56:59], a[216:219], v[152:155], v[56:59]\n\t"
    "v_exp_f32 v136, v136\n\t"
    "v_mfma_f32_16x16x32_bf16 v[60:63], a[220:223], v[152:155], v[60:63]\n\t"
    "ds_read_b64_tr_b16 a[206:207], v225 offset:21000\n\t"
    "v_fma_f32 v137, v137, s60, v171\n\t"
    "v_mfma_f32_16x16x32_bf16 v[144:147], a[160:163], a[128:131], 0\n\t"
    "v_exp_f32 v137, v137\n\t"
    "v_mfma_f32_16x16x32_bf16 v[144:147], a[164:167], a[132:135], v[144:147]\n\t"
    "s_add_u32 m0, s63, 40960\n\t"
    "v_fma_f32 v138, v138, s60, v171\n\t"
    "v_fma_f32 v139, v139, s60, v171\n\t"
    "v_mfma_f32_16x16x32_bf16 v[144:147], a[168:171], a[136:139], v[144:147]\n\t"
    "buffer_load_dwordx4 v226, s[40:43], s64 offen offset:0 lds\n\t"
    "v_mfma_f32_16x16x32_bf16 v[144:147], a[172:175], a[140:143], v[144:147]\n\t"
    "v_exp_f32 v138, v138\n\t"
    "v_mfma_f32_16x16x32_bf16 v[148:151], a[176:179], a[128:131], 0\n\t"
    "v_exp_f32 v139, v139\n\t"
    "v_mfma_f32_16x16x32_bf16 v[148:151], a[180:183], a[132:135], v[148:151]\n\t"
    "v_fma_f32 v140, v140, s60, v171\n\t"
    "v_fma_f32 v141, v141, s60, v171\n\t"
    "v_mfma_f32_16x16x32_bf16 v[148:151], a[184:187], a[136:139], v[148:151]\n\t"
    "v_exp_f32 v140, v140\n\t"
    "v_mfma_f32_16x16x32_bf16 v[148:151], a[188:191], a[140:143], v[148:151]\n\t"
    "buffer_load_dwordx4 v226, s[40:43], s64 offen offset:2048 lds\n\t"
    "v_mfma_f32_16x16x32_bf16 a[232:235], a[248:251], v[128:131], a[232:235]\n\t"
    "v_exp_f32 v141, v141\n\t"
    "v_mfma_f32_16x16x32_bf16 v[64:67], a[32:35], v[128:131], v[64:67]\n\t"
    "v_fma_f32 v142, v142, s60, v171\n\t"
    "v_fma_f32 v143, v143, s60, v171\n\t"
    "v_mfma_f32_16x16x32_bf16 v[68:71], a[36:39], v[128:131], v[68:71]\n\t"
    "v_exp_f32 v142, v142\n\t"
    "v_mfma_f32_16x16x32_bf16 v[72:75], a[40:43], v[128:131], v[72:75]\n\t"
    "v_exp_f32 v143, v143\n\t"
    "v_mfma_f32_16x16x32_bf16 v[76:79], a[44:47], v[128:131], v[76:79]\n\t"
    "v_cvt_pk_bf16_f32 v136, v136, v137\n\t"
    "v_cvt_pk_bf16_f32 v137, v138, v139\n\t"
    "v_mfma_f32_16x16x32_bf16 v[80:83], a[208:211], v[128:131], v[80:83]\n\t"
    "v_cvt_pk_bf16_f32 v138, v140, v141\n\t"
    "v_cvt_pk_bf16_f32 v139, v142, v143\n\t"
    "v_mfma_f32_16x16x32_bf16 v[84:87], a[212:215], v[128:131], v[84:87]\n\t"
    "v_fma_f32 v144, v144, s60, v172\n\t"
    "v_fma_f32 v145, v145, s60, v172\n\t"
    "v_mfma_f32_16x16x32_bf16 v[88:91], a[216:219], v[128:131], v[88:91]\n\t"
    "v_exp_f32 v144, v144\n\t"
    "v_mfma_f32_16x16x32_bf16 v[92:95], a[220:223], v[128:131], v[92:95]\n\t"
    "v_exp_f32 v145, v145\n\t"
    "v_mfma_f32_16x16x32_bf16 v[152:155], a[160:163], a[144:147], 0\n\t"
    "ds_read_b128 a[160:163], v224 offset:16384\n\t"
    "v_fma_f32 v146, v146, s60, v172\n\t"
    "v_mfma_f32_16x16x32_bf16 v[152:155], a[164:167], a[148:151], v[152:155]\n\t"
    "v_exp_f32 v146, v146\n\t"
    "v_mfma_f32_16x16x32_bf16 v[152:155], a[168:171], a[152:155], v[152:155]\n\t"
    "ds_read_b128 a[164:167], v224 offset:16896\n\t"
    "v_fma_f32 v147, v147, s60, v172\n\t"
    "v_mfma_f32_16x16x32_bf16 v[152:155], a[172:175], a[156:159], v[152:155]\n\t"
    "v_exp_f32 v147, v147\n\t"
    "v_mfma_f32_16x16x32_bf16 v[156:159], a[176:179], a[144:147], 0\n\t"
    "ds_read_b128 a[168:171], v224 offset:17408\n\t"
    "v_fma_f32 v148, v148, s60, v172\n\t"
    "v_mfma_f32_16x16x32_bf16 v[156:159], a[180:183], a[148:151], v[156:159]\n\t"
    "v_exp_f32 v148, v148\n\t"
    "v_mfma_f32_16x16x32_bf16 v[156:159], a[184:187], a[152:155], v[156:159]\n\t"
    "ds_read_b128 a[172:175], v224 offset:17920\n\t"
    "v_fma_f32 v149, v149, s60, v172\n\t"
    "v_mfma_f32_16x16x32_bf16 v[156:159], a[188:191], a[156:159], v[156:159]\n\t"
    "v_exp_f32 v149, v149\n\t"
    "v_mfma_f32_16x16x32_bf16 a[236:239], a[248:251], v[136:139], a[236:239]\n\t"
    "ds_read_b128 a[176:179], v224 offset:20480\n\t"
    "v_fma_f32 v150, v150, s60, v172\n\t"
    "v_mfma_f32_16x16x32_bf16 v[96:99], a[32:35], v[136:139], v[96:99]\n\t"
    "v_exp_f32 v150, v150\n\t"
    "v_mfma_f32_16x16x32_bf16 v[100:103], a[36:39], v[136:139], v[100:103]\n\t"
    "ds_read_b128 a[180:183], v224 offset:20992\n\t"
    "v_fma_f32 v151, v151, s60, v172\n\t"
    "v_mfma_f32_16x16x32_bf16 v[104:107], a[40:43], v[136:139], v[104:107]\n\t"
    "v_exp_f32 v151, v151\n\t"
    "v_mfma_f32_16x16x32_bf16 v[108:111], a[44:47], v[136:139], v[108:111]\n\t"
    "ds_read_b128 a[184:187], v224 offset:21504\n\t"
    "v_cvt_pk_bf16_f32 v144, v144, v145\n\t"
    "v_mfma_f32_16x16x32_bf16 v[112:115], a[208:211], v[136:139], v[112:115]\n\t"
    "ds_read_b128 a[188:191], v224 offset:22016\n\t"
    "v_cvt_pk_bf16_f32 v145, v146, v147\n\t"
    "v_mfma_f32_16x16x32_bf16 v[116:119], a[212:215], v[136:139], v[116:119]\n\t"
    "v_cvt_pk_bf16_f32 v146, v148, v149\n\t"
    "v_cvt_pk_bf16_f32 v147, v150, v151\n\t"
    "v_mfma_f32_16x16x32_bf16 v[120:123], a[216:219], v[136:139], v[120:123]\n\t"
    "v_fma_f32 v152, v152, s60, v173\n\t"
    "v_fma_f32 v153, v153, s60, v173\n\t"
    "v_mfma_f32_16x16x32_bf16 v[124:127], a[220:223], v[136:139], v[124:127]\n\t"
    "s_waitcnt lgkmcnt(6)\n\t"
    "v_exp_f32 v152, v152\n\t"
    "v_mfma_f32_16x16x32_bf16 v[128:131], a[160:163], a[64:67], 0\n\t"
    "buffer_store_dwordx4 a[56:59], v227, s[48:51], s66 offen offset:2048 nt\n\t"
    "v_mfma_f32_16x16x32_bf16 v[128:131], a[164:167], a[68:71], v[128:131]\n\t"
    "s_waitcnt lgkmcnt(4)\n\t"
    "v_exp_f32 v153, v153\n\t"
    "v_mfma_f32_16x16x32_bf16 v[128:131], a[168:171], a[72:75], v[128:131]\n\t"
    "v_fma_f32 v154, v154, s60, v173\n\t"
    "v_fma_f32 v155, v155, s60, v173\n\t"
    "v_mfma_f32_16x16x32_bf16 v[128:131], a[172:175], a[76:79], v[128:131]\n\t"
    "s_waitcnt lgkmcnt(2)\n\t"
    "v_exp_f32 v154, v154\n\t"
    "v_mfma_f32_16x16x32_bf16 v[132:135], a[176:179], a[64:67], 0\n\t"
    "v_fma_f32 v156, v156, s60, v173\n\t"
    "v_fma_f32 v157, v157, s60, v173\n\t"
    "v_mfma_f32_16x16x32_bf16 v[132:135], a[180:183], a[68:71], v[132:135]\n\t"
    "s_waitcnt lgkmcnt(0)\n\t"
    "v_exp_f32 v155, v155\n\t"
    "v_mfma_f32_16x16x32_bf16 v[132:135], a[184:187], a[72:75], v[132:135]\n\t"
    "v_fma_f32 v158, v158, s60, v173\n\t"
    "v_fma_f32 v159, v159, s60, v173\n\t"
    "v_mfma_f32_16x16x32_bf16 v[132:135], a[188:191], a[76:79], v[132:135]\n\t"
    "v_exp_f32 v156, v156\n\t"
    "v_mfma_f32_16x16x32_bf16 a[240:243], a[248:251], v[144:147], a[240:243]\n\t"
    "v_cvt_pk_bf16_f32 v152, v152, v153\n\t"
    "v_cvt_pk_bf16_f32 v153, v154, v155\n\t"
    "v_mfma_f32_16x16x32_bf16 v[208:211], a[32:35], v[144:147], v[208:211]\n\t"
    "v_exp_f32 v157, v157\n\t"
    "v_mfma_f32_16x16x32_bf16 v[212:215], a[36:39], v[144:147], v[212:215]\n\t"
    "v_exp_f32 v158, v158\n\t"
    "v_mfma_f32_16x16x32_bf16 v[216:219], a[40:43], v[144:147], v[216:219]\n\t"
    "v_exp_f32 v159, v159\n\t"
    "v_mfma_f32_16x16x32_bf16 v[220:223], a[44:47], v[144:147], v[220:223]\n\t"
    "v_cvt_pk_bf16_f32 v154, v156, v157\n\t"
    "v_cvt_pk_bf16_f32 v155, v158, v159\n\t"
    "v_mfma_f32_16x16x32_bf16 v[240:243], a[208:211], v[144:147], v[240:243]\n\t"
    "v_fma_f32 v128, v128, s60, v168\n\t"
    "v_fma_f32 v129, v129, s60, v168\n\t"
    "v_mfma_f32_16x16x32_bf16 v[244:247], a[212:215], v[144:147], v[244:247]\n\t"
    "v_exp_f32 v128, v128\n\t"
    "v_mfma_f32_16x16x32_bf16 v[248:251], a[216:219], v[144:147], v[248:251]\n\t"
    "v_exp_f32 v129, v129\n\t"
    "v_mfma_f32_16x16x32_bf16 v[252:255], a[220:223], v[144:147], v[252:255]\n\t"
    "v_fma_f32 v130, v130, s60, v168\n\t"
    "v_fma_f32 v131, v131, s60, v168\n\t"
    "v_mfma_f32_16x16x32_bf16 v[136:139], a[160:163], a[80:83], 0\n\t"
    "v_exp_f32 v130, v130\n\t"
    "v_mfma_f32_16x16x32_bf16 v[136:139], a[164:167], a[84:87], v[136:139]\n\t"
    "v_exp_f32 v131, v131\n\t"
    "v_mfma_f32_16x16x32_bf16 v[136:139], a[168:171], a[88:91], v[136:139]\n\t"
    "v_fma_f32 v132, v132, s60, v168\n\t"
    "v_fma_f32 v133, v133, s60, v168\n\t"
    "v_mfma_f32_16x16x32_bf16 v[136:139], a[172:175], a[92:95], v[136:139]\n\t"
    "v_exp_f32 v132, v132\n\t"
    "v_mfma_f32_16x16x32_bf16 v[140:143], a[176:179], a[80:83], 0\n\t"
    "v_exp_f32 v133, v133\n\t"
    "v_mfma_f32_16x16x32_bf16 v[140:143], a[180:183], a[84:87], v[140:143]\n\t"
    "v_fma_f32 v134, v134, s60, v168\n\t"
    "v_fma_f32 v135, v135, s60, v168\n\t"
    "v_mfma_f32_16x16x32_bf16 v[140:143], a[184:187], a[88:91], v[140:143]\n\t"
    "v_exp_f32 v134, v134\n\t"
    "v_mfma_f32_16x16x32_bf16 v[140:143], a[188:191], a[92:95], v[140:143]\n\t"
    "v_exp_f32 v135, v135\n\t"
    "v_mfma_f32_16x16x32_bf16 a[244:247], a[248:251], v[152:155], a[244:247]\n\t"
    "v_cvt_pk_bf16_f32 v128, v128, v129\n\t"
    "v_cvt_pk_bf16_f32 v129, v130, v131\n\t"
    "v_mfma_f32_16x16x32_bf16 v[176:179], a[32:35], v[152:155], v[176:179]\n\t"
    "v_cvt_pk_bf16_f32 v130, v132, v133\n\t"
    "v_cvt_pk_bf16_f32 v131, v134, v135\n\t"
    "v_mfma_f32_16x16x32_bf16 v[180:183], a[36:39], v[152:155], v[180:183]\n\t"
    "v_fma_f32 v136, v136, s60, v169\n\t"
    "v_fma_f32 v137, v137, s60, v169\n\t"
    "v_mfma_f32_16x16x32_bf16 v[184:187], a[40:43], v[152:155], v[184:187]\n\t"
    "v_exp_f32 v136, v136\n\t"
    "v_mfma_f32_16x16x32_bf16 v[188:191], a[44:47], v[152:155], v[188:191]\n\t"
    "v_fma_f32 v138, v138, s60, v169\n\t"
    "v_fma_f32 v139, v139, s60, v169\n\t"
    "v_mfma_f32_16x16x32_bf16 v[192:195], a[208:211], v[152:155], v[192:195]\n\t"
    "ds_read_b64_tr_b16 a[208:209], v225 offset:17408\n\t"
    "v_fma_f32 v140, v140, s60, v169\n\t"
    "v_mfma_f32_16x16x32_bf16 v[196:199], a[212:215], v[152:155], v[196:199]\n\t"
    "v_exp_f32 v137, v137\n\t"
    "v_mfma_f32_16x16x32_bf16 v[200:203], a[216:219], v[152:155], v[200:203]\n\t"
    "ds_read_b64_tr_b16 a[210:211], v225 offset:21504\n\t"
    "v_fma_f32 v141, v141, s60, v169\n\t"
    "v_mfma_f32_16x16x32_bf16 v[204:207], a[220:223], v[152:155], v[204:207]\n\t"
    "v_exp_f32 v138, v138\n\t"
    "v_mfma_f32_16x16x32_bf16 v[144:147], a[160:163], a[96:99], 0\n\t"
    "ds_read_b64_tr_b16 a[212:213], v225 offset:17416\n\t"
    "v_and_b32 v239, 0x30, v227\n\t"
    "v_mfma_f32_16x16x32_bf16 v[144:147], a[164:167], a[100:103], v[144:147]\n\t"
    "v_bfe_u32 v238, v227, 8, 2\n\t"
    "v_lshl_add_u32 v239, v239, 2, %14\n\t"
    "v_mfma_f32_16x16x32_bf16 v[144:147], a[168:171], a[104:107], v[144:147]\n\t"
    "ds_read_b64_tr_b16 a[214:215], v225 offset:21512\n\t"
    "v_lshl_add_u32 v239, v238, 4, v239\n\t"
    "v_mfma_f32_16x16x32_bf16 v[144:147], a[172:175], a[108:111], v[144:147]\n\t"
    "ds_read_b64_tr_b16 a[216:217], v225 offset:17920\n\t"
    "v_fma_f32 v142, v142, s60, v169\n\t"
    "v_mfma_f32_16x16x32_bf16 v[148:151], a[176:179], a[96:99], 0\n\t"
    "ds_read_b64_tr_b16 a[218:219], v225 offset:22016\n\t"
    "v_fma_f32 v143, v143, s60, v169\n\t"
    "v_mfma_f32_16x16x32_bf16 v[148:151], a[180:183], a[100:103], v[148:151]\n\t"
    "ds_read_b64_tr_b16 a[220:221], v225 offset:17928\n\t"
    "v_cvt_pk_bf16_f32 v136, v136, v137\n\t"
    "v_mfma_f32_16x16x32_bf16 v[148:151], a[184:187], a[104:107], v[148:151]\n\t"
    "ds_read_b64_tr_b16 a[222:223], v225 offset:22024\n\t"
    "v_mfma_f32_16x16x32_bf16 v[148:151], a[188:191], a[108:111], v[148:151]\n\t"
    "v_exp_f32 v139, v139\n\t"
    "v_mfma_f32_16x16x32_bf16 a[224:227], a[248:251], v[128:131], a[224:227]\n\t"
    "s_waitcnt lgkmcnt(15)\n\t"
    "v_exp_f32 v140, v140\n\t"
    "v_mfma_f32_16x16x32_bf16 v[0:3], a[192:195], v[128:131], v[0:3]\n\t"
    "v_exp_f32 v141, v141\n\t"
    "v_mfma_f32_16x16x32_bf16 v[4:7], a[196:199], v[128:131], v[4:7]\n\t"
    "v_exp_f32 v142, v142\n\t"
    "v_mfma_f32_16x16x32_bf16 v[8:11], a[200:203], v[128:131], v[8:11]\n\t"
    "v_exp_f32 v143, v143\n\t"
    "v_mfma_f32_16x16x32_bf16 v[12:15], a[204:207], v[128:131], v[12:15]\n\t"
    "s_waitcnt lgkmcnt(4)\n\t"
    "v_cvt_pk_bf16_f32 v137, v138, v139\n\t"
    "v_cvt_pk_bf16_f32 v138, v140, v141\n\t"
    "v_mfma_f32_16x16x32_bf16 v[16:19], a[208:211], v[128:131], v[16:19]\n\t"
    "v_cvt_pk_bf16_f32 v139, v142, v143\n\t"
    "v_fma_f32 v144, v144, s60, v170\n\t"
    "v_mfma_f32_16x16x32_bf16 v[20:23], a[212:215], v[128:131], v[20:23]\n\t"
    "s_waitcnt lgkmcnt(0)\n\t"
    "v_exp_f32 v144, v144\n\t"
    "v_mfma_f32_16x16x32_bf16 v[24:27], a[216:219], v[128:131], v[24:27]\n\t"
    "v_fma_f32 v145, v145, s60, v170\n\t"
    "v_fma_f32 v146, v146, s60, v170\n\t"
    "v_mfma_f32_16x16x32_bf16 v[28:31], a[220:223], v[128:131], v[28:31]\n\t"
    "v_exp_f32 v145, v145\n\t"
    "v_mfma_f32_16x16x32_bf16 v[152:155], a[160:163], a[112:115], 0\n\t"
    "s_waitcnt vmcnt(15) lgkmcnt(0)\n\t"
    "v_exp_f32 v146, v146\n\t"
    "v_mfma_f32_16x16x32_bf16 v[152:155], a[164:167], a[116:119], v[152:155]\n\t"
    "s_barrier\n\t"
    "v_fma_f32 v147, v147, s60, v170\n\t"
    "v_fma_f32 v148, v148, s60, v170\n\t"
    "v_mfma_f32_16x16x32_bf16 v[152:155], a[168:171], a[120:123], v[152:155]\n\t"
    "ds_read_b64_tr_b16 a[32:33], v225 offset:24576\n\t"
    "v_fma_f32 v149, v149, s60, v170\n\t"
    "v_mfma_f32_16x16x32_bf16 v[152:155], a[172:175], a[124:127], v[152:155]\n\t"
    "s_xor_b32 s64, s52, 49152\n\t"
    "v_exp_f32 v147, v147\n\t"
    "v_mfma_f32_16x16x32_bf16 v[156:159], a[176:179], a[112:115], 0\n\t"
    "ds_read_b64_tr_b16 a[34:35], v225 offset:28672\n\t"
    "v_fma_f32 v150, v150, s60, v170\n\t"
    "v_mfma_f32_16x16x32_bf16 v[156:159], a[180:183], a[116:119], v[156:159]\n\t"
    "s_add_u32 m0, s63, 16384\n\t"
    "v_exp_f32 v148, v148\n\t"
    "v_mfma_f32_16x16x32_bf16 v[156:159], a[184:187], a[120:123], v[156:159]\n\t"
    "ds_read_b64_tr_b16 a[36:37], v225 offset:24584\n\t"
    "v_fma_f32 v151, v151, s60, v170\n\t"
    "v_mfma_f32_16x16x32_bf16 v[156:159], a[188:191], a[124:127], v[156:159]\n\t"
    "buffer_load_dwordx4 v226, s[36:39], s64 offen offset:0 lds\n\t"
    "v_mfma_f32_16x16x32_bf16 a[228:231], a[248:251], v[136:139], a[228:231]\n\t"
    "ds_read_b64_tr_b16 a[38:39], v225 offset:28680\n\t"
    "v_cvt_pk_bf16_f32 v144, v144, v145\n\t"
    "v_mfma_f32_16x16x32_bf16 v[32:35], a[192:195], v[136:139], v[32:35]\n\t"
    "buffer_store_dwordx4 a[60:63], v228, s[48:51], s66 offen offset:3072 nt\n\t"
    "v_mfma_f32_16x16x32_bf16 v[36:39], a[196:199], v[136:139], v[36:39]\n\t"
    "ds_read_b64_tr_b16 a[40:41], v225 offset:25088\n\t"
    "v_cvt_pk_bf16_f32 v145, v146, v147\n\t"
    "v_mfma_f32_16x16x32_bf16 v[40:43], a[200:203], v[136:139], v[40:43]\n\t"
    "v_exp_f32 v149, v149\n\t"
    "v_mfma_f32_16x16x32_bf16 v[44:47], a[204:207], v[136:139], v[44:47]\n\t"
    "ds_read_b64_tr_b16 a[42:43], v225 offset:29184\n\t"
    "v_cvt_pk_bf16_f32 v146, v148, v149\n\t"
    "v_mfma_f32_16x16x32_bf16 v[48:51], a[208:211], v[136:139], v[48:51]\n\t"
    "buffer_load_dwordx4 v226, s[36:39], s64 offen offset:2048 lds\n\t"
    "v_mfma_f32_16x16x32_bf16 v[52:55], a[212:215], v[136:139], v[52:55]\n\t"
    "ds_read_b64_tr_b16 a[44:45], v225 offset:25096\n\t"
    "v_fma_f32 v152, v152, s60, v171\n\t"
    "v_mfma_f32_16x16x32_bf16 v[56:59], a[216:219], v[136:139], v[56:59]\n\t"
    "v_exp_f32 v150, v150\n\t"
    "v_mfma_f32_16x16x32_bf16 v[60:63], a[220:223], v[136:139], v[60:63]\n\t"
    "ds_read_b64_tr_b16 a[46:47], v225 offset:29192\n\t"
    "v_fma_f32 v153, v153, s60, v171\n\t"
    ".Lent%=:\n\t"
    "v_mfma_f32_16x16x32_bf16 v[128:131], a[160:163], a[128:131], 0\n\t"
    "v_exp_f32 v151, v151\n\t"
    "v_mfma_f32_16x16x32_bf16 v[128:131], a[164:167], a[132:135], v[128:131]\n\t"
    "s_add_u32 m0, s63, 49152\n\t"
    "v_exp_f32 v152, v152\n\t"
    "v_mfma_f32_16x16x32_bf16 v[128:131], a[168:171], a[136:139], v[128:131]\n\t"
    "buffer_load_dwordx4 v226, s[40:43], s64 offen offset:0 lds\n\t"
    "v_mfma_f32_16x16x32_bf16 v[128:131], a[172:175], a[140:143], v[128:131]\n\t"
    "v_exp_f32 v153, v153\n\t"
    "v_mfma_f32_16x16x32_bf16 v[132:135], a[176:179], a[128:131], 0\n\t"
    "v_cvt_pk_bf16_f32 v147, v150, v151\n\t"
    "v_fma_f32 v154, v154, s60, v171\n\t"
    "v_mfma_f32_16x16x32_bf16 v[132:135], a[180:183], a[132:135], v[132:135]\n\t"
    "v_exp_f32 v154, v154\n\t"
    "v_mfma_f32_16x16x32_bf16 v[132:135], a[184:187], a[136:139], v[132:135]\n\t"
    "v_fma_f32 v155, v155, s60, v171\n\t"
    "v_fma_f32 v156, v156, s60, v171\n\t"
    "v_mfma_f32_16x16x32_bf16 v[132:135], a[188:191], a[140:143], v[132:135]\n\t"
    "buffer_load_dwordx4 v226, s[40:43], s64 offen offset:2048 lds\n\t"
    "v_mfma_f32_16x16x32_bf16 a[232:235], a[248:251], v[144:147], a[232:235]\n\t"
    "v_exp_f32 v155, v155\n\t"
    "v_mfma_f32_16x16x32_bf16 v[64:67], a[192:195], v[144:147], v[64:67]\n\t"
    "v_exp_f32 v156, v156\n\t"
    "v_mfma_f32_16x16x32_bf16 v[68:71], a[196:199], v[144:147], v[68:71]\n\t"
    "v_fma_f32 v157, v157, s60, v171\n\t"
    "v_fma_f32 v158, v158, s60, v171\n\t"
    "v_mfma_f32_16x16x32_bf16 v[72:75], a[200:203], v[144:147], v[72:75]\n\t"
    "v_exp_f32 v157, v157\n\t"
    "v_mfma_f32_16x16x32_bf16 v[76:79], a[204:207], v[144:147], v[76:79]\n\t"
    "v_exp_f32 v158, v158\n\t"
    "v_mfma_f32_16x16x32_bf16 v[80:83], a[208:211], v[144:147], v[80:83]\n\t"
    "v_fma_f32 v159, v159, s60, v171\n\t"
    "v_cvt_pk_bf16_f32 v152, v152, v153\n\t"
    "v_mfma_f32_16x16x32_bf16 v[84:87], a[212:215], v[144:147], v[84:87]\n\t"
    "v_exp_f32 v159, v159\n\t"
    "v_mfma_f32_16x16x32_bf16 v[88:91], a[216:219], v[144:147], v[88:91]\n\t"
    "v_cvt_pk_bf16_f32 v153, v154, v155\n\t"
    "v_cvt_pk_bf16_f32 v154, v156, v157\n\t"
    "v_mfma_f32_16x16x32_bf16 v[92:95], a[220:223], v[144:147], v[92:95]\n\t"
    "v_cvt_pk_bf16_f32 v155, v158, v159\n\t"
    "v_fma_f32 v128, v128, s60, v172\n\t"
    "v_mfma_f32_16x16x32_bf16 v[136:139], a[160:163], a[144:147], 0\n\t"
    "ds_read_b128 a[160:163], v224 offset:24576\n\t"
    "v_fma_f32 v129, v129, s60, v172\n\t"
    "v_mfma_f32_16x16x32_bf16 v[136:139], a[164:167], a[148:151], v[136:139]\n\t"
    "v_exp_f32 v128, v128\n\t"
    "v_mfma_f32_16x16x32_bf16 v[136:139], a[168:171], a[152:155], v[136:139]\n\t"
    "ds_read_b128 a[164:167], v224 offset:25088\n\t"
    "v_fma_f32 v130, v130, s60, v172\n\t"
    "v_mfma_f32_16x16x32_bf16 v[136:139], a[172:175], a[156:159], v[136:139]\n\t"
    "v_exp_f32 v129, v129\n\t"
    "v_mfma_f32_16x16x32_bf16 v[140:143], a[176:179], a[144:147], 0\n\t"
    "ds_read_b128 a[168:171], v224 offset:25600\n\t"
    "v_fma_f32 v131, v131, s60, v172\n\t"
    "v_mfma_f32_16x16x32_bf16 v[140:143], a[180:183], a[148:151], v[140:143]\n\t"
    "v_exp_f32 v130, v130\n\t"
    "v_mfma_f32_16x16x32_bf16 v[140:143], a[184:187], a[152:155], v[140:143]\n\t"
    "ds_read_b128 a[172:175], v224 offset:26112\n\t"
    "v_fma_f32 v132, v132, s60, v172\n\t"
    "v_mfma_f32_16x16x32_bf16 v[140:143], a[188:191], a[156:159], v[140:143]\n\t"
    "v_exp_f32 v131, v131\n\t"
    "v_mfma_f32_16x16x32_bf16 a[236:239], a[248:251], v[152:155], a[236:239]\n\t"
    "ds_read_b128 a[176:179], v224 offset:28672\n\t"
    "v_fma_f32 v133, v133, s60, v172\n\t"
    "v_mfma_f32_16x16x32_bf16 v[96:99], a[192:195], v[152:155], v[96:99]\n\t"
    "v_exp_f32 v132, v132\n\t"
    "v_mfma_f32_16x16x32_bf16 v[100:103], a[196:199], v[152:155], v[100:103]\n\t"
    "ds_read_b128 a[180:183], v224 offset:29184\n\t"
    "v_fma_f32 v134, v134, s60, v172\n\t"
    "v_mfma_f32_16x16x32_bf16 v[104:107], a[200:203], v[152:155], v[104:107]\n\t"
    "v_exp_f32 v133, v133\n\t"
    "v_mfma_f32_16x16x32_bf16 v[108:111], a[204:207], v[152:155], v[108:111]\n\t"
    "ds_read_b128 a[184:187], v224 offset:29696\n\t"
    "v_fma_f32 v135, v135, s60, v172\n\t"
    "v_mfma_f32_16x16x32_bf16 v[112:115], a[208:211], v[152:155], v[112:115]\n\t"
    "ds_read_b128 a[188:191], v224 offset:30208\n\t"
    "v_cvt_pk_bf16_f32 v128, v128, v129\n\t"
    "v_mfma_f32_16x16x32_bf16 v[116:119], a[212:215], v[152:155], v[116:119]\n\t"
    "v_exp_f32 v134, v134\n\t"
    "v_mfma_f32_16x16x32_bf16 v[120:123], a[216:219], v[152:155], v[120:123]\n\t"
    "v_exp_f32 v135, v135\n\t"
    "v_mfma_f32_16x16x32_bf16 v[124:127], a[220:223], v[152:155], v[124:127]\n\t"
    "s_waitcnt lgkmcnt(6)\n\t"
    "v_cvt_pk_bf16_f32 v129, v130, v131\n\t"
    "v_cvt_pk_bf16_f32 v130, v132, v133\n\t"
    "v_mfma_f32_16x16x32_bf16 v[144:147], a[160:163], a[64:67], 0\n\t"
    "v_cvt_pk_bf16_f32 v131, v134, v135\n\t"
    "v_fma_f32 v136, v136, s60, v173\n\t"
    "v_mfma_f32_16x16x32_bf16 v[144:147], a[164:167], a[68:71], v[144:147]\n\t"
    "s_waitcnt lgkmcnt(4)\n\t"
    "v_exp_f32 v136, v136\n\t"
    "v_mfma_f32_16x16x32_bf16 v[144:147], a[168:171], a[72:75], v[144:147]\n\t"
    "v_fma_f32 v137, v137, s60, v173\n\t"
    "v_fma_f32 v138, v138, s60, v173\n\t"
    "v_mfma_f32_16x16x32_bf16 v[144:147], a[172:175], a[76:79], v[144:147]\n\t"
    "s_waitcnt lgkmcnt(2)\n\t"
    "v_exp_f32 v137, v137\n\t"
    "v_mfma_f32_16x16x32_bf16 v[148:151], a[176:179], a[64:67], 0\n\t"
    "v_exp_f32 v138, v138\n\t"
    "v_mfma_f32_16x16x32_bf16 v[148:151], a[180:183], a[68:71], v[148:151]\n\t"
    "s_waitcnt lgkmcnt(0)\n\t"
    "v_fma_f32 v139, v139, s60, v173\n\t"
    "v_fma_f32 v140, v140, s60, v173\n\t"
    "v_mfma_f32_16x16x32_bf16 v[148:151], a[184:187], a[72:75], v[148:151]\n\t"
    "v_exp_f32 v139, v139\n\t"
    "v_mfma_f32_16x16x32_bf16 v[148:151], a[188:191], a[76:79], v[148:151]\n\t"
    "v_exp_f32 v140, v140\n\t"
    "v_mfma_f32_16x16x32_bf16 a[240:243], a[248:251], v[128:131], a[240:243]\n\t"
    "v_fma_f32 v141, v141, s60, v173\n\t"
    "v_fma_f32 v142, v142, s60, v173\n\t"
    "v_mfma_f32_16x16x32_bf16 v[208:211], a[192:195], v[128:131], v[208:211]\n\t"
    "v_exp_f32 v141, v141\n\t"
    "v_mfma_f32_16x16x32_bf16 v[212:215], a[196:199], v[128:131], v[212:215]\n\t"
    "v_exp_f32 v142, v142\n\t"
    "v_mfma_f32_16x16x32_bf16 v[216:219], a[200:203], v[128:131], v[216:219]\n\t"
    "v_fma_f32 v143, v143, s60, v173\n\t"
    "v_cvt_pk_bf16_f32 v136, v136, v137\n\t"
    "v_mfma_f32_16x16x32_bf16 v[220:223], a[204:207], v[128:131], v[220:223]\n\t"
    "v_exp_f32 v143, v143\n\t"
    "v_mfma_f32_16x16x32_bf16 v[240:243], a[208:211], v[128:131], v[240:243]\n\t"
    "v_cvt_pk_bf16_f32 v137, v138, v139\n\t"
    "v_cvt_pk_bf16_f32 v138, v140, v141\n\t"
    "v_mfma_f32_16x16x32_bf16 v[244:247], a[212:215], v[128:131], v[244:247]\n\t"
    "v_cvt_pk_bf16_f32 v139, v142, v143\n\t"
    "v_fma_f32 v144, v144, s60, v168\n\t"
    "v_mfma_f32_16x16x32_bf16 v[248:251], a[216:219], v[128:131], v[248:251]\n\t"
    "v_exp_f32 v144, v144\n\t"
    "v_mfma_f32_16x16x32_bf16 v[252:255], a[220:223], v[128:131], v[252:255]\n\t"
    "v_fma_f32 v145, v145, s60, v168\n\t"
    "v_fma_f32 v146, v146, s60, v168\n\t"
    "v_mfma_f32_16x16x32_bf16 v[152:155], a[160:163], a[80:83], 0\n\t"
    "ds_read_b128 a[0:3], v232 offset:4096\n\t"
    "v_fma_f32 v147, v147, s60, v168\n\t"
    "v_mfma_f32_16x16x32_bf16 v[152:155], a[164:167], a[84:87], v[152:155]\n\t"
    "v_exp_f32 v145, v145\n\t"
    "v_mfma_f32_16x16x32_bf16 v[152:155], a[168:171], a[88:91], v[152:155]\n\t"
    "ds_read_b128 a[4:7], v232 offset:5120\n\t"
    "v_fma_f32 v148, v148, s60, v168\n\t"
    "v_mfma_f32_16x16x32_bf16 v[152:155], a[172:175], a[92:95], v[152:155]\n\t"
    "v_exp_f32 v146, v146\n\t"
    "v_mfma_f32_16x16x32_bf16 v[156:159], a[176:179], a[80:83], 0\n\t"
    "ds_read_b128 a[8:11], v232 offset:6144\n\t"
    "v_fma_f32 v149, v149, s60, v168\n\t"
    "v_mfma_f32_16x16x32_bf16 v[156:159], a[180:183], a[84:87], v[156:159]\n\t"
    "v_exp_f32 v147, v147\n\t"
    "v_mfma_f32_16x16x32_bf16 v[156:159], a[184:187], a[88:91], v[156:159]\n\t"
    "ds_read_b128 a[12:15], v232 offset:7168\n\t"
    "v_fma_f32 v150, v150, s60, v168\n\t"
    "v_mfma_f32_16x16x32_bf16 v[156:159], a[188:191], a[92:95], v[156:159]\n\t"
    "v_exp_f32 v148, v148\n\t"
    "v_mfma_f32_16x16x32_bf16 a[244:247], a[248:251], v[136:139], a[244:247]\n\t"
    "v_exp_f32 v149, v149\n\t"
    "v_mfma_f32_16x16x32_bf16 v[176:179], a[192:195], v[136:139], v[176:179]\n\t"
    "v_exp_f32 v150, v150\n\t"
    "v_mfma_f32_16x16x32_bf16 v[180:183], a[196:199], v[136:139], v[180:183]\n\t"
    "v_fma_f32 v151, v151, s60, v168\n\t"
    "v_cvt_pk_bf16_f32 v144, v144, v145\n\t"
    "v_mfma_f32_16x16x32_bf16 v[184:187], a[200:203], v[136:139], v[184:187]\n\t"
    "v_exp_f32 v151, v151\n\t"
    "v_mfma_f32_16x16x32_bf16 v[188:191], a[204:207], v[136:139], v[188:191]\n\t"
    "v_cvt_pk_bf16_f32 v145, v146, v147\n\t"
    "v_cvt_pk_bf16_f32 v146, v148, v149\n\t"
    "v_mfma_f32_16x16x32_bf16 v[192:195], a[208:211], v[136:139], v[192:195]\n\t"
    "ds_read_b64_tr_b16 a[208:209], v225 offset:25600\n\t"
    "v_cvt_pk_bf16_f32 v147, v150, v151\n\t"
    "v_mfma_f32_16x16x32_bf16 v[196:199], a[212:215], v[136:139], v[196:199]\n\t"
    "v_fma_f32 v152, v152, s60, v169\n\t"
    "v_fma_f32 v153, v153, s60, v169\n\t"
    "v_mfma_f32_16x16x32_bf16 v[200:203], a[216:219], v[136:139], v[200:203]\n\t"
    "ds_read_b64_tr_b16 a[210:211], v225 offset:29696\n\t"
    "v_fma_f32 v154, v154, s60, v169\n\t"
    "v_mfma_f32_16x16x32_bf16 v[204:207], a[220:223], v[136:139], v[204:207]\n\t"
    "v_exp_f32 v152, v152\n\t"
    "v_mfma_f32_16x16x32_bf16 v[128:131], a[160:163], a[96:99], 0\n\t"
    "ds_read_b64_tr_b16 a[212:213], v225 offset:25608\n\t"
    "v_fma_f32 v155, v155, s60, v169\n\t"
    "v_mfma_f32_16x16x32_bf16 v[128:131], a[164:167], a[100:103], v[128:131]\n\t"
    "s_waitcnt lgkmcnt(3)\n\t"
    "v_exp_f32 v153, v153\n\t"
    "v_mfma_f32_16x16x32_bf16 v[128:131], a[168:171], a[104:107], v[128:131]\n\t"
    "ds_read_b64_tr_b16 a[214:215], v225 offset:29704\n\t"
    "s_add_u32 s65, s53, 4096\n\t"
    "v_mfma_f32_16x16x32_bf16 v[128:131], a[172:175], a[108:111], v[128:131]\n\t"
    "ds_read_b64_tr_b16 a[216:217], v225 offset:26112\n\t"
    "s_add_u32 m0, s69, 4096\n\t"
    "v_mfma_f32_16x16x32_bf16 v[132:135], a[176:179], a[96:99], 0\n\t"
    "ds_read_b64_tr_b16 a[218:219], v225 offset:30208\n\t"
    "s_add_u32 s66, s54, 4096\n\t"
    "v_mfma_f32_16x16x32_bf16 v[132:135], a[180:183], a[100:103], v[132:135]\n\t"
    "ds_read_b64_tr_b16 a[220:221], v225 offset:26120\n\t"
    "v_fma_f32 v156, v156, s60, v169\n\t"
    "v_mfma_f32_16x16x32_bf16 v[132:135], a[184:187], a[104:107], v[132:135]\n\t"
    "ds_read_b64_tr_b16 a[222:223], v225 offset:30216\n\t"
    "v_fma_f32 v157, v157, s60, v169\n\t"
    "v_mfma_f32_16x16x32_bf16 v[132:135], a[188:191], a[108:111], v[132:135]\n\t"
    "buffer_load_dwordx4 v226, s[44:47], s65 offen offset:0 nt lds\n\t"
    "v_mfma_f32_16x16x32_bf16 a[224:227], a[248:251], v[144:147], a[224:227]\n\t"
    "s_waitcnt lgkmcnt(15)\n\t"
    "v_exp_f32 v154, v154\n\t"
    "v_mfma_f32_16x16x32_bf16 v[0:3], a[32:35], v[144:147], v[0:3]\n\t"
    "buffer_load_dwordx4 v226, s[44:47], s65 offen offset:2048 nt lds\n\t"
    "v_mfma_f32_16x16x32_bf16 v[4:7], a[36:39], v[144:147], v[4:7]\n\t"
    "buffer_store_dwordx4 a[0:3], v227, s[48:51], s66 offen offset:0 nt\n\t"
    "v_mfma_f32_16x16x32_bf16 v[8:11], a[40:43], v[144:147], v[8:11]\n\t"
    "v_exp_f32 v155, v155\n\t"
    "v_mfma_f32_16x16x32_bf16 v[12:15], a[44:47], v[144:147], v[12:15]\n\t"
    "s_waitcnt lgkmcnt(4)\n\t"
    "v_exp_f32 v156, v156\n\t"
    "v_mfma_f32_16x16x32_bf16 v[16:19], a[208:211], v[144:147], v[16:19]\n\t"
    "v_exp_f32 v157, v157\n\t"
    "v_mfma_f32_16x16x32_bf16 v[20:23], a[212:215], v[144:147], v[20:23]\n\t"
    "s_waitcnt lgkmcnt(0)\n\t"
    "v_fma_f32 v158, v158, s60, v169\n\t"
    "v_fma_f32 v159, v159, s60, v169\n\t"
    "v_mfma_f32_16x16x32_bf16 v[24:27], a[216:219], v[144:147], v[24:27]\n\t"
    "v_exp_f32 v158, v158\n\t"
    "v_mfma_f32_16x16x32_bf16 v[28:31], a[220:223], v[144:147], v[28:31]\n\t"
    "v_exp_f32 v159, v159\n\t"
    "v_mfma_f32_16x16x32_bf16 v[136:139], a[160:163], a[112:115], 0\n\t"
    "s_waitcnt vmcnt(11) lgkmcnt(0)\n\t"
    "v_cvt_pk_bf16_f32 v152, v152, v153\n\t"
    "v_cvt_pk_bf16_f32 v153, v154, v155\n\t"
    "v_mfma_f32_16x16x32_bf16 v[136:139], a[164:167], a[116:119], v[136:139]\n\t"
    "s_barrier\n\t"
    "v_cvt_pk_bf16_f32 v154, v156, v157\n\t"
    "v_cvt_pk_bf16_f32 v155, v158, v159\n\t"
    "v_mfma_f32_16x16x32_bf16 v[136:139], a[168:171], a[120:123], v[136:139]\n\t"
    "ds_read_b64_tr_b16 a[192:193], v225 offset:0\n\t"
    "v_fma_f32 v128, v128, s60, v170\n\t"
    "v_mfma_f32_16x16x32_bf16 v[136:139], a[172:175], a[124:127], v[136:139]\n\t"
    "s_xor_b32 s64, s52, 57344\n\t"
    "v_exp_f32 v128, v128\n\t"
    "v_mfma_f32_16x16x32_bf16 v[140:143], a[176:179], a[112:115], 0\n\t"
    "ds_read_b64_tr_b16 a[194:195], v225 offset:4096\n\t"
    "v_fma_f32 v129, v129, s60, v170\n\t"
    "v_mfma_f32_16x16x32_bf16 v[140:143], a[180:183], a[116:119], v[140:143]\n\t"
    "s_add_u32 m0, s63, 24576\n\t"
    "v_exp_f32 v129, v129\n\t"
    "v_mfma_f32_16x16x32_bf16 v[140:143], a[184:187], a[120:123], v[140:143]\n\t"
    "ds_read_b64_tr_b16 a[196:197], v225 offset:8\n\t"
    "v_fma_f32 v130, v130, s60, v170\n\t"
    "v_mfma_f32_16x16x32_bf16 v[140:143], a[188:191], a[124:127], v[140:143]\n\t"
    "buffer_load_dwordx4 v226, s[36:39], s64 offen offset:0 lds\n\t"
    "v_mfma_f32_16x16x32_bf16 a[228:231], a[248:251], v[152:155], a[228:231]\n\t"
    "ds_read_b64_tr_b16 a[198:199], v225 offset:4104\n\t"
    "v_fma_f32 v131, v131, s60, v170\n\t"
    "v_mfma_f32_16x16x32_bf16 v[32:35], a[32:35], v[152:155], v[32:35]\n\t"
    "v_exp_f32 v130, v130\n\t"
    "v_mfma_f32_16x16x32_bf16 v[36:39], a[36:39], v[152:155], v[36:39]\n\t"
    "ds_read_b64_tr_b16 a[200:201], v225 offset:512\n\t"
    "v_fma_f32 v132, v132, s60, v170\n\t"
    "v_mfma_f32_16x16x32_bf16 v[40:43], a[40:43], v[152:155], v[40:43]\n\t"
    "v_exp_f32 v131, v131\n\t"
    "v_mfma_f32_16x16x32_bf16 v[44:47], a[44:47], v[152:155], v[44:47]\n\t"
    "ds_read_b64_tr_b16 a[202:203], v225 offset:4608\n\t"
    "v_fma_f32 v133, v133, s60, v170\n\t"
    "v_mfma_f32_16x16x32_bf16 v[48:51], a[208:211], v[152:155], v[48:51]\n\t"
    "buffer_load_dwordx4 v226, s[36:39], s64 offen offset:2048 lds\n\t"
    "v_fma_f32 v134, v134, s60, v170\n\t"
    "v_mfma_f32_16x16x32_bf16 v[52:55], a[212:215], v[152:155], v[52:55]\n\t"
    "ds_read_b64_tr_b16 a[204:205], v225 offset:520\n\t"
    "v_fma_f32 v135, v135, s60, v170\n\t"
    "v_exp_f32 v132, v132\n\t"
    "v_mfma_f32_16x16x32_bf16 v[56:59], a[216:219], v[152:155], v[56:59]\n\t"
    "v_exp_f32 v133, v133\n\t"
    "v_mfma_f32_16x16x32_bf16 v[60:63], a[220:223], v[152:155], v[60:63]\n\t"
    "ds_read_b64_tr_b16 a[206:207], v225 offset:4616\n\t"
    "v_cvt_pk_bf16_f32 v128, v128, v129\n\t"
    "v_mfma_f32_16x16x32_bf16 v[144:147], a[160:163], a[128:131], 0\n\t"
    "v_exp_f32 v134, v134\n\t"
    "v_mfma_f32_16x16x32_bf16 v[144:147], a[164:167], a[132:135], v[144:147]\n\t"
    "s_add_u32 m0, s63, 57344\n\t"
    "v_exp_f32 v135, v135\n\t"
    "v_mfma_f32_16x16x32_bf16 v[144:147], a[168:171], a[136:139], v[144:147]\n\t"
    "buffer_load_dwordx4 v226, s[40:43], s64 offen offset:0 lds\n\t"
    "v_mfma_f32_16x16x32_bf16 v[144:147], a[172:175], a[140:143], v[144:147]\n\t"
    "v_cvt_pk_bf16_f32 v129, v130, v131\n\t"
    "v_cvt_pk_bf16_f32 v130, v132, v133\n\t"
    "v_mfma_f32_16x16x32_bf16 v[148:151], a[176:179], a[128:131], 0\n\t"
    "v_cvt_pk_bf16_f32 v131, v134, v135\n\t"
    "v_fma_f32 v136, v136, s60, v171\n\t"
    "v_mfma_f32_16x16x32_bf16 v[148:151], a[180:183], a[132:135], v[148:151]\n\t"
    "v_exp_f32 v136, v136\n\t"
    "v_mfma_f32_16x16x32_bf16 v[148:151], a[184:187], a[136:139], v[148:151]\n\t"
    "v_fma_f32 v137, v137, s60, v171\n\t"
    "v_fma_f32 v138, v138, s60, v171\n\t"
    "v_mfma_f32_16x16x32_bf16 v[148:151], a[188:191], a[140:143], v[148:151]\n\t"
    "buffer_load_dwordx4 v226, s[40:43], s64 offen offset:2048 lds\n\t"
    "v_mfma_f32_16x16x32_bf16 a[232:235], a[248:251], v[128:131], a[232:235]\n\t"
    "s_add_u32 s65, s53, 4224\n\t"
    "v_exp_f32 v137, v137\n\t"
    "v_mfma_f32_16x16x32_bf16 v[64:67], a[32:35], v[128:131], v[64:67]\n\t"
    "s_add_u32 m0, s69, 5120\n\t"
    "v_exp_f32 v138, v138\n\t"
    "v_mfma_f32_16x16x32_bf16 v[68:71], a[36:39], v[128:131], v[68:71]\n\t"
    "buffer_load_dwordx4 v226, s[44:47], s65 offen offset:0 nt lds\n\t"
    "v_mfma_f32_16x16x32_bf16 v[72:75], a[40:43], v[128:131], v[72:75]\n\t"
    "v_fma_f32 v139, v139, s60, v171\n\t"
    "v_fma_f32 v140, v140, s60, v171\n\t"
    "v_mfma_f32_16x16x32_bf16 v[76:79], a[44:47], v[128:131], v[76:79]\n\t"
    "buffer_load_dwordx4 v226, s[44:47], s65 offen offset:2048 nt lds\n\t"
    "v_mfma_f32_16x16x32_bf16 v[80:83], a[208:211], v[128:131], v[80:83]\n\t"
    "v_exp_f32 v139, v139\n\t"
    "v_mfma_f32_16x16x32_bf16 v[84:87], a[212:215], v[128:131], v[84:87]\n\t"
    "v_exp_f32 v140, v140\n\t"
    "v_fma_f32 v141, v141, s60, v171\n\t"
    "v_mfma_f32_16x16x32_bf16 v[88:91], a[216:219], v[128:131], v[88:91]\n\t"
    "v_exp_f32 v141, v141\n\t"
    "v_mfma_f32_16x16x32_bf16 v[92:95], a[220:223], v[128:131], v[92:95]\n\t"
    "v_fma_f32 v142, v142, s60, v171\n\t"
    "v_fma_f32 v143, v143, s60, v171\n\t"
    "v_mfma_f32_16x16x32_bf16 v[152:155], a[160:163], a[144:147], 0\n\t"
    "ds_read_b128 a[160:163], v224 offset:0\n\t"
    "v_cvt_pk_bf16_f32 v136, v136, v137\n\t"
    "v_mfma_f32_16x16x32_bf16 v[152:155], a[164:167], a[148:151], v[152:155]\n\t"
    "v_exp_f32 v142, v142\n\t"
    "v_mfma_f32_16x16x32_bf16 v[152:155], a[168:171], a[152:155], v[152:155]\n\t"
    "ds_read_b128 a[164:167], v224 offset:512\n\t"
    "v_cvt_pk_bf16_f32 v137, v138, v139\n\t"
    "v_mfma_f32_16x16x32_bf16 v[152:155], a[172:175], a[156:159], v[152:155]\n\t"
    "v_exp_f32 v143, v143\n\t"
    "v_mfma_f32_16x16x32_bf16 v[156:159], a[176:179], a[144:147], 0\n\t"
    "ds_read_b128 a[168:171], v224 offset:1024\n\t"
    "v_cvt_pk_bf16_f32 v138, v140, v141\n\t"
    "v_mfma_f32_16x16x32_bf16 v[156:159], a[180:183], a[148:151], v[156:159]\n\t"
    "v_cvt_pk_bf16_f32 v139, v142, v143\n\t"
    "v_fma_f32 v144, v144, s60, v172\n\t"
    "v_mfma_f32_16x16x32_bf16 v[156:159], a[184:187], a[152:155], v[156:159]\n\t"
    "ds_read_b128 a[172:175], v224 offset:1536\n\t"
    "v_fma_f32 v145, v145, s60, v172\n\t"
    "v_mfma_f32_16x16x32_bf16 v[156:159], a[188:191], a[156:159], v[156:159]\n\t"
    "v_exp_f32 v144, v144\n\t"
    "v_mfma_f32_16x16x32_bf16 a[236:239], a[248:251], v[136:139], a[236:239]\n\t"
    "ds_read_b128 a[176:179], v224 offset:4096\n\t"
    "v_fma_f32 v146, v146, s60, v172\n\t"
    "v_mfma_f32_16x16x32_bf16 v[96:99], a[32:35], v[136:139], v[96:99]\n\t"
    "buffer_store_dwordx4 a[4:7], v228, s[48:51], s66 offen offset:1024 nt\n\t"
    "v_mfma_f32_16x16x32_bf16 v[100:103], a[36:39], v[136:139], v[100:103]\n\t"
    "ds_read_b128 a[180:183], v224 offset:4608\n\t"
    "v_fma_f32 v147, v147, s60, v172\n\t"
    "v_mfma_f32_16x16x32_bf16 v[104:107], a[40:43], v[136:139], v[104:107]\n\t"
    "v_exp_f32 v145, v145\n\t"
    "v_mfma_f32_16x16x32_bf16 v[108:111], a[44:47], v[136:139], v[108:111]\n\t"
    "ds_read_b128 a[184:187], v224 offset:5120\n\t"
    "v_fma_f32 v148, v148, s60, v172\n\t"
    "v_mfma_f32_16x16x32_bf16 v[112:115], a[208:211], v[136:139], v[112:115]\n\t"
    "ds_read_b128 a[188:191], v224 offset:5632\n\t"
    "v_fma_f32 v149, v149, s60, v172\n\t"
    "v_mfma_f32_16x16x32_bf16 v[116:119], a[212:215], v[136:139], v[116:119]\n\t"
    "v_exp_f32 v146, v146\n\t"
    "v_mfma_f32_16x16x32_bf16 v[120:123], a[216:219], v[136:139], v[120:123]\n\t"
    "v_exp_f32 v147, v147\n\t"
    "v_mfma_f32_16x16x32_bf16 v[124:127], a[220:223], v[136:139], v[124:127]\n\t"
    "s_waitcnt lgkmcnt(6)\n\t"
    "v_exp_f32 v148, v148\n\t"
    "v_mfma_f32_16x16x32_bf16 v[128:131], a[160:163], a[64:67], 0\n\t"
    "v_exp_f32 v149, v149\n\t"
    "v_mfma_f32_16x16x32_bf16 v[128:131], a[164:167], a[68:71], v[128:131]\n\t"
    "s_waitcnt lgkmcnt(4)\n\t"
    "v_fma_f32 v150, v150, s60, v172\n\t"
    "v_fma_f32 v151, v151, s60, v172\n\t"
    "v_mfma_f32_16x16x32_bf16 v[128:131], a[168:171], a[72:75], v[128:131]\n\t"
    "v_exp_f32 v150, v150\n\t"
    "v_mfma_f32_16x16x32_bf16 v[128:131], a[172:175], a[76:79], v[128:131]\n\t"
    "s_waitcnt lgkmcnt(2)\n\t"
    "v_exp_f32 v151, v151\n\t"
    "v_mfma_f32_16x16x32_bf16 v[132:135], a[176:179], a[64:67], 0\n\t"
    "v_cvt_pk_bf16_f32 v144, v144, v145\n\t"
    "v_cvt_pk_bf16_f32 v145, v146, v147\n\t"
    "v_mfma_f32_16x16x32_bf16 v[132:135], a[180:183], a[68:71], v[132:135]\n\t"
    "s_waitcnt lgkmcnt(0)\n\t"
    "v_cvt_pk_bf16_f32 v146, v148, v149\n\t"
    "v_cvt_pk_bf16_f32 v147, v150, v151\n\t"
    "v_mfma_f32_16x16x32_bf16 v[132:135], a[184:187], a[72:75], v[132:135]\n\t"
    "v_fma_f32 v152, v152, s60, v173\n\t"
    "v_fma_f32 v153, v153, s60, v173\n\t"
    "v_mfma_f32_16x16x32_bf16 v[132:135], a[188:191], a[76:79], v[132:135]\n\t"
    "v_exp_f32 v152, v152\n\t"
    "v_mfma_f32_16x16x32_bf16 a[240:243], a[248:251], v[144:147], a[240:243]\n\t"
    "v_exp_f32 v153, v153\n\t"
    "v_mfma_f32_16x16x32_bf16 v[208:211], a[32:35], v[144:147], v[208:211]\n\t"
    "v_fma_f32 v154, v154, s60, v173\n\t"
    "v_fma_f32 v155, v155, s60, v173\n\t"
    "v_mfma_f32_16x16x32_bf16 v[212:215], a[36:39], v[144:147], v[212:215]\n\t"
    "v_exp_f32 v154, v154\n\t"
    "v_mfma_f32_16x16x32_bf16 v[216:219], a[40:43], v[144:147], v[216:219]\n\t"
    "v_exp_f32 v155, v155\n\t"
    "v_mfma_f32_16x16x32_bf16 v[220:223], a[44:47], v[144:147], v[220:223]\n\t"
    "v_fma_f32 v156, v156, s60, v173\n\t"
    "v_fma_f32 v157, v157, s60, v173\n\t"
    "v_mfma_f32_16x16x32_bf16 v[240:243], a[208:211], v[144:147], v[240:243]\n\t"
    "v_exp_f32 v156, v156\n\t"
    "v_mfma_f32_16x16x32_bf16 v[244:247], a[212:215], v[144:147], v[244:247]\n\t"
    "v_exp_f32 v157, v157\n\t"
    "v_mfma_f32_16x16x32_bf16 v[248:251], a[216:219], v[144:147], v[248:251]\n\t"
    "v_fma_f32 v158, v158, s60, v173\n\t"
    "v_fma_f32 v159, v159, s60, v173\n\t"
    "v_mfma_f32_16x16x32_bf16 v[252:255], a[220:223], v[144:147], v[252:255]\n\t"
    "v_exp_f32 v158, v158\n\t"
    "v_mfma_f32_16x16x32_bf16 v[136:139], a[160:163], a[80:83], 0\n\t"
    "v_exp_f32 v159, v159\n\t"
    "v_mfma_f32_16x16x32_bf16 v[136:139], a[164:167], a[84:87], v[136:139]\n\t"
    "v_cvt_pk_bf16_f32 v152, v152, v153\n\t"
    "v_cvt_pk_bf16_f32 v153, v154, v155\n\t"
    "v_mfma_f32_16x16x32_bf16 v[136:139], a[168:171], a[88:91], v[136:139]\n\t"
    "v_cvt_pk_bf16_f32 v154, v156, v157\n\t"
    "v_cvt_pk_bf16_f32 v155, v158, v159\n\t"
    "v_mfma_f32_16x16x32_bf16 v[136:139], a[172:175], a[92:95], v[136:139]\n\t"
    "v_fma_f32 v128, v128, s60, v168\n\t"
    "v_fma_f32 v129, v129, s60, v168\n\t"
    "v_mfma_f32_16x16x32_bf16 v[140:143], a[176:179], a[80:83], 0\n\t"
    "v_exp_f32 v128, v128\n\t"
    "v_mfma_f32_16x16x32_bf16 v[140:143], a[180:183], a[84:87], v[140:143]\n\t"
    "v_exp_f32 v129, v129\n\t"
    "v_mfma_f32_16x16x32_bf16 v[140:143], a[184:187], a[88:91], v[140:143]\n\t"
    "v_fma_f32 v130, v130, s60, v168\n\t"
    "v_fma_f32 v131, v131, s60, v168\n\t"
    "v_mfma_f32_16x16x32_bf16 v[140:143], a[188:191], a[92:95], v[140:143]\n\t"
    "v_exp_f32 v130, v130\n\t"
    "v_mfma_f32_16x16x32_bf16 a[244:247], a[248:251], v[152:155], a[244:247]\n\t"
    "v_exp_f32 v131, v131\n\t"
    "v_mfma_f32_16x16x32_bf16 v[176:179], a[32:35], v[152:155], v[176:179]\n\t"
    "v_fma_f32 v132, v132, s60, v168\n\t"
    "v_fma_f32 v133, v133, s60, v168\n\t"
    "v_mfma_f32_16x16x32_bf16 v[180:183], a[36:39], v[152:155], v[180:183]\n\t"
    "v_exp_f32 v132, v132\n\t"
    "v_mfma_f32_16x16x32_bf16 v[184:187], a[40:43], v[152:155], v[184:187]\n\t"
    "v_exp_f32 v133, v133\n\t"
    "v_mfma_f32_16x16x32_bf16 v[188:191], a[44:47], v[152:155], v[188:191]\n\t"
    "v_fma_f32 v134, v134, s60, v168\n\t"
    "v_fma_f32 v135, v135, s60, v168\n\t"
    "v_mfma_f32_16x16x32_bf16 v[192:195], a[208:211], v[152:155], v[192:195]\n\t"
    "ds_read_b64_tr_b16 a[208:209], v225 offset:1024\n\t"
    "v_cvt_pk_bf16_f32 v128, v128, v129\n\t"
    "v_mfma_f32_16x16x32_bf16 v[196:199], a[212:215], v[152:155], v[196:199]\n\t"
    "v_exp_f32 v134, v134\n\t"
    "v_mfma_f32_16x16x32_bf16 v[200:203], a[216:219], v[152:155], v[200:203]\n\t"
    "ds_read_b64_tr_b16 a[210:211], v225 offset:5120\n\t"
    "v_cvt_pk_bf16_f32 v129, v130, v131\n\t"
    "v_mfma_f32_16x16x32_bf16 v[204:207], a[220:223], v[152:155], v[204:207]\n\t"
    "v_exp_f32 v135, v135\n\t"
    "v_mfma_f32_16x16x32_bf16 v[144:147], a[160:163], a[96:99], 0\n\t"
    "ds_read_b64_tr_b16 a[212:213], v225 offset:1032\n\t"
    "v_cvt_pk_bf16_f32 v130, v132, v133\n\t"
    "v_mfma_f32_16x16x32_bf16 v[144:147], a[164:167], a[100:103], v[144:147]\n\t"
    "v_cvt_pk_bf16_f32 v131, v134, v135\n\t"
    "v_fma_f32 v136, v136, s60, v169\n\t"
    "v_mfma_f32_16x16x32_bf16 v[144:147], a[168:171], a[104:107], v[144:147]\n\t"
    "ds_read_b64_tr_b16 a[214:215], v225 offset:5128\n\t"
    "v_fma_f32 v137, v137, s60, v169\n\t"
    "v_mfma_f32_16x16x32_bf16 v[144:147], a[172:175], a[108:111], v[144:147]\n\t"
    "ds_read_b64_tr_b16 a[216:217], v225 offset:1536\n\t"
    "v_fma_f32 v138, v138, s60, v169\n\t"
    "v_mfma_f32_16x16x32_bf16 v[148:151], a[176:179], a[96:99], 0\n\t"
    "ds_read_b64_tr_b16 a[218:219], v225 offset:5632\n\t"
    "v_fma_f32 v139, v139, s60, v169\n\t"
    "v_mfma_f32_16x16x32_bf16 v[148:151], a[180:183], a[100:103], v[148:151]\n\t"
    "ds_read_b64_tr_b16 a[220:221], v225 offset:1544\n\t"
    "v_fma_f32 v140, v140, s60, v169\n\t"
    "v_mfma_f32_16x16x32_bf16 v[148:151], a[184:187], a[104:107], v[148:151]\n\t"
    "ds_read_b64_tr_b16 a[222:223], v225 offset:5640\n\t"
    "v_fma_f32 v141, v141, s60, v169\n\t"
    "v_mfma_f32_16x16x32_bf16 v[148:151], a[188:191], a[108:111], v[148:151]\n\t"
    "v_exp_f32 v136, v136\n\t"
    "v_mfma_f32_16x16x32_bf16 a[224:227], a[248:251], v[128:131], a[224:227]\n\t"
    "s_waitcnt lgkmcnt(15)\n\t"
    "v_exp_f32 v137, v137\n\t"
    "v_mfma_f32_16x16x32_bf16 v[0:3], a[192:195], v[128:131], v[0:3]\n\t"
    "v_exp_f32 v138, v138\n\t"
    "v_mfma_f32_16x16x32_bf16 v[4:7], a[196:199], v[128:131], v[4:7]\n\t"
    "v_exp_f32 v139, v139\n\t"
    "v_mfma_f32_16x16x32_bf16 v[8:11], a[200:203], v[128:131], v[8:11]\n\t"
    "buffer_store_dwordx4 a[8:11], v227, s[48:51], s66 offen offset:2048 nt\n\t"
    "v_mfma_f32_16x16x32_bf16 v[12:15], a[204:207], v[128:131], v[12:15]\n\t"
    "s_waitcnt lgkmcnt(4)\n\t"
    "v_exp_f32 v140, v140\n\t"
    "v_mfma_f32_16x16x32_bf16 v[16:19], a[208:211], v[128:131], v[16:19]\n\t"
    "v_exp_f32 v141, v141\n\t"
    "v_mfma_f32_16x16x32_bf16 v[20:23], a[212:215], v[128:131], v[20:23]\n\t"
    "s_waitcnt lgkmcnt(0)\n\t"
    "v_fma_f32 v142, v142, s60, v169\n\t"
    "v_fma_f32 v143, v143, s60, v169\n\t"
    "v_mfma_f32_16x16x32_bf16 v[24:27], a[216:219], v[128:131], v[24:27]\n\t"
    "v_exp_f32 v142, v142\n\t"
    "v_mfma_f32_16x16x32_bf16 v[28:31], a[220:223], v[128:131], v[28:31]\n\t"
    "v_exp_f32 v143, v143\n\t"
    "v_mfma_f32_16x16x32_bf16 v[152:155], a[160:163], a[112:115], 0\n\t"
    "s_waitcnt vmcnt(15) lgkmcnt(0)\n\t"
    "v_cvt_pk_bf16_f32 v136, v136, v137\n\t"
    "v_cvt_pk_bf16_f32 v137, v138, v139\n\t"
    "v_mfma_f32_16x16x32_bf16 v[152:155], a[164:167], a[116:119], v[152:155]\n\t"
    "s_barrier\n\t"
    "v_cvt_pk_bf16_f32 v138, v140, v141\n\t"
    "v_cvt_pk_bf16_f32 v139, v142, v143\n\t"
    "v_mfma_f32_16x16x32_bf16 v[152:155], a[168:171], a[120:123], v[152:155]\n\t"
    "ds_read_b64_tr_b16 a[32:33], v225 offset:8192\n\t"
    "v_fma_f32 v144, v144, s60, v170\n\t"
    "v_mfma_f32_16x16x32_bf16 v[152:155], a[172:175], a[124:127], v[152:155]\n\t"
    "s_xor_b32 s64, s52, 65536\n\t"
    "v_exp_f32 v144, v144\n\t"
    "v_mfma_f32_16x16x32_bf16 v[156:159], a[176:179], a[112:115], 0\n\t"
    "ds_read_b64_tr_b16 a[34:35], v225 offset:12288\n\t"
    "v_fma_f32 v145, v145, s60, v170\n\t"
    "v_mfma_f32_16x16x32_bf16 v[156:159], a[180:183], a[116:119], v[156:159]\n\t"
    "s_add_u32 m0, s63, 0\n\t"
    "v_exp_f32 v145, v145\n\t"
    "v_mfma_f32_16x16x32_bf16 v[156:159], a[184:187], a[120:123], v[156:159]\n\t"
    "ds_read_b64_tr_b16 a[36:37], v225 offset:8200\n\t"
    "v_fma_f32 v146, v146, s60, v170\n\t"
    "v_mfma_f32_16x16x32_bf16 v[156:159], a[188:191], a[124:127], v[156:159]\n\t"
    "buffer_load_dwordx4 v226, s[36:39], s64 offen offset:0 lds\n\t"
    "v_mfma_f32_16x16x32_bf16 a[228:231], a[248:251], v[136:139], a[228:231]\n\t"
    "ds_read_b64_tr_b16 a[38:39], v225 offset:12296\n\t"
    "v_fma_f32 v147, v147, s60, v170\n\t"
    "v_mfma_f32_16x16x32_bf16 v[32:35], a[192:195], v[136:139], v[32:35]\n\t"
    "v_exp_f32 v146, v146\n\t"
    "v_mfma_f32_16x16x32_bf16 v[36:39], a[196:199], v[136:139], v[36:39]\n\t"
    "ds_read_b64_tr_b16 a[40:41], v225 offset:8704\n\t"
    "v_fma_f32 v148, v148, s60, v170\n\t"
    "v_mfma_f32_16x16x32_bf16 v[40:43], a[200:203], v[136:139], v[40:43]\n\t"
    "v_exp_f32 v147, v147\n\t"
    "v_mfma_f32_16x16x32_bf16 v[44:47], a[204:207], v[136:139], v[44:47]\n\t"
    "ds_read_b64_tr_b16 a[42:43], v225 offset:12800\n\t"
    "v_fma_f32 v149, v149, s60, v170\n\t"
    "v_mfma_f32_16x16x32_bf16 v[48:51], a[208:211], v[136:139], v[48:51]\n\t"
    "buffer_load_dwordx4 v226, s[36:39], s64 offen offset:2048 lds\n\t"
    "v_mfma_f32_16x16x32_bf16 v[52:55], a[212:215], v[136:139], v[52:55]\n\t"
    "ds_read_b64_tr_b16 a[44:45], v225 offset:8712\n\t"
    "v_fma_f32 v150, v150, s60, v170\n\t"
    "v_mfma_f32_16x16x32_bf16 v[56:59], a[216:219], v[136:139], v[56:59]\n\t"
    "v_exp_f32 v148, v148\n\t"
    "v_mfma_f32_16x16x32_bf16 v[60:63], a[220:223], v[136:139], v[60:63]\n\t"
    "ds_read_b64_tr_b16 a[46:47], v225 offset:12808\n\t"
    "v_fma_f32 v151, v151, s60, v170\n\t"
    "v_mfma_f32_16x16x32_bf16 v[128:131], a[160:163], a[128:131], 0\n\t"
    "v_exp_f32 v149, v149\n\t"
    "v_mfma_f32_16x16x32_bf16 v[128:131], a[164:167], a[132:135], v[128:131]\n\t"
    "s_add_u32 m0, s63, 32768\n\t"
    "v_exp_f32 v150, v150\n\t"
    "v_mfma_f32_16x16x32_bf16 v[128:131], a[168:171], a[136:139], v[128:131]\n\t"
    "buffer_load_dwordx4 v226, s[40:43], s64 offen offset:0 lds\n\t"
    "v_mfma_f32_16x16x32_bf16 v[128:131], a[172:175], a[140:143], v[128:131]\n\t"
    "v_exp_f32 v151, v151\n\t"
    "v_mfma_f32_16x16x32_bf16 v[132:135], a[176:179], a[128:131], 0\n\t"
    "v_cvt_pk_bf16_f32 v144, v144, v145\n\t"
    "v_cvt_pk_bf16_f32 v145, v146, v147\n\t"
    "v_mfma_f32_16x16x32_bf16 v[132:135], a[180:183], a[132:135], v[132:135]\n\t"
    "v_cvt_pk_bf16_f32 v146, v148, v149\n\t"
    "v_cvt_pk_bf16_f32 v147, v150, v151\n\t"
    "v_mfma_f32_16x16x32_bf16 v[132:135], a[184:187], a[136:139], v[132:135]\n\t"
    "v_fma_f32 v152, v152, s60, v171\n\t"
    "v_fma_f32 v153, v153, s60, v171\n\t"
    "v_mfma_f32_16x16x32_bf16 v[132:135], a[188:191], a[140:143], v[132:135]\n\t"
    "buffer_load_dwordx4 v226, s[40:43], s64 offen offset:2048 lds\n\t"
    "v_mfma_f32_16x16x32_bf16 a[232:235], a[248:251], v[144:147], a[232:235]\n\t"
    "v_exp_f32 v152, v152\n\t"
    "v_mfma_f32_16x16x32_bf16 v[64:67], a[192:195], v[144:147], v[64:67]\n\t"
    "v_exp_f32 v153, v153\n\t"
    "v_mfma_f32_16x16x32_bf16 v[68:71], a[196:199], v[144:147], v[68:71]\n\t"
    "v_fma_f32 v154, v154, s60, v171\n\t"
    "v_fma_f32 v155, v155, s60, v171\n\t"
    "v_mfma_f32_16x16x32_bf16 v[72:75], a[200:203], v[144:147], v[72:75]\n\t"
    "v_exp_f32 v154, v154\n\t"
    "v_mfma_f32_16x16x32_bf16 v[76:79], a[204:207], v[144:147], v[76:79]\n\t"
    "v_exp_f32 v155, v155\n\t"
    "v_mfma_f32_16x16x32_bf16 v[80:83], a[208:211], v[144:147], v[80:83]\n\t"
    "v_fma_f32 v156, v156, s60, v171\n\t"
    "v_fma_f32 v157, v157, s60, v171\n\t"
    "v_mfma_f32_16x16x32_bf16 v[84:87], a[212:215], v[144:147], v[84:87]\n\t"
    "v_exp_f32 v156, v156\n\t"
    "v_mfma_f32_16x16x32_bf16 v[88:91], a[216:219], v[144:147], v[88:91]\n\t"
    "v_exp_f32 v157, v157\n\t"
    "v_mfma_f32_16x16x32_bf16 v[92:95], a[220:223], v[144:147], v[92:95]\n\t"
    "v_fma_f32 v158, v158, s60, v171\n\t"
    "v_fma_f32 v159, v159, s60, v171\n\t"
    "v_mfma_f32_16x16x32_bf16 v[136:139], a[160:163], a[144:147], 0\n\t"
    "ds_read_b128 a[160:163], v224 offset:8192\n\t"
    "v_cvt_pk_bf16_f32 v152, v152, v153\n\t"
    "v_mfma_f32_16x16x32_bf16 v[136:139], a[164:167], a[148:151], v[136:139]\n\t"
    "v_exp_f32 v158, v158\n\t"
    "v_mfma_f32_16x16x32_bf16 v[136:139], a[168:171], a[152:155], v[136:139]\n\t"
    "ds_read_b128 a[164:167], v224 offset:8704\n\t"
    "v_cvt_pk_bf16_f32 v153, v154, v155\n\t"
    "v_mfma_f32_16x16x32_bf16 v[136:139], a[172:175], a[156:159], v[136:139]\n\t"
    "v_exp_f32 v159, v159\n\t"
    "v_mfma_f32_16x16x32_bf16 v[140:143], a[176:179], a[144:147], 0\n\t"
    "ds_read_b128 a[168:171], v224 offset:9216\n\t"
    "v_cvt_pk_bf16_f32 v154, v156, v157\n\t"
    "v_mfma_f32_16x16x32_bf16 v[140:143], a[180:183], a[148:151], v[140:143]\n\t"
    "v_cvt_pk_bf16_f32 v155, v158, v159\n\t"
    "v_fma_f32 v128, v128, s60, v172\n\t"
    "v_mfma_f32_16x16x32_bf16 v[140:143], a[184:187], a[152:155], v[140:143]\n\t"
    "ds_read_b128 a[172:175], v224 offset:9728\n\t"
    "v_fma_f32 v129, v129, s60, v172\n\t"
    "v_mfma_f32_16x16x32_bf16 v[140:143], a[188:191], a[156:159], v[140:143]\n\t"
    "v_exp_f32 v128, v128\n\t"
    "v_mfma_f32_16x16x32_bf16 a[236:239], a[248:251], v[152:155], a[236:239]\n\t"
    "ds_read_b128 a[176:179], v224 offset:12288\n\t"
    "v_fma_f32 v130, v130, s60, v172\n\t"
    "v_mfma_f32_16x16x32_bf16 v[96:99], a[192:195], v[152:155], v[96:99]\n\t"
    "v_exp_f32 v129, v129\n\t"
    "v_mfma_f32_16x16x32_bf16 v[100:103], a[196:199], v[152:155], v[100:103]\n\t"
    "ds_read_b128 a[180:183], v224 offset:12800\n\t"
    "v_fma_f32 v131, v131, s60, v172\n\t"
    "v_mfma_f32_16x16x32_bf16 v[104:107], a[200:203], v[152:155], v[104:107]\n\t"
    "v_exp_f32 v130, v130\n\t"
    "v_mfma_f32_16x16x32_bf16 v[108:111], a[204:207], v[152:155], v[108:111]\n\t"
    "ds_read_b128 a[184:187], v224 offset:13312\n\t"
    "v_fma_f32 v132, v132, s60, v172\n\t"
    "v_mfma_f32_16x16x32_bf16 v[112:115], a[208:211], v[152:155], v[112:115]\n\t"
    "ds_read_b128 a[188:191], v224 offset:13824\n\t"
    "v_fma_f32 v133, v133, s60, v172\n\t"
    "v_mfma_f32_16x16x32_bf16 v[116:119], a[212:215], v[152:155], v[116:119]\n\t"
    "buffer_store_dwordx4 a[12:15], v228, s[48:51], s66 offen offset:3072 nt\n\t"
    "v_mfma_f32_16x16x32_bf16 v[120:123], a[216:219], v[152:155], v[120:123]\n\t"
    "v_exp_f32 v131, v131\n\t"
    "v_mfma_f32_16x16x32_bf16 v[124:127], a[220:223], v[152:155], v[124:127]\n\t"
    "s_waitcnt lgkmcnt(6)\n\t"
    "v_exp_f32 v132, v132\n\t"
    "v_mfma_f32_16x16x32_bf16 v[144:147], a[160:163], a[64:67], 0\n\t"
    "v_exp_f32 v133, v133\n\t"
    "v_mfma_f32_16x16x32_bf16 v[144:147], a[164:167], a[68:71], v[144:147]\n\t"
    "s_waitcnt lgkmcnt(4)\n\t"
    "v_fma_f32 v134, v134, s60, v172\n\t"
    "v_fma_f32 v135, v135, s60, v172\n\t"
    "v_mfma_f32_16x16x32_bf16 v[144:147], a[168:171], a[72:75], v[144:147]\n\t"
    "v_exp_f32 v134, v134\n\t"
    "v_mfma_f32_16x16x32_bf16 v[144:147], a[172:175], a[76:79], v[144:147]\n\t"
    "s_waitcnt lgkmcnt(2)\n\t"
    "v_exp_f32 v135, v135\n\t"
    "v_mfma_f32_16x16x32_bf16 v[148:151], a[176:179], a[64:67], 0\n\t"
    "v_cvt_pk_bf16_f32 v128, v128, v129\n\t"
    "v_cvt_pk_bf16_f32 v129, v130, v131\n\t"
    "v_mfma_f32_16x16x32_bf16 v[148:151], a[180:183], a[68:71], v[148:151]\n\t"
    "s_waitcnt lgkmcnt(0)\n\t"
    "v_cvt_pk_bf16_f32 v130, v132, v133\n\t"
    "v_cvt_pk_bf16_f32 v131, v134, v135\n\t"
    "v_mfma_f32_16x16x32_bf16 v[148:151], a[184:187], a[72:75], v[148:151]\n\t"
    "v_fma_f32 v136, v136, s60, v173\n\t"
    "v_fma_f32 v137, v137, s60, v173\n\t"
    "v_mfma_f32_16x16x32_bf16 v[148:151], a[188:191], a[76:79], v[148:151]\n\t"
    "v_exp_f32 v136, v136\n\t"
    "v_mfma_f32_16x16x32_bf16 a[240:243], a[248:251], v[128:131], a[240:243]\n\t"
    "v_exp_f32 v137, v137\n\t"
    "v_mfma_f32_16x16x32_bf16 v[208:211], a[192:195], v[128:131], v[208:211]\n\t"
    "v_fma_f32 v138, v138, s60, v173\n\t"
    "v_fma_f32 v139, v139, s60, v173\n\t"
    "v_mfma_f32_16x16x32_bf16 v[212:215], a[196:199], v[128:131], v[212:215]\n\t"
    "v_exp_f32 v138, v138\n\t"
    "v_mfma_f32_16x16x32_bf16 v[216:219], a[200:203], v[128:131], v[216:219]\n\t"
    "v_exp_f32 v139, v139\n\t"
    "v_mfma_f32_16x16x32_bf16 v[220:223], a[204:207], v[128:131], v[220:223]\n\t"
    "v_fma_f32 v140, v140, s60, v173\n\t"
    "v_fma_f32 v141, v141, s60, v173\n\t"
    "v_mfma_f32_16x16x32_bf16 v[240:243], a[208:211], v[128:131], v[240:243]\n\t"
    "v_exp_f32 v140, v140\n\t"
    "v_mfma_f32_16x16x32_bf16 v[244:247], a[212:215], v[128:131], v[244:247]\n\t"
    "v_exp_f32 v141, v141\n\t"
    "v_mfma_f32_16x16x32_bf16 v[248:251], a[216:219], v[128:131], v[248:251]\n\t"
    "v_fma_f32 v142, v142, s60, v173\n\t"
    "v_fma_f32 v143, v143, s60, v173\n\t"
    "v_mfma_f32_16x16x32_bf16 v[252:255], a[220:223], v[128:131], v[252:255]\n\t"
    "v_exp_f32 v142, v142\n\t"
    "v_mfma_f32_16x16x32_bf16 v[152:155], a[160:163], a[80:83], 0\n\t"
    "v_exp_f32 v143, v143\n\t"
    "v_mfma_f32_16x16x32_bf16 v[152:155], a[164:167], a[84:87], v[152:155]\n\t"
    "v_cvt_pk_bf16_f32 v136, v136, v137\n\t"
    "v_cvt_pk_bf16_f32 v137, v138, v139\n\t"
    "v_mfma_f32_16x16x32_bf16 v[152:155], a[168:171], a[88:91], v[152:155]\n\t"
    "v_cvt_pk_bf16_f32 v138, v140, v141\n\t"
    "v_cvt_pk_bf16_f32 v139, v142, v143\n\t"
    "v_mfma_f32_16x16x32_bf16 v[152:155], a[172:175], a[92:95], v[152:155]\n\t"
    "v_fma_f32 v144, v144, s60, v168\n\t"
    "v_fma_f32 v145, v145, s60, v168\n\t"
    "v_mfma_f32_16x16x32_bf16 v[156:159], a[176:179], a[80:83], 0\n\t"
    "v_exp_f32 v144, v144\n\t"
    "v_mfma_f32_16x16x32_bf16 v[156:159], a[180:183], a[84:87], v[156:159]\n\t"
    "v_exp_f32 v145, v145\n\t"
    "v_mfma_f32_16x16x32_bf16 v[156:159], a[184:187], a[88:91], v[156:159]\n\t"
    "v_fma_f32 v146, v146, s60, v168\n\t"
    "v_fma_f32 v147, v147, s60, v168\n\t"
    "v_mfma_f32_16x16x32_bf16 v[156:159], a[188:191], a[92:95], v[156:159]\n\t"
    "v_exp_f32 v146, v146\n\t"
    "v_mfma_f32_16x16x32_bf16 a[244:247], a[248:251], v[136:139], a[244:247]\n\t"
    "v_exp_f32 v147, v147\n\t"
    "v_mfma_f32_16x16x32_bf16 v[176:179], a[192:195], v[136:139], v[176:179]\n\t"
    "v_fma_f32 v148, v148, s60, v168\n\t"
    "v_fma_f32 v149, v149, s60, v168\n\t"
    "v_mfma_f32_16x16x32_bf16 v[180:183], a[196:199], v[136:139], v[180:183]\n\t"
    "v_exp_f32 v148, v148\n\t"
    "v_mfma_f32_16x16x32_bf16 v[184:187], a[200:203], v[136:139], v[184:187]\n\t"
    "v_exp_f32 v149, v149\n\t"
    "v_mfma_f32_16x16x32_bf16 v[188:191], a[204:207], v[136:139], v[188:191]\n\t"
    "v_fma_f32 v150, v150, s60, v168\n\t"
    "v_fma_f32 v151, v151, s60, v168\n\t"
    "v_mfma_f32_16x16x32_bf16 v[192:195], a[208:211], v[136:139], v[192:195]\n\t"
    "ds_read_b64_tr_b16 a[208:209], v225 offset:9216\n\t"
    "v_cvt_pk_bf16_f32 v144, v144, v145\n\t"
    "v_mfma_f32_16x16x32_bf16 v[196:199], a[212:215], v[136:139], v[196:199]\n\t"
    "v_exp_f32 v150, v150\n\t"
    "v_mfma_f32_16x16x32_bf16 v[200:203], a[216:219], v[136:139], v[200:203]\n\t"
    "ds_read_b64_tr_b16 a[210:211], v225 offset:13312\n\t"
    "v_cvt_pk_bf16_f32 v145, v146, v147\n\t"
    "v_mfma_f32_16x16x32_bf16 v[204:207], a[220:223], v[136:139], v[204:207]\n\t"
    "v_exp_f32 v151, v151\n\t"
    "v_mfma_f32_16x16x32_bf16 v[128:131], a[160:163], a[96:99], 0\n\t"
    "ds_read_b64_tr_b16 a[212:213], v225 offset:9224\n\t"
    "v_cvt_pk_bf16_f32 v146, v148, v149\n\t"
    "v_mfma_f32_16x16x32_bf16 v[128:131], a[164:167], a[100:103], v[128:131]\n\t"
    "v_cvt_pk_bf16_f32 v147, v150, v151\n\t"
    "v_fma_f32 v152, v152, s60, v169\n\t"
    "v_mfma_f32_16x16x32_bf16 v[128:131], a[168:171], a[104:107], v[128:131]\n\t"
    "ds_read_b64_tr_b16 a[214:215], v225 offset:13320\n\t"
    "v_fma_f32 v153, v153, s60, v169\n\t"
    "v_mfma_f32_16x16x32_bf16 v[128:131], a[172:175], a[108:111], v[128:131]\n\t"
    "ds_read_b64_tr_b16 a[216:217], v225 offset:9728\n\t"
    "v_fma_f32 v154, v154, s60, v169\n\t"
    "v_mfma_f32_16x16x32_bf16 v[132:135], a[176:179], a[96:99], 0\n\t"
    "ds_read_b64_tr_b16 a[218:219], v225 offset:13824\n\t"
    "v_fma_f32 v155, v155, s60, v169\n\t"
    "v_mfma_f32_16x16x32_bf16 v[132:135], a[180:183], a[100:103], v[132:135]\n\t"
    "ds_read_b64_tr_b16 a[220:221], v225 offset:9736\n\t"
    "v_fma_f32 v156, v156, s60, v169\n\t"
    "v_mfma_f32_16x16x32_bf16 v[132:135], a[184:187], a[104:107], v[132:135]\n\t"
    "ds_read_b64_tr_b16 a[222:223], v225 offset:13832\n\t"
    "v_fma_f32 v157, v157, s60, v169\n\t"
    "v_mfma_f32_16x16x32_bf16 v[132:135], a[188:191], a[108:111], v[132:135]\n\t"
    "v_exp_f32 v152, v152\n\t"
    "v_mfma_f32_16x16x32_bf16 a[224:227], a[248:251], v[144:147], a[224:227]\n\t"
    "s_waitcnt lgkmcnt(15)\n\t"
    "v_exp_f32 v153, v153\n\t"
    "v_mfma_f32_16x16x32_bf16 v[0:3], a[32:35], v[144:147], v[0:3]\n\t"
    "v_exp_f32 v154, v154\n\t"
    "v_mfma_f32_16x16x32_bf16 v[4:7], a[36:39], v[144:147], v[4:7]\n\t"
    "v_exp_f32 v155, v155\n\t"
    "v_mfma_f32_16x16x32_bf16 v[8:11], a[40:43], v[144:147], v[8:11]\n\t"
    "v_exp_f32 v156, v156\n\t"
    "v_mfma_f32_16x16x32_bf16 v[12:15], a[44:47], v[144:147], v[12:15]\n\t"
    "s_waitcnt lgkmcnt(4)\n\t"
    "v_exp_f32 v157, v157\n\t"
    "v_mfma_f32_16x16x32_bf16 v[16:19], a[208:211], v[144:147], v[16:19]\n\t"
    "v_fma_f32 v158, v158, s60, v169\n\t"
    "v_fma_f32 v159, v159, s60, v169\n\t"
    "v_cvt_pk_bf16_f32 v152, v152, v153\n\t"
    "v_cvt_pk_bf16_f32 v153, v154, v155\n\t"
    "v_mfma_f32_16x16x32_bf16 v[20:23], a[212:215], v[144:147], v[20:23]\n\t"
    "s_waitcnt lgkmcnt(0)\n\t"
    "v_exp_f32 v158, v158\n\t"
    "v_exp_f32 v159, v159\n\t"
    "v_mfma_f32_16x16x32_bf16 v[24:27], a[216:219], v[144:147], v[24:27]\n\t"
    "v_cvt_pk_bf16_f32 v154, v156, v157\n\t"
    "v_cvt_pk_bf16_f32 v155, v158, v159\n\t"
    "v_mfma_f32_16x16x32_bf16 v[28:31], a[220:223], v[144:147], v[28:31]\n\t"
    "v_fma_f32 v128, v128, s60, v170\n\t"
    "v_fma_f32 v129, v129, s60, v170\n\t"
    "v_mfma_f32_16x16x32_bf16 v[136:139], a[160:163], a[112:115], 0\n\t"
    "ds_read_b128 a[0:3], v232 offset:8192\n\t"
    "s_waitcnt vmcnt(16) lgkmcnt(1)\n\t"
    "v_mfma_f32_16x16x32_bf16 v[136:139], a[164:167], a[116:119], v[136:139]\n\t"
    "s_barrier\n\t"
    "v_exp_f32 v128, v128\n\t"
    "v_mfma_f32_16x16x32_bf16 v[136:139], a[168:171], a[120:123], v[136:139]\n\t"
    "ds_read_b64_tr_b16 a[192:193], v225 offset:16384\n\t"
    "v_fma_f32 v130, v130, s60, v170\n\t"
    "v_mfma_f32_16x16x32_bf16 v[136:139], a[172:175], a[124:127], v[136:139]\n\t"
    "ds_read_b128 a[4:7], v232 offset:9216\n\t"
    "s_xor_b32 s64, s52, 73728\n\t"
    "v_mfma_f32_16x16x32_bf16 v[140:143], a[176:179], a[112:115], 0\n\t"
    "ds_read_b64_tr_b16 a[194:195], v225 offset:20480\n\t"
    "v_fma_f32 v131, v131, s60, v170\n\t"
    "v_mfma_f32_16x16x32_bf16 v[140:143], a[180:183], a[116:119], v[140:143]\n\t"
    "s_add_u32 m0, s63, 8192\n\t"
    "v_exp_f32 v129, v129\n\t"
    "v_mfma_f32_16x16x32_bf16 v[140:143], a[184:187], a[120:123], v[140:143]\n\t"
    "ds_read_b64_tr_b16 a[196:197], v225 offset:16392\n\t"
    "v_fma_f32 v132, v132, s60, v170\n\t"
    "v_mfma_f32_16x16x32_bf16 v[140:143], a[188:191], a[124:127], v[140:143]\n\t"
    "buffer_load_dwordx4 v226, s[36:39], s64 offen offset:0 lds\n\t"
    "v_mfma_f32_16x16x32_bf16 a[228:231], a[248:251], v[152:155], a[228:231]\n\t"
    "ds_read_b64_tr_b16 a[198:199], v225 offset:20488\n\t"
    "v_fma_f32 v133, v133, s60, v170\n\t"
    "v_mfma_f32_16x16x32_bf16 v[32:35], a[32:35], v[152:155], v[32:35]\n\t"
    "ds_read_b128 a[8:11], v232 offset:10240\n\t"
    "v_fma_f32 v134, v134, s60, v170\n\t"
    "v_mfma_f32_16x16x32_bf16 v[36:39], a[36:39], v[152:155], v[36:39]\n\t"
    "ds_read_b64_tr_b16 a[200:201], v225 offset:16896\n\t"
    "v_fma_f32 v135, v135, s60, v170\n\t"
    "v_mfma_f32_16x16x32_bf16 v[40:43], a[40:43], v[152:155], v[40:43]\n\t"
    "ds_read_b128 a[12:15], v232 offset:11264\n\t"
    "v_cvt_pk_bf16_f32 v128, v128, v129\n\t"
    "v_mfma_f32_16x16x32_bf16 v[44:47], a[44:47], v[152:155], v[44:47]\n\t"
    "ds_read_b64_tr_b16 a[202:203], v225 offset:20992\n\t"
    "v_fma_f32 v136, v136, s60, v171\n\t"
    "v_exp_f32 v130, v130\n\t"
    "v_mfma_f32_16x16x32_bf16 v[48:51], a[208:211], v[152:155], v[48:51]\n\t"
    "buffer_load_dwordx4 v226, s[36:39], s64 offen offset:2048 lds\n\t"
    "v_exp_f32 v131, v131\n\t"
    "v_mfma_f32_16x16x32_bf16 v[52:55], a[212:215], v[152:155], v[52:55]\n\t"
    "ds_read_b64_tr_b16 a[204:205], v225 offset:16904\n\t"
    "v_cvt_pk_bf16_f32 v129, v130, v131\n\t"
    "v_exp_f32 v132, v132\n\t"
    "v_mfma_f32_16x16x32_bf16 v[56:59], a[216:219], v[152:155], v[56:59]\n\t"
    "v_exp_f32 v133, v133\n\t"
    "v_mfma_f32_16x16x32_bf16 v[60:63], a[220:223], v[152:155], v[60:63]\n\t"
    "ds_read_b64_tr_b16 a[206:207], v225 offset:21000\n\t"
    "v_cvt_pk_bf16_f32 v130, v132, v133\n\t"
    "v_mfma_f32_16x16x32_bf16 v[144:147], a[160:163], a[128:131], 0\n\t"
    "v_exp_f32 v134, v134\n\t"
    "v_mfma_f32_16x16x32_bf16 v[144:147], a[164:167], a[132:135], v[144:147]\n\t"
    "s_add_u32 m0, s63, 40960\n\t"
    "v_exp_f32 v135, v135\n\t"
    "v_mfma_f32_16x16x32_bf16 v[144:147], a[168:171], a[136:139], v[144:147]\n\t"
    "buffer_load_dwordx4 v226, s[40:43], s64 offen offset:0 lds\n\t"
    "v_mfma_f32_16x16x32_bf16 v[144:147], a[172:175], a[140:143], v[144:147]\n\t"
    "v_exp_f32 v136, v136\n\t"
    "v_mfma_f32_16x16x32_bf16 v[148:151], a[176:179], a[128:131], 0\n\t"
    "v_cvt_pk_bf16_f32 v131, v134, v135\n\t"
    "v_fma_f32 v137, v137, s60, v171\n\t"
    "v_mfma_f32_16x16x32_bf16 v[148:151], a[180:183], a[132:135], v[148:151]\n\t"
    "v_exp_f32 v137, v137\n\t"
    "v_mfma_f32_16x16x32_bf16 v[148:151], a[184:187], a[136:139], v[148:151]\n\t"
    "s_waitcnt lgkmcnt(3)\n\t"
    "v_fma_f32 v138, v138, s60, v171\n\t"
    "v_fma_f32 v139, v139, s60, v171\n\t"
    "v_mfma_f32_16x16x32_bf16 v[148:151], a[188:191], a[140:143], v[148:151]\n\t"
    "buffer_load_dwordx4 v226, s[40:43], s64 offen offset:2048 lds\n\t"
    "v_mfma_f32_16x16x32_bf16 a[232:235], a[248:251], v[128:131], a[232:235]\n\t"
    "s_add_u32 s65, s53, 8192\n\t"
    "v_exp_f32 v138, v138\n\t"
    "v_mfma_f32_16x16x32_bf16 v[64:67], a[32:35], v[128:131], v[64:67]\n\t"
    "s_add_u32 m0, s69, 8192\n\t"
    "v_exp_f32 v139, v139\n\t"
    "v_mfma_f32_16x16x32_bf16 v[68:71], a[36:39], v[128:131], v[68:71]\n\t"
    "buffer_load_dwordx4 v226, s[44:47], s65 offen offset:0 nt lds\n\t"
    "v_mfma_f32_16x16x32_bf16 v[72:75], a[40:43], v[128:131], v[72:75]\n\t"
    "s_add_u32 s66, s54, 8192\n\t"
    "v_fma_f32 v140, v140, s60, v171\n\t"
    "v_fma_f32 v141, v141, s60, v171\n\t"
    "v_mfma_f32_16x16x32_bf16 v[76:79], a[44:47], v[128:131], v[76:79]\n\t"
    "buffer_load_dwordx4 v226, s[44:47], s65 offen offset:2048 nt lds\n\t"
    "v_mfma_f32_16x16x32_bf16 v[80:83], a[208:211], v[128:131], v[80:83]\n\t"
    "buffer_store_dwordx4 a[0:3], v227, s[48:51], s66 offen offset:0 nt\n\t"
    "v_mfma_f32_16x16x32_bf16 v[84:87], a[212:215], v[128:131], v[84:87]\n\t"
    "v_exp_f32 v140, v140\n\t"
    "v_fma_f32 v142, v142, s60, v171\n\t"
    "v_fma_f32 v143, v143, s60, v171\n\t"
    "v_mfma_f32_16x16x32_bf16 v[88:91], a[216:219], v[128:131], v[88:91]\n\t"
    "v_exp_f32 v141, v141\n\t"
    "v_mfma_f32_16x16x32_bf16 v[92:95], a[220:223], v[128:131], v[92:95]\n\t"
    "v_exp_f32 v142, v142\n\t"
    "v_mfma_f32_16x16x32_bf16 v[152:155], a[160:163], a[144:147], 0\n\t"
    "ds_read_b128 a[160:163], v224 offset:16384\n\t"
    "v_cvt_pk_bf16_f32 v136, v136, v137\n\t"
    "v_mfma_f32_16x16x32_bf16 v[152:155], a[164:167], a[148:151], v[152:155]\n\t"
    "v_exp_f32 v143, v143\n\t"
    "v_mfma_f32_16x16x32_bf16 v[152:155], a[168:171], a[152:155], v[152:155]\n\t"
    "ds_read_b128 a[164:167], v224 offset:16896\n\t"
    "v_cvt_pk_bf16_f32 v137, v138, v139\n\t"
    "v_mfma_f32_16x16x32_bf16 v[152:155], a[172:175], a[156:159], v[152:155]\n\t"
    "v_cvt_pk_bf16_f32 v138, v140, v141\n\t"
    "v_cvt_pk_bf16_f32 v139, v142, v143\n\t"
    "v_mfma_f32_16x16x32_bf16 v[156:159], a[176:179], a[144:147], 0\n\t"
    "ds_read_b128 a[168:171], v224 offset:17408\n\t"
    "v_fma_f32 v144, v144, s60, v172\n\t"
    "v_mfma_f32_16x16x32_bf16 v[156:159], a[180:183], a[148:151], v[156:159]\n\t"
    "s_add_u32 s65, s53, 8320\n\t"
    "v_exp_f32 v144, v144\n\t"
    "v_mfma_f32_16x16x32_bf16 v[156:159], a[184:187], a[152:155], v[156:159]\n\t"
    "ds_read_b128 a[172:175], v224 offset:17920\n\t"
    "v_fma_f32 v145, v145, s60, v172\n\t"
    "v_mfma_f32_16x16x32_bf16 v[156:159], a[188:191], a[156:159], v[156:159]\n\t"
    "s_add_u32 m0, s69, 9216\n\t"
    "v_exp_f32 v145, v145\n\t"
    "v_mfma_f32_16x16x32_bf16 a[236:239], a[248:251], v[136:139], a[236:239]\n\t"
    "ds_read_b128 a[176:179], v224 offset:20480\n\t"
    "v_fma_f32 v146, v146, s60, v172\n\t"
    "v_mfma_f32_16x16x32_bf16 v[96:99], a[32:35], v[136:139], v[96:99]\n\t"
    "buffer_load_dwordx4 v226, s[44:47], s65 offen offset:0 nt lds\n\t"
    "v_mfma_f32_16x16x32_bf16 v[100:103], a[36:39], v[136:139], v[100:103]\n\t"
    "ds_read_b128 a[180:183], v224 offset:20992\n\t"
    "v_fma_f32 v147, v147, s60, v172\n\t"
    "v_mfma_f32_16x16x32_bf16 v[104:107], a[40:43], v[136:139], v[104:107]\n\t"
    "buffer_load_dwordx4 v226, s[44:47], s65 offen offset:2048 nt lds\n\t"
    "v_mfma_f32_16x16x32_bf16 v[108:111], a[44:47], v[136:139], v[108:111]\n\t"
    "ds_read_b128 a[184:187], v224 offset:21504\n\t"
    "v_fma_f32 v148, v148, s60, v172\n\t"
    "v_mfma_f32_16x16x32_bf16 v[112:115], a[208:211], v[136:139], v[112:115]\n\t"
    "ds_read_b128 a[188:191], v224 offset:22016\n\t"
    "v_fma_f32 v149, v149, s60, v172\n\t"
    "v_mfma_f32_16x16x32_bf16 v[116:119], a[212:215], v[136:139], v[116:119]\n\t"
    "v_exp_f32 v146, v146\n\t"
    "v_mfma_f32_16x16x32_bf16 v[120:123], a[216:219], v[136:139], v[120:123]\n\t"
    "v_exp_f32 v147, v147\n\t"
    "v_mfma_f32_16x16x32_bf16 v[124:127], a[220:223], v[136:139], v[124:127]\n\t"
    "s_waitcnt lgkmcnt(6)\n\t"
    "v_exp_f32 v148, v148\n\t"
    "v_mfma_f32_16x16x32_bf16 v[128:131], a[160:163], a[64:67], 0\n\t"
    "v_exp_f32 v149, v149\n\t"
    "v_mfma_f32_16x16x32_bf16 v[128:131], a[164:167], a[68:71], v[128:131]\n\t"
    "s_waitcnt lgkmcnt(4)\n\t"
    "v_fma_f32 v150, v150, s60, v172\n\t"
    "v_fma_f32 v151, v151, s60, v172\n\t"
    "v_mfma_f32_16x16x32_bf16 v[128:131], a[168:171], a[72:75], v[128:131]\n\t"
    "v_exp_f32 v150, v150\n\t"
    "v_mfma_f32_16x16x32_bf16 v[128:131], a[172:175], a[76:79], v[128:131]\n\t"
    "s_waitcnt lgkmcnt(2)\n\t"
    "v_exp_f32 v151, v151\n\t"
    "v_mfma_f32_16x16x32_bf16 v[132:135], a[176:179], a[64:67], 0\n\t"
    "v_cvt_pk_bf16_f32 v144, v144, v145\n\t"
    "v_cvt_pk_bf16_f32 v145, v146, v147\n\t"
    "v_mfma_f32_16x16x32_bf16 v[132:135], a[180:183], a[68:71], v[132:135]\n\t"
    "s_waitcnt lgkmcnt(0)\n\t"
    "v_cvt_pk_bf16_f32 v146, v148, v149\n\t"
    "v_cvt_pk_bf16_f32 v147, v150, v151\n\t"
    "v_mfma_f32_16x16x32_bf16 v[132:135], a[184:187], a[72:75], v[132:135]\n\t"
    "v_fma_f32 v152, v152, s60, v173\n\t"
    "v_fma_f32 v153, v153, s60, v173\n\t"
    "v_mfma_f32_16x16x32_bf16 v[132:135], a[188:191], a[76:79], v[132:135]\n\t"
    "v_exp_f32 v152, v152\n\t"
    "v_mfma_f32_16x16x32_bf16 a[240:243], a[248:251], v[144:147], a[240:243]\n\t"
    "v_exp_f32 v153, v153\n\t"
    "v_mfma_f32_16x16x32_bf16 v[208:211], a[32:35], v[144:147], v[208:211]\n\t"
    "v_fma_f32 v154, v154, s60, v173\n\t"
    "v_fma_f32 v155, v155, s60, v173\n\t"
    "v_mfma_f32_16x16x32_bf16 v[212:215], a[36:39], v[144:147], v[212:215]\n\t"
    "v_exp_f32 v154, v154\n\t"
    "v_mfma_f32_16x16x32_bf16 v[216:219], a[40:43], v[144:147], v[216:219]\n\t"
    "v_exp_f32 v155, v155\n\t"
    "v_mfma_f32_16x16x32_bf16 v[220:223], a[44:47], v[144:147], v[220:223]\n\t"
    "v_fma_f32 v156, v156, s60, v173\n\t"
    "v_fma_f32 v157, v157, s60, v173\n\t"
    "v_mfma_f32_16x16x32_bf16 v[240:243], a[208:211], v[144:147], v[240:243]\n\t"
    "v_exp_f32 v156, v156\n\t"
    "v_mfma_f32_16x16x32_bf16 v[244:247], a[212:215], v[144:147], v[244:247]\n\t"
    "v_exp_f32 v157, v157\n\t"
    "v_mfma_f32_16x16x32_bf16 v[248:251], a[216:219], v[144:147], v[248:251]\n\t"
    "v_fma_f32 v158, v158, s60, v173\n\t"
    "v_fma_f32 v159, v159, s60, v173\n\t"
    "v_mfma_f32_16x16x32_bf16 v[252:255], a[220:223], v[144:147], v[252:255]\n\t"
    "v_exp_f32 v158, v158\n\t"
    "v_mfma_f32_16x16x32_bf16 v[136:139], a[160:163], a[80:83], 0\n\t"
    "v_exp_f32 v159, v159\n\t"
    "v_mfma_f32_16x16x32_bf16 v[136:139], a[164:167], a[84:87], v[136:139]\n\t"
    "v_cvt_pk_bf16_f32 v152, v152, v153\n\t"
    "v_cvt_pk_bf16_f32 v153, v154, v155\n\t"
    "v_mfma_f32_16x16x32_bf16 v[136:139], a[168:171], a[88:91], v[136:139]\n\t"
    "v_cvt_pk_bf16_f32 v154, v156, v157\n\t"
    "v_cvt_pk_bf16_f32 v155, v158, v159\n\t"
    "v_mfma_f32_16x16x32_bf16 v[136:139], a[172:175], a[92:95], v[136:139]\n\t"
    "v_fma_f32 v128, v128, s60, v168\n\t"
    "v_fma_f32 v129, v129, s60, v168\n\t"
    "v_mfma_f32_16x16x32_bf16 v[140:143], a[176:179], a[80:83], 0\n\t"
    "v_exp_f32 v128, v128\n\t"
    "v_mfma_f32_16x16x32_bf16 v[140:143], a[180:183], a[84:87], v[140:143]\n\t"
    "v_exp_f32 v129, v129\n\t"
    "v_mfma_f32_16x16x32_bf16 v[140:143], a[184:187], a[88:91], v[140:143]\n\t"
    "v_fma_f32 v130, v130, s60, v168\n\t"
    "v_fma_f32 v131, v131, s60, v168\n\t"
    "v_mfma_f32_16x16x32_bf16 v[140:143], a[188:191], a[92:95], v[140:143]\n\t"
    "v_exp_f32 v130, v130\n\t"
    "v_mfma_f32_16x16x32_bf16 a[244:247], a[248:251], v[152:155], a[244:247]\n\t"
    "v_exp_f32 v131, v131\n\t"
    "v_mfma_f32_16x16x32_bf16 v[176:179], a[32:35], v[152:155], v[176:179]\n\t"
    "v_fma_f32 v132, v132, s60, v168\n\t"
    "v_fma_f32 v133, v133, s60, v168\n\t"
    "v_mfma_f32_16x16x32_bf16 v[180:183], a[36:39], v[152:155], v[180:183]\n\t"
    "v_exp_f32 v132, v132\n\t"
    "v_mfma_f32_16x16x32_bf16 v[184:187], a[40:43], v[152:155], v[184:187]\n\t"
    "v_exp_f32 v133, v133\n\t"
    "v_mfma_f32_16x16x32_bf16 v[188:191], a[44:47], v[152:155], v[188:191]\n\t"
    "v_fma_f32 v134, v134, s60, v168\n\t"
    "v_fma_f32 v135, v135, s60, v168\n\t"
    "v_mfma_f32_16x16x32_bf16 v[192:195], a[208:211], v[152:155], v[192:195]\n\t"
    "ds_read_b64_tr_b16 a[208:209], v225 offset:17408\n\t"
    "v_cvt_pk_bf16_f32 v128, v128, v129\n\t"
    "v_mfma_f32_16x16x32_bf16 v[196:199], a[212:215], v[152:155], v[196:199]\n\t"
    "buffer_store_dwordx4 a[4:7], v228, s[48:51], s66 offen offset:1024 nt\n\t"
    "v_mfma_f32_16x16x32_bf16 v[200:203], a[216:219], v[152:155], v[200:203]\n\t"
    "ds_read_b64_tr_b16 a[210:211], v225 offset:21504\n\t"
    "v_cvt_pk_bf16_f32 v129, v130, v131\n\t"
    "v_mfma_f32_16x16x32_bf16 v[204:207], a[220:223], v[152:155], v[204:207]\n\t"
    "v_exp_f32 v134, v134\n\t"
    "v_mfma_f32_16x16x32_bf16 v[144:147], a[160:163], a[96:99], 0\n\t"
    "ds_read_b64_tr_b16 a[212:213], v225 offset:17416\n\t"
    "v_cvt_pk_bf16_f32 v130, v132, v133\n\t"
    "v_mfma_f32_16x16x32_bf16 v[144:147], a[164:167], a[100:103], v[144:147]\n\t"
    "v_exp_f32 v135, v135\n\t"
    "v_mfma_f32_16x16x32_bf16 v[144:147], a[168:171], a[104:107], v[144:147]\n\t"
    "ds_read_b64_tr_b16 a[214:215], v225 offset:21512\n\t"
    "v_cvt_pk_bf16_f32 v131, v134, v135\n\t"
    "v_mfma_f32_16x16x32_bf16 v[144:147], a[172:175], a[108:111], v[144:147]\n\t"
    "ds_read_b64_tr_b16 a[216:217], v225 offset:17920\n\t"
    "v_fma_f32 v136, v136, s60, v169\n\t"
    "v_mfma_f32_16x16x32_bf16 v[148:151], a[176:179], a[96:99], 0\n\t"
    "ds_read_b64_tr_b16 a[218:219], v225 offset:22016\n\t"
    "v_fma_f32 v137, v137, s60, v169\n\t"
    "v_mfma_f32_16x16x32_bf16 v[148:151], a[180:183], a[100:103], v[148:151]\n\t"
    "ds_read_b64_tr_b16 a[220:221], v225 offset:17928\n\t"
    "v_fma_f32 v138, v138, s60, v169\n\t"
    "v_mfma_f32_16x16x32_bf16 v[148:151], a[184:187], a[104:107], v[148:151]\n\t"
    "ds_read_b64_tr_b16 a[222:223], v225 offset:22024\n\t"
    "v_fma_f32 v139, v139, s60, v169\n\t"
    "v_mfma_f32_16x16x32_bf16 v[148:151], a[188:191], a[108:111], v[148:151]\n\t"
    "v_exp_f32 v136, v136\n\t"
    "v_mfma_f32_16x16x32_bf16 a[224:227], a[248:251], v[128:131], a[224:227]\n\t"
    "s_waitcnt lgkmcnt(15)\n\t"
    "v_exp_f32 v137, v137\n\t"
    "v_mfma_f32_16x16x32_bf16 v[0:3], a[192:195], v[128:131], v[0:3]\n\t"
    "v_exp_f32 v138, v138\n\t"
    "v_mfma_f32_16x16x32_bf16 v[4:7], a[196:199], v[128:131], v[4:7]\n\t"
    "v_exp_f32 v139, v139\n\t"
    "v_mfma_f32_16x16x32_bf16 v[8:11], a[200:203], v[128:131], v[8:11]\n\t"
    "v_fma_f32 v140, v140, s60, v169\n\t"
    "v_fma_f32 v141, v141, s60, v169\n\t"
    "v_mfma_f32_16x16x32_bf16 v[12:15], a[204:207], v[128:131], v[12:15]\n\t"
    "s_waitcnt lgkmcnt(4)\n\t"
    "v_exp_f32 v140, v140\n\t"
    "v_mfma_f32_16x16x32_bf16 v[16:19], a[208:211], v[128:131], v[16:19]\n\t"
    "v_exp_f32 v141, v141\n\t"
    "v_mfma_f32_16x16x32_bf16 v[20:23], a[212:215], v[128:131], v[20:23]\n\t"
    "s_waitcnt lgkmcnt(0)\n\t"
    "v_fma_f32 v142, v142, s60, v169\n\t"
    "v_fma_f32 v143, v143, s60, v169\n\t"
    "v_mfma_f32_16x16x32_bf16 v[24:27], a[216:219], v[128:131], v[24:27]\n\t"
    "v_exp_f32 v142, v142\n\t"
    "v_mfma_f32_16x16x32_bf16 v[28:31], a[220:223], v[128:131], v[28:31]\n\t"
    "v_exp_f32 v143, v143\n\t"
    "v_mfma_f32_16x16x32_bf16 v[152:155], a[160:163], a[112:115], 0\n\t"
    "s_waitcnt vmcnt(19) lgkmcnt(0)\n\t"
    "v_cvt_pk_bf16_f32 v136, v136, v137\n\t"
    "v_cvt_pk_bf16_f32 v137, v138, v139\n\t"
    "v_mfma_f32_16x16x32_bf16 v[152:155], a[164:167], a[116:119], v[152:155]\n\t"
    "s_barrier\n\t"
    "v_cvt_pk_bf16_f32 v138, v140, v141\n\t"
    "v_cvt_pk_bf16_f32 v139, v142, v143\n\t"
    "v_mfma_f32_16x16x32_bf16 v[152:155], a[168:171], a[120:123], v[152:155]\n\t"
    "ds_read_b64_tr_b16 a[32:33], v225 offset:24576\n\t"
    "v_fma_f32 v144, v144, s60, v170\n\t"
    "v_mfma_f32_16x16x32_bf16 v[152:155], a[172:175], a[124:127], v[152:155]\n\t"
    "s_xor_b32 s64, s52, 81920\n\t"
    "v_exp_f32 v144, v144\n\t"
    "v_mfma_f32_16x16x32_bf16 v[156:159], a[176:179], a[112:115], 0\n\t"
    "ds_read_b64_tr_b16 a[34:35], v225 offset:28672\n\t"
    "v_fma_f32 v145, v145, s60, v170\n\t"
    "v_mfma_f32_16x16x32_bf16 v[156:159], a[180:183], a[116:119], v[156:159]\n\t"
    "s_add_u32 m0, s63, 16384\n\t"
    "v_exp_f32 v145, v145\n\t"
    "v_mfma_f32_16x16x32_bf16 v[156:159], a[184:187], a[120:123], v[156:159]\n\t"
    "ds_read_b64_tr_b16 a[36:37], v225 offset:24584\n\t"
    "v_fma_f32 v146, v146, s60, v170\n\t"
    "v_mfma_f32_16x16x32_bf16 v[156:159], a[188:191], a[124:127], v[156:159]\n\t"
    "buffer_load_dwordx4 v226, s[36:39], s64 offen offset:0 lds\n\t"
    "v_mfma_f32_16x16x32_bf16 a[228:231], a[248:251], v[136:139], a[228:231]\n\t"
    "ds_read_b64_tr_b16 a[38:39], v225 offset:28680\n\t"
    "v_fma_f32 v147, v147, s60, v170\n\t"
    "v_mfma_f32_16x16x32_bf16 v[32:35], a[192:195], v[136:139], v[32:35]\n\t"
    "v_exp_f32 v146, v146\n\t"
    "v_mfma_f32_16x16x32_bf16 v[36:39], a[196:199], v[136:139], v[36:39]\n\t"
    "ds_read_b64_tr_b16 a[40:41], v225 offset:25088\n\t"
    "v_fma_f32 v148, v148, s60, v170\n\t"
    "v_mfma_f32_16x16x32_bf16 v[40:43], a[200:203], v[136:139], v[40:43]\n\t"
    "v_exp_f32 v147, v147\n\t"
    "v_mfma_f32_16x16x32_bf16 v[44:47], a[204:207], v[136:139], v[44:47]\n\t"
    "ds_read_b64_tr_b16 a[42:43], v225 offset:29184\n\t"
    "v_fma_f32 v149, v149, s60, v170\n\t"
    "v_mfma_f32_16x16x32_bf16 v[48:51], a[208:211], v[136:139], v[48:51]\n\t"
    "buffer_load_dwordx4 v226, s[36:39], s64 offen offset:2048 lds\n\t"
    "v_mfma_f32_16x16x32_bf16 v[52:55], a[212:215], v[136:139], v[52:55]\n\t"
    "ds_read_b64_tr_b16 a[44:45], v225 offset:25096\n\t"
    "v_fma_f32 v150, v150, s60, v170\n\t"
    "v_fma_f32 v151, v151, s60, v170\n\t"
    "v_mfma_f32_16x16x32_bf16 v[56:59], a[216:219], v[136:139], v[56:59]\n\t"
    "v_exp_f32 v148, v148\n\t"
    "v_mfma_f32_16x16x32_bf16 v[60:63], a[220:223], v[136:139], v[60:63]\n\t"
    "ds_read_b64_tr_b16 a[46:47], v225 offset:29192\n\t"
    "v_cvt_pk_bf16_f32 v144, v144, v145\n\t"
    "v_mfma_f32_16x16x32_bf16 v[128:131], a[160:163], a[128:131], 0\n\t"
    "v_exp_f32 v149, v149\n\t"
    "v_mfma_f32_16x16x32_bf16 v[128:131], a[164:167], a[132:135], v[128:131]\n\t"
    "s_add_u32 m0, s63, 49152\n\t"
    "v_exp_f32 v150, v150\n\t"
    "v_mfma_f32_16x16x32_bf16 v[128:131], a[168:171], a[136:139], v[128:131]\n\t"
    "buffer_load_dwordx4 v226, s[40:43], s64 offen offset:0 lds\n\t"
    "v_mfma_f32_16x16x32_bf16 v[128:131], a[172:175], a[140:143], v[128:131]\n\t"
    "v_exp_f32 v151, v151\n\t"
    "v_mfma_f32_16x16x32_bf16 v[132:135], a[176:179], a[128:131], 0\n\t"
    "v_cvt_pk_bf16_f32 v145, v146, v147\n\t"
    "v_cvt_pk_bf16_f32 v146, v148, v149\n\t"
    "v_mfma_f32_16x16x32_bf16 v[132:135], a[180:183], a[132:135], v[132:135]\n\t"
    "v_cvt_pk_bf16_f32 v147, v150, v151\n\t"
    "v_fma_f32 v152, v152, s60, v171\n\t"
    "v_mfma_f32_16x16x32_bf16 v[132:135], a[184:187], a[136:139], v[132:135]\n\t"
    "v_exp_f32 v152, v152\n\t"
    "v_mfma_f32_16x16x32_bf16 v[132:135], a[188:191], a[140:143], v[132:135]\n\t"
    "buffer_load_dwordx4 v226, s[40:43], s64 offen offset:2048 lds\n\t"
    "v_mfma_f32_16x16x32_bf16 a[232:235], a[248:251], v[144:147], a[232:235]\n\t"
    "v_fma_f32 v153, v153, s60, v171\n\t"
    "v_fma_f32 v154, v154, s60, v171\n\t"
    "v_mfma_f32_16x16x32_bf16 v[64:67], a[192:195], v[144:147], v[64:67]\n\t"
    "v_exp_f32 v153, v153\n\t"
    "v_mfma_f32_16x16x32_bf16 v[68:71], a[196:199], v[144:147], v[68:71]\n\t"
    "v_exp_f32 v154, v154\n\t"
    "v_mfma_f32_16x16x32_bf16 v[72:75], a[200:203], v[144:147], v[72:75]\n\t"
    "v_fma_f32 v155, v155, s60, v171\n\t"
    "v_fma_f32 v156, v156, s60, v171\n\t"
    "v_mfma_f32_16x16x32_bf16 v[76:79], a[204:207], v[144:147], v[76:79]\n\t"
    "v_exp_f32 v155, v155\n\t"
    "v_mfma_f32_16x16x32_bf16 v[80:83], a[208:211], v[144:147], v[80:83]\n\t"
    "v_exp_f32 v156, v156\n\t"
    "v_mfma_f32_16x16x32_bf16 v[84:87], a[212:215], v[144:147], v[84:87]\n\t"
    "buffer_store_dwordx4 a[8:11], v227, s[48:51], s66 offen offset:2048 nt\n\t"
    "v_mfma_f32_16x16x32_bf16 v[88:91], a[216:219], v[144:147], v[88:91]\n\t"
    "v_fma_f32 v157, v157, s60, v171\n\t"
    "v_fma_f32 v158, v158, s60, v171\n\t"
    "v_mfma_f32_16x16x32_bf16 v[92:95], a[220:223], v[144:147], v[92:95]\n\t"
    "v_exp_f32 v157, v157\n\t"
    "v_mfma_f32_16x16x32_bf16 v[136:139], a[160:163], a[144:147], 0\n\t"
    "ds_read_b128 a[160:163], v224 offset:24576\n\t"
    "v_fma_f32 v159, v159, s60, v171\n\t"
    "v_mfma_f32_16x16x32_bf16 v[136:139], a[164:167], a[148:151], v[136:139]\n\t"
    "v_exp_f32 v158, v158\n\t"
    "v_mfma_f32_16x16x32_bf16 v[136:139], a[168:171], a[152:155], v[136:139]\n\t"
    "ds_read_b128 a[164:167], v224 offset:25088\n\t"
    "v_cvt_pk_bf16_f32 v152, v152, v153\n\t"
    "v_mfma_f32_16x16x32_bf16 v[136:139], a[172:175], a[156:159], v[136:139]\n\t"
    "v_exp_f32 v159, v159\n\t"
    "v_mfma_f32_16x16x32_bf16 v[140:143], a[176:179], a[144:147], 0\n\t"
    "ds_read_b128 a[168:171], v224 offset:25600\n\t"
    "v_cvt_pk_bf16_f32 v153, v154, v155\n\t"
    "v_mfma_f32_16x16x32_bf16 v[140:143], a[180:183], a[148:151], v[140:143]\n\t"
    "v_cvt_pk_bf16_f32 v154, v156, v157\n\t"
    "v_cvt_pk_bf16_f32 v155, v158, v159\n\t"
    "v_mfma_f32_16x16x32_bf16 v[140:143], a[184:187], a[152:155], v[140:143]\n\t"
    "ds_read_b128 a[172:175], v224 offset:26112\n\t"
    "v_fma_f32 v128, v128, s60, v172\n\t"
    "v_mfma_f32_16x16x32_bf16 v[140:143], a[188:191], a[156:159], v[140:143]\n\t"
    "v_exp_f32 v128, v128\n\t"
    "v_mfma_f32_16x16x32_bf16 a[236:239], a[248:251], v[152:155], a[236:239]\n\t"
    "ds_read_b128 a[176:179], v224 offset:28672\n\t"
    "v_fma_f32 v129, v129, s60, v172\n\t"
    "v_mfma_f32_16x16x32_bf16 v[96:99], a[192:195], v[152:155], v[96:99]\n\t"
    "v_exp_f32 v129, v129\n\t"
    "v_mfma_f32_16x16x32_bf16 v[100:103], a[196:199], v[152:155], v[100:103]\n\t"
    "ds_read_b128 a[180:183], v224 offset:29184\n\t"
    "v_fma_f32 v130, v130, s60, v172\n\t"
    "v_mfma_f32_16x16x32_bf16 v[104:107], a[200:203], v[152:155], v[104:107]\n\t"
    "v_exp_f32 v130, v130\n\t"
    "v_mfma_f32_16x16x32_bf16 v[108:111], a[204:207], v[152:155], v[108:111]\n\t"
    "ds_read_b128 a[184:187], v224 offset:29696\n\t"
    "v_fma_f32 v131, v131, s60, v172\n\t"
    "v_mfma_f32_16x16x32_bf16 v[112:115], a[208:211], v[152:155], v[112:115]\n\t"
    "ds_read_b128 a[188:191], v224 offset:30208\n\t"
    "v_fma_f32 v132, v132, s60, v172\n\t"
    "v_mfma_f32_16x16x32_bf16 v[116:119], a[212:215], v[152:155], v[116:119]\n\t"
    "v_exp_f32 v131, v131\n\t"
    "v_mfma_f32_16x16x32_bf16 v[120:123], a[216:219], v[152:155], v[120:123]\n\t"
    "v_exp_f32 v132, v132\n\t"
    "v_mfma_f32_16x16x32_bf16 v[124:127], a[220:223], v[152:155], v[124:127]\n\t"
    "s_waitcnt lgkmcnt(6)\n\t"
    "v_fma_f32 v133, v133, s60, v172\n\t"
    "v_fma_f32 v134, v134, s60, v172\n\t"
    "v_mfma_f32_16x16x32_bf16 v[144:147], a[160:163], a[64:67], 0\n\t"
    "v_exp_f32 v133, v133\n\t"
    "v_mfma_f32_16x16x32_bf16 v[144:147], a[164:167], a[68:71], v[144:147]\n\t"
    "s_waitcnt lgkmcnt(4)\n\t"
    "v_exp_f32 v134, v134\n\t"
    "v_mfma_f32_16x16x32_bf16 v[144:147], a[168:171], a[72:75], v[144:147]\n\t"
    "v_fma_f32 v135, v135, s60, v172\n\t"
    "v_cvt_pk_bf16_f32 v128, v128, v129\n\t"
    "v_mfma_f32_16x16x32_bf16 v[144:147], a[172:175], a[76:79], v[144:147]\n\t"
    "s_waitcnt lgkmcnt(2)\n\t"
    "v_exp_f32 v135, v135\n\t"
    "v_mfma_f32_16x16x32_bf16 v[148:151], a[176:179], a[64:67], 0\n\t"
    "v_cvt_pk_bf16_f32 v129, v130, v131\n\t"
    "v_cvt_pk_bf16_f32 v130, v132, v133\n\t"
    "v_mfma_f32_16x16x32_bf16 v[148:151], a[180:183], a[68:71], v[148:151]\n\t"
    "s_waitcnt lgkmcnt(0)\n\t"
    "v_cvt_pk_bf16_f32 v131, v134, v135\n\t"
    "v_fma_f32 v136, v136, s60, v173\n\t"
    "v_mfma_f32_16x16x32_bf16 v[148:151], a[184:187], a[72:75], v[148:151]\n\t"
    "v_exp_f32 v136, v136\n\t"
    "v_mfma_f32_16x16x32_bf16 v[148:151], a[188:191], a[76:79], v[148:151]\n\t"
    "v_fma_f32 v137, v137, s60, v173\n\t"
    "v_fma_f32 v138, v138, s60, v173\n\t"
    "v_mfma_f32_16x16x32_bf16 a[240:243], a[248:251], v[128:131], a[240:243]\n\t"
    "v_exp_f32 v137, v137\n\t"
    "v_mfma_f32_16x16x32_bf16 v[208:211], a[192:195], v[128:131], v[208:211]\n\t"
    "v_exp_f32 v138, v138\n\t"
    "v_mfma_f32_16x16x32_bf16 v[212:215], a[196:199], v[128:131], v[212:215]\n\t"
    "v_fma_f32 v139, v139, s60, v173\n\t"
    "v_fma_f32 v140, v140, s60, v173\n\t"
    "v_mfma_f32_16x16x32_bf16 v[216:219], a[200:203], v[128:131], v[216:219]\n\t"
    "v_exp_f32 v139, v139\n\t"
    "v_mfma_f32_16x16x32_bf16 v[220:223], a[204:207], v[128:131], v[220:223]\n\t"
    "v_exp_f32 v140, v140\n\t"
    "v_mfma_f32_16x16x32_bf16 v[240:243], a[208:211], v[128:131], v[240:243]\n\t"
    "v_fma_f32 v141, v141, s60, v173\n\t"
    "v_fma_f32 v142, v142, s60, v173\n\t"
    "v_mfma_f32_16x16x32_bf16 v[244:247], a[212:215], v[128:131], v[244:247]\n\t"
    "v_exp_f32 v141, v141\n\t"
    "v_mfma_f32_16x16x32_bf16 v[248:251], a[216:219], v[128:131], v[248:251]\n\t"
    "v_exp_f32 v142, v142\n\t"
    "v_mfma_f32_16x16x32_bf16 v[252:255], a[220:223], v[128:131], v[252:255]\n\t"
    "v_fma_f32 v143, v143, s60, v173\n\t"
    "v_cvt_pk_bf16_f32 v136, v136, v137\n\t"
    "v_mfma_f32_16x16x32_bf16 v[152:155], a[160:163], a[80:83], 0\n\t"
    "v_exp_f32 v143, v143\n\t"
    "v_mfma_f32_16x16x32_bf16 v[152:155], a[164:167], a[84:87], v[152:155]\n\t"
    "v_cvt_pk_bf16_f32 v137, v138, v139\n\t"
    "v_cvt_pk_bf16_f32 v138, v140, v141\n\t"
    "v_mfma_f32_16x16x32_bf16 v[152:155], a[168:171], a[88:91], v[152:155]\n\t"
    "v_cvt_pk_bf16_f32 v139, v142, v143\n\t"
    "v_fma_f32 v144, v144, s60, v168\n\t"
    "v_mfma_f32_16x16x32_bf16 v[152:155], a[172:175], a[92:95], v[152:155]\n\t"
    "v_exp_f32 v144, v144\n\t"
    "v_mfma_f32_16x16x32_bf16 v[156:159], a[176:179], a[80:83], 0\n\t"
    "v_fma_f32 v145, v145, s60, v168\n\t"
    "v_fma_f32 v146, v146, s60, v168\n\t"
    "v_mfma_f32_16x16x32_bf16 v[156:159], a[180:183], a[84:87], v[156:159]\n\t"
    "v_exp_f32 v145, v145\n\t"
    "v_mfma_f32_16x16x32_bf16 v[156:159], a[184:187], a[88:91], v[156:159]\n\t"
    "v_exp_f32 v146, v146\n\t"
    "v_mfma_f32_16x16x32_bf16 v[156:159], a[188:191], a[92:95], v[156:159]\n\t"
    "v_fma_f32 v147, v147, s60, v168\n\t"
    "v_fma_f32 v148, v148, s60, v168\n\t"
    "v_mfma_f32_16x16x32_bf16 a[244:247], a[248:251], v[136:139], a[244:247]\n\t"
    "v_exp_f32 v147, v147\n\t"
    "v_mfma_f32_16x16x32_bf16 v[176:179], a[192:195], v[136:139], v[176:179]\n\t"
    "v_exp_f32 v148, v148\n\t"
    "v_mfma_f32_16x16x32_bf16 v[180:183], a[196:199], v[136:139], v[180:183]\n\t"
    "v_fma_f32 v149, v149, s60, v168\n\t"
    "v_fma_f32 v150, v150, s60, v168\n\t"
    "v_mfma_f32_16x16x32_bf16 v[184:187], a[200:203], v[136:139], v[184:187]\n\t"
    "v_exp_f32 v149, v149\n\t"
    "v_mfma_f32_16x16x32_bf16 v[188:191], a[204:207], v[136:139], v[188:191]\n\t"
    "v_exp_f32 v150, v150\n\t"
    "v_mfma_f32_16x16x32_bf16 v[192:195], a[208:211], v[136:139], v[192:195]\n\t"
    "ds_read_b64_tr_b16 a[208:209], v225 offset:25600\n\t"
    "v_fma_f32 v151, v151, s60, v168\n\t"
    "v_mfma_f32_16x16x32_bf16 v[196:199], a[212:215], v[136:139], v[196:199]\n\t"
    "v_exp_f32 v151, v151\n\t"
    "v_mfma_f32_16x16x32_bf16 v[200:203], a[216:219], v[136:139], v[200:203]\n\t"
    "ds_read_b64_tr_b16 a[210:211], v225 offset:29696\n\t"
    "v_cvt_pk_bf16_f32 v144, v144, v145\n\t"
    "v_mfma_f32_16x16x32_bf16 v[204:207], a[220:223], v[136:139], v[204:207]\n\t"
    "buffer_store_dwordx4 a[12:15], v228, s[48:51], s66 offen offset:3072 nt\n\t"
    "v_mfma_f32_16x16x32_bf16 v[128:131], a[160:163], a[96:99], 0\n\t"
    "ds_read_b64_tr_b16 a[212:213], v225 offset:25608\n\t"
    "v_cvt_pk_bf16_f32 v145, v146, v147\n\t"
    "v_mfma_f32_16x16x32_bf16 v[128:131], a[164:167], a[100:103], v[128:131]\n\t"
    "v_cvt_pk_bf16_f32 v146, v148, v149\n\t"
    "v_cvt_pk_bf16_f32 v147, v150, v151\n\t"
    "v_mfma_f32_16x16x32_bf16 v[128:131], a[168:171], a[104:107], v[128:131]\n\t"
    "ds_read_b64_tr_b16 a[214:215], v225 offset:29704\n\t"
    "v_fma_f32 v152, v152, s60, v169\n\t"
    "v_mfma_f32_16x16x32_bf16 v[128:131], a[172:175], a[108:111], v[128:131]\n\t"
    "ds_read_b64_tr_b16 a[216:217], v225 offset:26112\n\t"
    "v_fma_f32 v153, v153, s60, v169\n\t"
    "v_mfma_f32_16x16x32_bf16 v[132:135], a[176:179], a[96:99], 0\n\t"
    "ds_read_b64_tr_b16 a[218:219], v225 offset:30208\n\t"
    "v_fma_f32 v154, v154, s60, v169\n\t"
    "v_mfma_f32_16x16x32_bf16 v[132:135], a[180:183], a[100:103], v[132:135]\n\t"
    "ds_read_b64_tr_b16 a[220:221], v225 offset:26120\n\t"
    "v_fma_f32 v155, v155, s60, v169\n\t"
    "v_mfma_f32_16x16x32_bf16 v[132:135], a[184:187], a[104:107], v[132:135]\n\t"
    "ds_read_b64_tr_b16 a[222:223], v225 offset:30216\n\t"
    "v_fma_f32 v156, v156, s60, v169\n\t"
    "v_mfma_f32_16x16x32_bf16 v[132:135], a[188:191], a[108:111], v[132:135]\n\t"
    "v_exp_f32 v152, v152\n\t"
    "v_mfma_f32_16x16x32_bf16 a[224:227], a[248:251], v[144:147], a[224:227]\n\t"
    "s_waitcnt lgkmcnt(15)\n\t"
    "v_exp_f32 v153, v153\n\t"
    "v_mfma_f32_16x16x32_bf16 v[0:3], a[32:35], v[144:147], v[0:3]\n\t"
    "v_exp_f32 v154, v154\n\t"
    "v_mfma_f32_16x16x32_bf16 v[4:7], a[36:39], v[144:147], v[4:7]\n\t"
    "v_exp_f32 v155, v155\n\t"
    "v_mfma_f32_16x16x32_bf16 v[8:11], a[40:43], v[144:147], v[8:11]\n\t"
    "v_exp_f32 v156, v156\n\t"
    "v_mfma_f32_16x16x32_bf16 v[12:15], a[44:47], v[144:147], v[12:15]\n\t"
    "s_waitcnt lgkmcnt(4)\n\t"
    "v_fma_f32 v157, v157, s60, v169\n\t"
    "v_fma_f32 v158, v158, s60, v169\n\t"
    "v_mfma_f32_16x16x32_bf16 v[16:19], a[208:211], v[144:147], v[16:19]\n\t"
    "v_exp_f32 v157, v157\n\t"
    "v_mfma_f32_16x16x32_bf16 v[20:23], a[212:215], v[144:147], v[20:23]\n\t"
    "s_waitcnt lgkmcnt(0)\n\t"
    "v_exp_f32 v158, v158\n\t"
    "v_mfma_f32_16x16x32_bf16 v[24:27], a[216:219], v[144:147], v[24:27]\n\t"
    "v_fma_f32 v159, v159, s60, v169\n\t"
    "v_cvt_pk_bf16_f32 v152, v152, v153\n\t"
    "v_mfma_f32_16x16x32_bf16 v[28:31], a[220:223], v[144:147], v[28:31]\n\t"
    "v_exp_f32 v159, v159\n\t"
    "v_mfma_f32_16x16x32_bf16 v[136:139], a[160:163], a[112:115], 0\n\t"
    "s_waitcnt vmcnt(17) lgkmcnt(0)\n\t"
    "v_cvt_pk_bf16_f32 v153, v154, v155\n\t"
    "v_cvt_pk_bf16_f32 v154, v156, v157\n\t"
    "v_mfma_f32_16x16x32_bf16 v[136:139], a[164:167], a[116:119], v[136:139]\n\t"
    "s_barrier\n\t"
    "v_cvt_pk_bf16_f32 v155, v158, v159\n\t"
    "v_fma_f32 v128, v128, s60, v170\n\t"
    "v_mfma_f32_16x16x32_bf16 v[136:139], a[168:171], a[120:123], v[136:139]\n\t"
    "ds_read_b64_tr_b16 a[192:193], v225 offset:0\n\t"
    "v_fma_f32 v129, v129, s60, v170\n\t"
    "v_mfma_f32_16x16x32_bf16 v[136:139], a[172:175], a[124:127], v[136:139]\n\t"
    "s_xor_b32 s64, s52, 90112\n\t"
    "v_exp_f32 v128, v128\n\t"
    "v_mfma_f32_16x16x32_bf16 v[140:143], a[176:179], a[112:115], 0\n\t"
    "ds_read_b64_tr_b16 a[194:195], v225 offset:4096\n\t"
    "v_fma_f32 v130, v130, s60, v170\n\t"
    "v_mfma_f32_16x16x32_bf16 v[140:143], a[180:183], a[116:119], v[140:143]\n\t"
    "s_add_u32 m0, s63, 24576\n\t"
    "v_exp_f32 v129, v129\n\t"
    "v_mfma_f32_16x16x32_bf16 v[140:143], a[184:187], a[120:123], v[140:143]\n\t"
    "ds_read_b64_tr_b16 a[196:197], v225 offset:8\n\t"
    "v_fma_f32 v131, v131, s60, v170\n\t"
    "v_mfma_f32_16x16x32_bf16 v[140:143], a[188:191], a[124:127], v[140:143]\n\t"
    "buffer_load_dwordx4 v226, s[36:39], s64 offen offset:0 lds\n\t"
    "v_mfma_f32_16x16x32_bf16 a[228:231], a[248:251], v[152:155], a[228:231]\n\t"
    "ds_read_b64_tr_b16 a[198:199], v225 offset:4104\n\t"
    "v_fma_f32 v132, v132, s60, v170\n\t"
    "v_mfma_f32_16x16x32_bf16 v[32:35], a[32:35], v[152:155], v[32:35]\n\t"
    "v_exp_f32 v130, v130\n\t"
    "v_mfma_f32_16x16x32_bf16 v[36:39], a[36:39], v[152:155], v[36:39]\n\t"
    "ds_read_b64_tr_b16 a[200:201], v225 offset:512\n\t"
    "v_fma_f32 v133, v133, s60, v170\n\t"
    "v_mfma_f32_16x16x32_bf16 v[40:43], a[40:43], v[152:155], v[40:43]\n\t"
    "v_exp_f32 v131, v131\n\t"
    "v_mfma_f32_16x16x32_bf16 v[44:47], a[44:47], v[152:155], v[44:47]\n\t"
    "ds_read_b64_tr_b16 a[202:203], v225 offset:4608\n\t"
    "v_fma_f32 v134, v134, s60, v170\n\t"
    "v_mfma_f32_16x16x32_bf16 v[48:51], a[208:211], v[152:155], v[48:51]\n\t"
    "buffer_load_dwordx4 v226, s[36:39], s64 offen offset:2048 lds\n\t"
    "v_mfma_f32_16x16x32_bf16 v[52:55], a[212:215], v[152:155], v[52:55]\n\t"
    "ds_read_b64_tr_b16 a[204:205], v225 offset:520\n\t"
    "v_fma_f32 v135, v135, s60, v170\n\t"
    "v_exp_f32 v132, v132\n\t"
    "v_mfma_f32_16x16x32_bf16 v[56:59], a[216:219], v[152:155], v[56:59]\n\t"
    "v_exp_f32 v133, v133\n\t"
    "v_mfma_f32_16x16x32_bf16 v[60:63], a[220:223], v[152:155], v[60:63]\n\t"
    "ds_read_b64_tr_b16 a[206:207], v225 offset:4616\n\t"
    "v_cvt_pk_bf16_f32 v128, v128, v129\n\t"
    "v_mfma_f32_16x16x32_bf16 v[144:147], a[160:163], a[128:131], 0\n\t"
    "v_exp_f32 v134, v134\n\t"
    "v_mfma_f32_16x16x32_bf16 v[144:147], a[164:167], a[132:135], v[144:147]\n\t"
    "s_add_u32 m0, s63, 57344\n\t"
    "v_exp_f32 v135, v135\n\t"
    "v_mfma_f32_16x16x32_bf16 v[144:147], a[168:171], a[136:139], v[144:147]\n\t"
    "buffer_load_dwordx4 v226, s[40:43], s64 offen offset:0 lds\n\t"
    "v_mfma_f32_16x16x32_bf16 v[144:147], a[172:175], a[140:143], v[144:147]\n\t"
    "v_cvt_pk_bf16_f32 v129, v130, v131\n\t"
    "v_cvt_pk_bf16_f32 v130, v132, v133\n\t"
    "v_mfma_f32_16x16x32_bf16 v[148:151], a[176:179], a[128:131], 0\n\t"
    "v_cvt_pk_bf16_f32 v131, v134, v135\n\t"
    "v_fma_f32 v136, v136, s60, v171\n\t"
    "v_mfma_f32_16x16x32_bf16 v[148:151], a[180:183], a[132:135], v[148:151]\n\t"
    "v_exp_f32 v136, v136\n\t"
    "v_mfma_f32_16x16x32_bf16 v[148:151], a[184:187], a[136:139], v[148:151]\n\t"
    "v_fma_f32 v137, v137, s60, v171\n\t"
    "v_fma_f32 v138, v138, s60, v171\n\t"
    "v_mfma_f32_16x16x32_bf16 v[148:151], a[188:191], a[140:143], v[148:151]\n\t"
    "buffer_load_dwordx4 v226, s[40:43], s64 offen offset:2048 lds\n\t"
    "v_mfma_f32_16x16x32_bf16 a[232:235], a[248:251], v[128:131], a[232:235]\n\t"
    "v_exp_f32 v137, v137\n\t"
    "v_mfma_f32_16x16x32_bf16 v[64:67], a[32:35], v[128:131], v[64:67]\n\t"
    "v_exp_f32 v138, v138\n\t"
    "v_mfma_f32_16x16x32_bf16 v[68:71], a[36:39], v[128:131], v[68:71]\n\t"
    "v_fma_f32 v139, v139, s60, v171\n\t"
    "v_fma_f32 v140, v140, s60, v171\n\t"
    "v_mfma_f32_16x16x32_bf16 v[72:75], a[40:43], v[128:131], v[72:75]\n\t"
    "v_exp_f32 v139, v139\n\t"
    "v_mfma_f32_16x16x32_bf16 v[76:79], a[44:47], v[128:131], v[76:79]\n\t"
    "v_exp_f32 v140, v140\n\t"
    "v_mfma_f32_16x16x32_bf16 v[80:83], a[208:211], v[128:131], v[80:83]\n\t"
    "v_fma_f32 v141, v141, s60, v171\n\t"
    "v_fma_f32 v142, v142, s60, v171\n\t"
    "v_mfma_f32_16x16x32_bf16 v[84:87], a[212:215], v[128:131], v[84:87]\n\t"
    "v_exp_f32 v141, v141\n\t"
    "v_mfma_f32_16x16x32_bf16 v[88:91], a[216:219], v[128:131], v[88:91]\n\t"
    "v_exp_f32 v142, v142\n\t"
    "v_mfma_f32_16x16x32_bf16 v[92:95], a[220:223], v[128:131], v[92:95]\n\t"
    "v_fma_f32 v143, v143, s60, v171\n\t"
    "v_cvt_pk_bf16_f32 v136, v136, v137\n\t"
    "v_mfma_f32_16x16x32_bf16 v[152:155], a[160:163], a[144:147], 0\n\t"
    "ds_read_b128 a[160:163], v224 offset:0\n\t"
    "v_cvt_pk_bf16_f32 v137, v138, v139\n\t"
    "v_mfma_f32_16x16x32_bf16 v[152:155], a[164:167], a[148:151], v[152:155]\n\t"
    "ds_read_b128 a[0:3], v232 offset:12288\n\t"
    "v_cvt_pk_bf16_f32 v138, v140, v141\n\t"
    "v_mfma_f32_16x16x32_bf16 v[152:155], a[168:171], a[152:155], v[152:155]\n\t"
    "ds_read_b128 a[164:167], v224 offset:512\n\t"
    "v_fma_f32 v144, v144, s60, v172\n\t"
    "v_mfma_f32_16x16x32_bf16 v[152:155], a[172:175], a[156:159], v[152:155]\n\t"
    "ds_read_b128 a[4:7], v232 offset:13312\n\t"
    "v_fma_f32 v145, v145, s60, v172\n\t"
    "v_exp_f32 v143, v143\n\t"
    "v_mfma_f32_16x16x32_bf16 v[156:159], a[176:179], a[144:147], 0\n\t"
    "ds_read_b128 a[168:171], v224 offset:1024\n\t"
    "v_cvt_pk_bf16_f32 v139, v142, v143\n\t"
    "v_mfma_f32_16x16x32_bf16 v[156:159], a[180:183], a[148:151], v[156:159]\n\t"
    "ds_read_b128 a[8:11], v232 offset:14336\n\t"
    "v_fma_f32 v146, v146, s60, v172\n\t"
    "v_mfma_f32_16x16x32_bf16 v[156:159], a[184:187], a[152:155], v[156:159]\n\t"
    "ds_read_b128 a[172:175], v224 offset:1536\n\t"
    "v_fma_f32 v147, v147, s60, v172\n\t"
    "v_mfma_f32_16x16x32_bf16 v[156:159], a[188:191], a[156:159], v[156:159]\n\t"
    "ds_read_b128 a[12:15], v232 offset:15360\n\t"
    "v_fma_f32 v148, v148, s60, v172\n\t"
    "v_mfma_f32_16x16x32_bf16 a[236:239], a[248:251], v[136:139], a[236:239]\n\t"
    "ds_read_b128 a[176:179], v224 offset:4096\n\t"
    "v_fma_f32 v149, v149, s60, v172\n\t"
    "v_mfma_f32_16x16x32_bf16 v[96:99], a[32:35], v[136:139], v[96:99]\n\t"
    "v_exp_f32 v144, v144\n\t"
    "v_mfma_f32_16x16x32_bf16 v[100:103], a[36:39], v[136:139], v[100:103]\n\t"
    "ds_read_b128 a[180:183], v224 offset:4608\n\t"
    "v_fma_f32 v150, v150, s60, v172\n\t"
    "v_mfma_f32_16x16x32_bf16 v[104:107], a[40:43], v[136:139], v[104:107]\n\t"
    "v_exp_f32 v145, v145\n\t"
    "v_mfma_f32_16x16x32_bf16 v[108:111], a[44:47], v[136:139], v[108:111]\n\t"
    "ds_read_b128 a[184:187], v224 offset:5120\n\t"
    "v_fma_f32 v151, v151, s60, v172\n\t"
    "v_mfma_f32_16x16x32_bf16 v[112:115], a[208:211], v[136:139], v[112:115]\n\t"
    "ds_read_b128 a[188:191], v224 offset:5632\n\t"
    "v_cvt_pk_bf16_f32 v144, v144, v145\n\t"
    "v_mfma_f32_16x16x32_bf16 v[116:119], a[212:215], v[136:139], v[116:119]\n\t"
    "v_exp_f32 v146, v146\n\t"
    "v_mfma_f32_16x16x32_bf16 v[120:123], a[216:219], v[136:139], v[120:123]\n\t"
    "v_exp_f32 v147, v147\n\t"
    "v_mfma_f32_16x16x32_bf16 v[124:127], a[220:223], v[136:139], v[124:127]\n\t"
    "s_waitcnt lgkmcnt(9)\n\t"
    "v_exp_f32 v148, v148\n\t"
    "v_mfma_f32_16x16x32_bf16 v[128:131], a[160:163], a[64:67], 0\n\t"
    "v_exp_f32 v149, v149\n\t"
    "v_mfma_f32_16x16x32_bf16 v[128:131], a[164:167], a[68:71], v[128:131]\n\t"
    "s_waitcnt lgkmcnt(5)\n\t"
    "v_exp_f32 v150, v150\n\t"
    "v_mfma_f32_16x16x32_bf16 v[128:131], a[168:171], a[72:75], v[128:131]\n\t"
    "s_waitcnt lgkmcnt(4)\n\t"
    "v_exp_f32 v151, v151\n\t"
    "v_mfma_f32_16x16x32_bf16 v[128:131], a[172:175], a[76:79], v[128:131]\n\t"
    "s_waitcnt lgkmcnt(2)\n\t"
    "v_cvt_pk_bf16_f32 v145, v146, v147\n\t"
    "v_cvt_pk_bf16_f32 v146, v148, v149\n\t"
    "v_mfma_f32_16x16x32_bf16 v[132:135], a[176:179], a[64:67], 0\n\t"
    "s_add_u32 s65, s53, 12288\n\t"
    "v_cvt_pk_bf16_f32 v147, v150, v151\n\t"
    "v_fma_f32 v152, v152, s60, v173\n\t"
    "v_mfma_f32_16x16x32_bf16 v[132:135], a[180:183], a[68:71], v[132:135]\n\t"
    "s_waitcnt lgkmcnt(0)\n\t"
    "v_exp_f32 v152, v152\n\t"
    "v_mfma_f32_16x16x32_bf16 v[132:135], a[184:187], a[72:75], v[132:135]\n\t"
    "s_add_u32 m0, s69, 12288\n\t"
    "v_fma_f32 v153, v153, s60, v173\n\t"
    "v_fma_f32 v154, v154, s60, v173\n\t"
    "v_mfma_f32_16x16x32_bf16 v[132:135], a[188:191], a[76:79], v[132:135]\n\t"
    "buffer_load_dwordx4 v226, s[44:47], s65 offen offset:0 nt lds\n\t"
    "v_mfma_f32_16x16x32_bf16 a[240:243], a[248:251], v[144:147], a[240:243]\n\t"
    "s_add_u32 s66, s54, 12288\n\t"
    "v_exp_f32 v153, v153\n\t"
    "v_mfma_f32_16x16x32_bf16 v[208:211], a[32:35], v[144:147], v[208:211]\n\t"
    "buffer_load_dwordx4 v226, s[44:47], s65 offen offset:2048 nt lds\n\t"
    "v_mfma_f32_16x16x32_bf16 v[212:215], a[36:39], v[144:147], v[212:215]\n\t"
    "buffer_store_dwordx4 a[0:3], v227, s[48:51], s66 offen offset:0 nt\n\t"
    "v_mfma_f32_16x16x32_bf16 v[216:219], a[40:43], v[144:147], v[216:219]\n\t"
    "v_exp_f32 v154, v154\n\t"
    "v_mfma_f32_16x16x32_bf16 v[220:223], a[44:47], v[144:147], v[220:223]\n\t"
    "v_fma_f32 v155, v155, s60, v173\n\t"
    "v_fma_f32 v156, v156, s60, v173\n\t"
    "v_mfma_f32_16x16x32_bf16 v[240:243], a[208:211], v[144:147], v[240:243]\n\t"
    "v_exp_f32 v155, v155\n\t"
    "v_mfma_f32_16x16x32_bf16 v[244:247], a[212:215], v[144:147], v[244:247]\n\t"
    "v_exp_f32 v156, v156\n\t"
    "v_fma_f32 v157, v157, s60, v173\n\t"
    "v_fma_f32 v158, v158, s60, v173\n\t"
    "v_mfma_f32_16x16x32_bf16 v[248:251], a[216:219], v[144:147], v[248:251]\n\t"
    "v_exp_f32 v157, v157\n\t"
    "v_mfma_f32_16x16x32_bf16 v[252:255], a[220:223], v[144:147], v[252:255]\n\t"
    "v_exp_f32 v158, v158\n\t"
    "v_mfma_f32_16x16x32_bf16 v[136:139], a[160:163], a[80:83], 0\n\t"
    "v_fma_f32 v159, v159, s60, v173\n\t"
    "v_cvt_pk_bf16_f32 v152, v152, v153\n\t"
    "v_mfma_f32_16x16x32_bf16 v[136:139], a[164:167], a[84:87], v[136:139]\n\t"
    "v_exp_f32 v159, v159\n\t"
    "v_mfma_f32_16x16x32_bf16 v[136:139], a[168:171], a[88:91], v[136:139]\n\t"
    "s_add_u32 s65, s53, 12416\n\t"
    "v_cvt_pk_bf16_f32 v153, v154, v155\n\t"
    "v_cvt_pk_bf16_f32 v154, v156, v157\n\t"
    "v_mfma_f32_16x16x32_bf16 v[136:139], a[172:175], a[92:95], v[136:139]\n\t"
    "s_add_u32 m0, s69, 13312\n\t"
    "v_cvt_pk_bf16_f32 v155, v158, v159\n\t"
    "v_fma_f32 v128, v128, s60, v168\n\t"
    "v_mfma_f32_16x16x32_bf16 v[140:143], a[176:179], a[80:83], 0\n\t"
    "buffer_load_dwordx4 v226, s[44:47], s65 offen offset:0 nt lds\n\t"
    "v_mfma_f32_16x16x32_bf16 v[140:143], a[180:183], a[84:87], v[140:143]\n\t"
    "v_exp_f32 v128, v128\n\t"
    "v_mfma_f32_16x16x32_bf16 v[140:143], a[184:187], a[88:91], v[140:143]\n\t"
    "buffer_load_dwordx4 v226, s[44:47], s65 offen offset:2048 nt lds\n\t"
    "v_mfma_f32_16x16x32_bf16 v[140:143], a[188:191], a[92:95], v[140:143]\n\t"
    "v_fma_f32 v129, v129, s60, v168\n\t"
    "v_fma_f32 v130, v130, s60, v168\n\t"
    "v_mfma_f32_16x16x32_bf16 a[244:247], a[248:251], v[152:155], a[244:247]\n\t"
    "v_exp_f32 v129, v129\n\t"
    "v_mfma_f32_16x16x32_bf16 v[176:179], a[32:35], v[152:155], v[176:179]\n\t"
    "v_exp_f32 v130, v130\n\t"
    "v_mfma_f32_16x16x32_bf16 v[180:183], a[36:39], v[152:155], v[180:183]\n\t"
    "v_fma_f32 v131, v131, s60, v168\n\t"
    "v_fma_f32 v132, v132, s60, v168\n\t"
    "v_mfma_f32_16x16x32_bf16 v[184:187], a[40:43], v[152:155], v[184:187]\n\t"
    "v_exp_f32 v131, v131\n\t"
    "v_mfma_f32_16x16x32_bf16 v[188:191], a[44:47], v[152:155], v[188:191]\n\t"
    "v_exp_f32 v132, v132\n\t"
    "v_mfma_f32_16x16x32_bf16 v[192:195], a[208:211], v[152:155], v[192:195]\n\t"
    "ds_read_b64_tr_b16 a[208:209], v225 offset:1024\n\t"
    "v_fma_f32 v133, v133, s60, v168\n\t"
    "v_mfma_f32_16x16x32_bf16 v[196:199], a[212:215], v[152:155], v[196:199]\n\t"
    "v_exp_f32 v133, v133\n\t"
    "v_mfma_f32_16x16x32_bf16 v[200:203], a[216:219], v[152:155], v[200:203]\n\t"
    "ds_read_b64_tr_b16 a[210:211], v225 offset:5120\n\t"
    "v_fma_f32 v134, v134, s60, v168\n\t"
    "v_mfma_f32_16x16x32_bf16 v[204:207], a[220:223], v[152:155], v[204:207]\n\t"
    "v_exp_f32 v134, v134\n\t"
    "v_mfma_f32_16x16x32_bf16 v[144:147], a[160:163], a[96:99], 0\n\t"
    "ds_read_b64_tr_b16 a[212:213], v225 offset:1032\n\t"
    "v_fma_f32 v135, v135, s60, v168\n\t"
    "v_mfma_f32_16x16x32_bf16 v[144:147], a[164:167], a[100:103], v[144:147]\n\t"
    "v_exp_f32 v135, v135\n\t"
    "v_mfma_f32_16x16x32_bf16 v[144:147], a[168:171], a[104:107], v[144:147]\n\t"
    "ds_read_b64_tr_b16 a[214:215], v225 offset:5128\n\t"
    "v_cvt_pk_bf16_f32 v128, v128, v129\n\t"
    "v_mfma_f32_16x16x32_bf16 v[144:147], a[172:175], a[108:111], v[144:147]\n\t"
    "ds_read_b64_tr_b16 a[216:217], v225 offset:1536\n\t"
    "v_cvt_pk_bf16_f32 v129, v130, v131\n\t"
    "v_mfma_f32_16x16x32_bf16 v[148:151], a[176:179], a[96:99], 0\n\t"
    "ds_read_b64_tr_b16 a[218:219], v225 offset:5632\n\t"
    "v_cvt_pk_bf16_f32 v130, v132, v133\n\t"
    "v_mfma_f32_16x16x32_bf16 v[148:151], a[180:183], a[100:103], v[148:151]\n\t"
    "ds_read_b64_tr_b16 a[220:221], v225 offset:1544\n\t"
    "v_cvt_pk_bf16_f32 v131, v134, v135\n\t"
    "v_mfma_f32_16x16x32_bf16 v[148:151], a[184:187], a[104:107], v[148:151]\n\t"
    "ds_read_b64_tr_b16 a[222:223], v225 offset:5640\n\t"
    "v_fma_f32 v136, v136, s60, v169\n\t"
    "v_mfma_f32_16x16x32_bf16 v[148:151], a[188:191], a[108:111], v[148:151]\n\t"
    "v_exp_f32 v136, v136\n\t"
    "v_mfma_f32_16x16x32_bf16 a[224:227], a[248:251], v[128:131], a[224:227]\n\t"
    "s_waitcnt lgkmcnt(15)\n\t"
    "v_fma_f32 v137, v137, s60, v169\n\t"
    "v_fma_f32 v138, v138, s60, v169\n\t"
    "v_mfma_f32_16x16x32_bf16 v[0:3], a[192:195], v[128:131], v[0:3]\n\t"
    "v_exp_f32 v137, v137\n\t"
    "v_mfma_f32_16x16x32_bf16 v[4:7], a[196:199], v[128:131], v[4:7]\n\t"
    "v_exp_f32 v138, v138\n\t"
    "v_mfma_f32_16x16x32_bf16 v[8:11], a[200:203], v[128:131], v[8:11]\n\t"
    "v_fma_f32 v139, v139, s60, v169\n\t"
    "v_fma_f32 v140, v140, s60, v169\n\t"
    "v_mfma_f32_16x16x32_bf16 v[12:15], a[204:207], v[128:131], v[12:15]\n\t"
    "s_waitcnt lgkmcnt(4)\n\t"
    "v_exp_f32 v139, v139\n\t"
    "v_mfma_f32_16x16x32_bf16 v[16:19], a[208:211], v[128:131], v[16:19]\n\t"
    "v_exp_f32 v140, v140\n\t"
    "v_fma_f32 v141, v141, s60, v169\n\t"
    "v_fma_f32 v142, v142, s60, v169\n\t"
    "v_fma_f32 v143, v143, s60, v169\n\t"
    "v_mfma_f32_16x16x32_bf16 v[20:23], a[212:215], v[128:131], v[20:23]\n\t"
    "s_waitcnt lgkmcnt(0)\n\t"
    "v_exp_f32 v141, v141\n\t"
    "v_exp_f32 v142, v142\n\t"
    "v_mfma_f32_16x16x32_bf16 v[24:27], a[216:219], v[128:131], v[24:27]\n\t"
    "v_exp_f32 v143, v143\n\t"
    "v_mfma_f32_16x16x32_bf16 v[28:31], a[220:223], v[128:131], v[28:31]\n\t"
    "v_cvt_pk_bf16_f32 v136, v136, v137\n\t"
    "v_cvt_pk_bf16_f32 v137, v138, v139\n\t"
    "v_mfma_f32_16x16x32_bf16 v[152:155], a[160:163], a[112:115], 0\n\t"
    "s_waitcnt vmcnt(21) lgkmcnt(0)\n\t"
    "v_cvt_pk_bf16_f32 v138, v140, v141\n\t"
    "v_cvt_pk_bf16_f32 v139, v142, v143\n\t"
    "v_mfma_f32_16x16x32_bf16 v[152:155], a[164:167], a[116:119], v[152:155]\n\t"
    "s_barrier\n\t"
    "v_fma_f32 v144, v144, s60, v170\n\t"
    "v_fma_f32 v145, v145, s60, v170\n\t"
    "v_mfma_f32_16x16x32_bf16 v[152:155], a[168:171], a[120:123], v[152:155]\n\t"
    "ds_read_b64_tr_b16 a[32:33], v225 offset:8192\n\t"
    "v_fma_f32 v146, v146, s60, v170\n\t"
    "v_mfma_f32_16x16x32_bf16 v[152:155], a[172:175], a[124:127], v[152:155]\n\t"
    "s_xor_b32 s64, s52, 98304\n\t"
    "v_exp_f32 v144, v144\n\t"
    "v_mfma_f32_16x16x32_bf16 v[156:159], a[176:179], a[112:115], 0\n\t"
    "ds_read_b64_tr_b16 a[34:35], v225 offset:12288\n\t"
    "v_fma_f32 v147, v147, s60, v170\n\t"
    "v_mfma_f32_16x16x32_bf16 v[156:159], a[180:183], a[116:119], v[156:159]\n\t"
    "s_add_u32 m0, s63, 0\n\t"
    "v_exp_f32 v145, v145\n\t"
    "v_mfma_f32_16x16x32_bf16 v[156:159], a[184:187], a[120:123], v[156:159]\n\t"
    "ds_read_b64_tr_b16 a[36:37], v225 offset:8200\n\t"
    "v_fma_f32 v148, v148, s60, v170\n\t"
    "v_mfma_f32_16x16x32_bf16 v[156:159], a[188:191], a[124:127], v[156:159]\n\t"
    "buffer_load_dwordx4 v226, s[36:39], s64 offen offset:0 lds\n\t"
    "v_mfma_f32_16x16x32_bf16 a[228:231], a[248:251], v[136:139], a[228:231]\n\t"
    "ds_read_b64_tr_b16 a[38:39], v225 offset:12296\n\t"
    "v_fma_f32 v149, v149, s60, v170\n\t"
    "v_mfma_f32_16x16x32_bf16 v[32:35], a[192:195], v[136:139], v[32:35]\n\t"
    "v_exp_f32 v146, v146\n\t"
    "v_mfma_f32_16x16x32_bf16 v[36:39], a[196:199], v[136:139], v[36:39]\n\t"
    "ds_read_b64_tr_b16 a[40:41], v225 offset:8704\n\t"
    "v_fma_f32 v150, v150, s60, v170\n\t"
    "v_mfma_f32_16x16x32_bf16 v[40:43], a[200:203], v[136:139], v[40:43]\n\t"
    "buffer_store_dwordx4 a[4:7], v228, s[48:51], s66 offen offset:1024 nt\n\t"
    "v_mfma_f32_16x16x32_bf16 v[44:47], a[204:207], v[136:139], v[44:47]\n\t"
    "ds_read_b64_tr_b16 a[42:43], v225 offset:12800\n\t"
    "v_fma_f32 v151, v151, s60, v170\n\t"
    "v_mfma_f32_16x16x32_bf16 v[48:51], a[208:211], v[136:139], v[48:51]\n\t"
    "buffer_load_dwordx4 v226, s[36:39], s64 offen offset:2048 lds\n\t"
    "v_mfma_f32_16x16x32_bf16 v[52:55], a[212:215], v[136:139], v[52:55]\n\t"
    "ds_read_b64_tr_b16 a[44:45], v225 offset:8712\n\t"
    "v_cvt_pk_bf16_f32 v144, v144, v145\n\t"
    "v_mfma_f32_16x16x32_bf16 v[56:59], a[216:219], v[136:139], v[56:59]\n\t"
    "v_exp_f32 v147, v147\n\t"
    "v_mfma_f32_16x16x32_bf16 v[60:63], a[220:223], v[136:139], v[60:63]\n\t"
    "ds_read_b64_tr_b16 a[46:47], v225 offset:12808\n\t"
    "v_cvt_pk_bf16_f32 v145, v146, v147\n\t"
    "v_mfma_f32_16x16x32_bf16 v[128:131], a[160:163], a[128:131], 0\n\t"
    "v_exp_f32 v148, v148\n\t"
    "v_mfma_f32_16x16x32_bf16 v[128:131], a[164:167], a[132:135], v[128:131]\n\t"
    "s_add_u32 m0, s63, 32768\n\t"
    "v_exp_f32 v149, v149\n\t"
    "v_mfma_f32_16x16x32_bf16 v[128:131], a[168:171], a[136:139], v[128:131]\n\t"
    "buffer_load_dwordx4 v226, s[40:43], s64 offen offset:0 lds\n\t"
    "v_mfma_f32_16x16x32_bf16 v[128:131], a[172:175], a[140:143], v[128:131]\n\t"
    "v_exp_f32 v150, v150\n\t"
    "v_mfma_f32_16x16x32_bf16 v[132:135], a[176:179], a[128:131], 0\n\t"
    "v_exp_f32 v151, v151\n\t"
    "v_mfma_f32_16x16x32_bf16 v[132:135], a[180:183], a[132:135], v[132:135]\n\t"
    "v_cvt_pk_bf16_f32 v146, v148, v149\n\t"
    "v_cvt_pk_bf16_f32 v147, v150, v151\n\t"
    "v_mfma_f32_16x16x32_bf16 v[132:135], a[184:187], a[136:139], v[132:135]\n\t"
    "v_fma_f32 v152, v152, s60, v171\n\t"
    "v_fma_f32 v153, v153, s60, v171\n\t"
    "v_mfma_f32_16x16x32_bf16 v[132:135], a[188:191], a[140:143], v[132:135]\n\t"
    "buffer_load_dwordx4 v226, s[40:43], s64 offen offset:2048 lds\n\t"
    "v_mfma_f32_16x16x32_bf16 a[232:235], a[248:251], v[144:147], a[232:235]\n\t"
    "v_exp_f32 v152, v152\n\t"
    "v_mfma_f32_16x16x32_bf16 v[64:67], a[192:195], v[144:147], v[64:67]\n\t"
    "v_exp_f32 v153, v153\n\t"
    "v_mfma_f32_16x16x32_bf16 v[68:71], a[196:199], v[144:147], v[68:71]\n\t"
    "v_fma_f32 v154, v154, s60, v171\n\t"
    "v_fma_f32 v155, v155, s60, v171\n\t"
    "v_mfma_f32_16x16x32_bf16 v[72:75], a[200:203], v[144:147], v[72:75]\n\t"
    "v_exp_f32 v154, v154\n\t"
    "v_mfma_f32_16x16x32_bf16 v[76:79], a[204:207], v[144:147], v[76:79]\n\t"
    "v_exp_f32 v155, v155\n\t"
    "v_mfma_f32_16x16x32_bf16 v[80:83], a[208:211], v[144:147], v[80:83]\n\t"
    "v_fma_f32 v156, v156, s60, v171\n\t"
    "v_fma_f32 v157, v157, s60, v171\n\t"
    "v_mfma_f32_16x16x32_bf16 v[84:87], a[212:215], v[144:147], v[84:87]\n\t"
    "v_exp_f32 v156, v156\n\t"
    "v_mfma_f32_16x16x32_bf16 v[88:91], a[216:219], v[144:147], v[88:91]\n\t"
    "v_exp_f32 v157, v157\n\t"
    "v_mfma_f32_16x16x32_bf16 v[92:95], a[220:223], v[144:147], v[92:95]\n\t"
    "v_fma_f32 v158, v158, s60, v171\n\t"
    "v_fma_f32 v159, v159, s60, v171\n\t"
    "v_mfma_f32_16x16x32_bf16 v[136:139], a[160:163], a[144:147], 0\n\t"
    "ds_read_b128 a[160:163], v224 offset:8192\n\t"
    "v_cvt_pk_bf16_f32 v152, v152, v153\n\t"
    "v_mfma_f32_16x16x32_bf16 v[136:139], a[164:167], a[148:151], v[136:139]\n\t"
    "v_exp_f32 v158, v158\n\t"
    "v_mfma_f32_16x16x32_bf16 v[136:139], a[168:171], a[152:155], v[136:139]\n\t"
    "ds_read_b128 a[164:167], v224 offset:8704\n\t"
    "v_cvt_pk_bf16_f32 v153, v154, v155\n\t"
    "v_mfma_f32_16x16x32_bf16 v[136:139], a[172:175], a[156:159], v[136:139]\n\t"
    "v_exp_f32 v159, v159\n\t"
    "v_mfma_f32_16x16x32_bf16 v[140:143], a[176:179], a[144:147], 0\n\t"
    "ds_read_b128 a[168:171], v224 offset:9216\n\t"
    "v_cvt_pk_bf16_f32 v154, v156, v157\n\t"
    "v_mfma_f32_16x16x32_bf16 v[140:143], a[180:183], a[148:151], v[140:143]\n\t"
    "v_cvt_pk_bf16_f32 v155, v158, v159\n\t"
    "v_fma_f32 v128, v128, s60, v172\n\t"
    "v_mfma_f32_16x16x32_bf16 v[140:143], a[184:187], a[152:155], v[140:143]\n\t"
    "ds_read_b128 a[172:175], v224 offset:9728\n\t"
    "v_fma_f32 v129, v129, s60, v172\n\t"
    "v_mfma_f32_16x16x32_bf16 v[140:143], a[188:191], a[156:159], v[140:143]\n\t"
    "v_exp_f32 v128, v128\n\t"
    "v_mfma_f32_16x16x32_bf16 a[236:239], a[248:251], v[152:155], a[236:239]\n\t"
    "ds_read_b128 a[176:179], v224 offset:12288\n\t"
    "v_fma_f32 v130, v130, s60, v172\n\t"
    "v_mfma_f32_16x16x32_bf16 v[96:99], a[192:195], v[152:155], v[96:99]\n\t"
    "v_exp_f32 v129, v129\n\t"
    "v_mfma_f32_16x16x32_bf16 v[100:103], a[196:199], v[152:155], v[100:103]\n\t"
    "ds_read_b128 a[180:183], v224 offset:12800\n\t"
    "v_fma_f32 v131, v131, s60, v172\n\t"
    "v_mfma_f32_16x16x32_bf16 v[104:107], a[200:203], v[152:155], v[104:107]\n\t"
    "v_exp_f32 v130, v130\n\t"
    "v_mfma_f32_16x16x32_bf16 v[108:111], a[204:207], v[152:155], v[108:111]\n\t"
    "ds_read_b128 a[184:187], v224 offset:13312\n\t"
    "v_fma_f32 v132, v132, s60, v172\n\t"
    "v_mfma_f32_16x16x32_bf16 v[112:115], a[208:211], v[152:155], v[112:115]\n\t"
    "ds_read_b128 a[188:191], v224 offset:13824\n\t"
    "v_fma_f32 v133, v133, s60, v172\n\t"
    "v_mfma_f32_16x16x32_bf16 v[116:119], a[212:215], v[152:155], v[116:119]\n\t"
    "v_exp_f32 v131, v131\n\t"
    "v_mfma_f32_16x16x32_bf16 v[120:123], a[216:219], v[152:155], v[120:123]\n\t"
    "v_exp_f32 v132, v132\n\t"
    "v_mfma_f32_16x16x32_bf16 v[124:127], a[220:223], v[152:155], v[124:127]\n\t"
    "s_waitcnt lgkmcnt(6)\n\t"
    "v_exp_f32 v133, v133\n\t"
    "v_mfma_f32_16x16x32_bf16 v[144:147], a[160:163], a[64:67], 0\n\t"
    "v_fma_f32 v134, v134, s60, v172\n\t"
    "v_fma_f32 v135, v135, s60, v172\n\t"
    "v_mfma_f32_16x16x32_bf16 v[144:147], a[164:167], a[68:71], v[144:147]\n\t"
    "s_waitcnt lgkmcnt(4)\n\t"
    "v_exp_f32 v134, v134\n\t"
    "v_mfma_f32_16x16x32_bf16 v[144:147], a[168:171], a[72:75], v[144:147]\n\t"
    "v_exp_f32 v135, v135\n\t"
    "v_mfma_f32_16x16x32_bf16 v[144:147], a[172:175], a[76:79], v[144:147]\n\t"
    "s_waitcnt lgkmcnt(2)\n\t"
    "v_cvt_pk_bf16_f32 v128, v128, v129\n\t"
    "v_cvt_pk_bf16_f32 v129, v130, v131\n\t"
    "v_mfma_f32_16x16x32_bf16 v[148:151], a[176:179], a[64:67], 0\n\t"
    "v_cvt_pk_bf16_f32 v130, v132, v133\n\t"
    "v_cvt_pk_bf16_f32 v131, v134, v135\n\t"
    "v_mfma_f32_16x16x32_bf16 v[148:151], a[180:183], a[68:71], v[148:151]\n\t"
    "s_waitcnt lgkmcnt(0)\n\t"
    "v_fma_f32 v136, v136, s60, v173\n\t"
    "v_fma_f32 v137, v137, s60, v173\n\t"
    "v_mfma_f32_16x16x32_bf16 v[148:151], a[184:187], a[72:75], v[148:151]\n\t"
    "v_exp_f32 v136, v136\n\t"
    "v_mfma_f32_16x16x32_bf16 v[148:151], a[188:191], a[76:79], v[148:151]\n\t"
    "v_exp_f32 v137, v137\n\t"
    "v_mfma_f32_16x16x32_bf16 a[240:243], a[248:251], v[128:131], a[240:243]\n\t"
    "v_fma_f32 v138, v138, s60, v173\n\t"
    "v_fma_f32 v139, v139, s60, v173\n\t"
    "v_mfma_f32_16x16x32_bf16 v[208:211], a[192:195], v[128:131], v[208:211]\n\t"
    "v_exp_f32 v138, v138\n\t"
    "v_mfma_f32_16x16x32_bf16 v[212:215], a[196:199], v[128:131], v[212:215]\n\t"
    "v_exp_f32 v139, v139\n\t"
    "v_mfma_f32_16x16x32_bf16 v[216:219], a[200:203], v[128:131], v[216:219]\n\t"
    "v_fma_f32 v140, v140, s60, v173\n\t"
    "v_fma_f32 v141, v141, s60, v173\n\t"
    "v_mfma_f32_16x16x32_bf16 v[220:223], a[204:207], v[128:131], v[220:223]\n\t"
    "v_exp_f32 v140, v140\n\t"
    "v_mfma_f32_16x16x32_bf16 v[240:243], a[208:211], v[128:131], v[240:243]\n\t"
    "buffer_store_dwordx4 a[8:11], v227, s[48:51], s66 offen offset:2048 nt\n\t"
    "v_mfma_f32_16x16x32_bf16 v[244:247], a[212:215], v[128:131], v[244:247]\n\t"
    "v_exp_f32 v141, v141\n\t"
    "v_mfma_f32_16x16x32_bf16 v[248:251], a[216:219], v[128:131], v[248:251]\n\t"
    "v_fma_f32 v142, v142, s60, v173\n\t"
    "v_fma_f32 v143, v143, s60, v173\n\t"
    "v_mfma_f32_16x16x32_bf16 v[252:255], a[220:223], v[128:131], v[252:255]\n\t"
    "v_exp_f32 v142, v142\n\t"
    "v_mfma_f32_16x16x32_bf16 v[152:155], a[160:163], a[80:83], 0\n\t"
    "v_exp_f32 v143, v143\n\t"
    "v_mfma_f32_16x16x32_bf16 v[152:155], a[164:167], a[84:87], v[152:155]\n\t"
    "v_cvt_pk_bf16_f32 v136, v136, v137\n\t"
    "v_cvt_pk_bf16_f32 v137, v138, v139\n\t"
    "v_mfma_f32_16x16x32_bf16 v[152:155], a[168:171], a[88:91], v[152:155]\n\t"
    "v_cvt_pk_bf16_f32 v138, v140, v141\n\t"
    "v_cvt_pk_bf16_f32 v139, v142, v143\n\t"
    "v_mfma_f32_16x16x32_bf16 v[152:155], a[172:175], a[92:95], v[152:155]\n\t"
    "v_fma_f32 v144, v144, s60, v168\n\t"
    "v_fma_f32 v145, v145, s60, v168\n\t"
    "v_mfma_f32_16x16x32_bf16 v[156:159], a[176:179], a[80:83], 0\n\t"
    "v_exp_f32 v144, v144\n\t"
    "v_mfma_f32_16x16x32_bf16 v[156:159], a[180:183], a[84:87], v[156:159]\n\t"
    "v_exp_f32 v145, v145\n\t"
    "v_mfma_f32_16x16x32_bf16 v[156:159], a[184:187], a[88:91], v[156:159]\n\t"
    "v_fma_f32 v146, v146, s60, v168\n\t"
    "v_fma_f32 v147, v147, s60, v168\n\t"
    "v_mfma_f32_16x16x32_bf16 v[156:159], a[188:191], a[92:95], v[156:159]\n\t"
    "v_exp_f32 v146, v146\n\t"
    "v_mfma_f32_16x16x32_bf16 a[244:247], a[248:251], v[136:139], a[244:247]\n\t"
    "v_exp_f32 v147, v147\n\t"
    "v_mfma_f32_16x16x32_bf16 v[176:179], a[192:195], v[136:139], v[176:179]\n\t"
    "v_fma_f32 v148, v148, s60, v168\n\t"
    "v_fma_f32 v149, v149, s60, v168\n\t"
    "v_mfma_f32_16x16x32_bf16 v[180:183], a[196:199], v[136:139], v[180:183]\n\t"
    "v_exp_f32 v148, v148\n\t"
    "v_mfma_f32_16x16x32_bf16 v[184:187], a[200:203], v[136:139], v[184:187]\n\t"
    "v_exp_f32 v149, v149\n\t"
    "v_mfma_f32_16x16x32_bf16 v[188:191], a[204:207], v[136:139], v[188:191]\n\t"
    "v_fma_f32 v150, v150, s60, v168\n\t"
    "v_fma_f32 v151, v151, s60, v168\n\t"
    "v_mfma_f32_16x16x32_bf16 v[192:195], a[208:211], v[136:139], v[192:195]\n\t"
    "ds_read_b64_tr_b16 a[208:209], v225 offset:9216\n\t"
    "v_cvt_pk_bf16_f32 v144, v144, v145\n\t"
    "v_mfma_f32_16x16x32_bf16 v[196:199], a[212:215], v[136:139], v[196:199]\n\t"
    "v_exp_f32 v150, v150\n\t"
    "v_mfma_f32_16x16x32_bf16 v[200:203], a[216:219], v[136:139], v[200:203]\n\t"
    "ds_read_b64_tr_b16 a[210:211], v225 offset:13312\n\t"
    "v_cvt_pk_bf16_f32 v145, v146, v147\n\t"
    "v_mfma_f32_16x16x32_bf16 v[204:207], a[220:223], v[136:139], v[204:207]\n\t"
    "v_exp_f32 v151, v151\n\t"
    "v_mfma_f32_16x16x32_bf16 v[128:131], a[160:163], a[96:99], 0\n\t"
    "ds_read_b64_tr_b16 a[212:213], v225 offset:9224\n\t"
    "v_cvt_pk_bf16_f32 v146, v148, v149\n\t"
    "v_mfma_f32_16x16x32_bf16 v[128:131], a[164:167], a[100:103], v[128:131]\n\t"
    "v_cvt_pk_bf16_f32 v147, v150, v151\n\t"
    "v_fma_f32 v152, v152, s60, v169\n\t"
    "v_mfma_f32_16x16x32_bf16 v[128:131], a[168:171], a[104:107], v[128:131]\n\t"
    "ds_read_b64_tr_b16 a[214:215], v225 offset:13320\n\t"
    "v_fma_f32 v153, v153, s60, v169\n\t"
    "v_mfma_f32_16x16x32_bf16 v[128:131], a[172:175], a[108:111], v[128:131]\n\t"
    "ds_read_b64_tr_b16 a[216:217], v225 offset:9728\n\t"
    "v_fma_f32 v154, v154, s60, v169\n\t"
    "v_mfma_f32_16x16x32_bf16 v[132:135], a[176:179], a[96:99], 0\n\t"
    "ds_read_b64_tr_b16 a[218:219], v225 offset:13824\n\t"
    "v_fma_f32 v155, v155, s60, v169\n\t"
    "v_mfma_f32_16x16x32_bf16 v[132:135], a[180:183], a[100:103], v[132:135]\n\t"
    "ds_read_b64_tr_b16 a[220:221], v225 offset:9736\n\t"
    "v_fma_f32 v156, v156, s60, v169\n\t"
    "v_mfma_f32_16x16x32_bf16 v[132:135], a[184:187], a[104:107], v[132:135]\n\t"
    "ds_read_b64_tr_b16 a[222:223], v225 offset:13832\n\t"
    "v_fma_f32 v157, v157, s60, v169\n\t"
    "v_mfma_f32_16x16x32_bf16 v[132:135], a[188:191], a[108:111], v[132:135]\n\t"
    "v_exp_f32 v152, v152\n\t"
    "v_mfma_f32_16x16x32_bf16 a[224:227], a[248:251], v[144:147], a[224:227]\n\t"
    "s_waitcnt lgkmcnt(15)\n\t"
    "v_exp_f32 v153, v153\n\t"
    "v_mfma_f32_16x16x32_bf16 v[0:3], a[32:35], v[144:147], v[0:3]\n\t"
    "v_exp_f32 v154, v154\n\t"
    "v_mfma_f32_16x16x32_bf16 v[4:7], a[36:39], v[144:147], v[4:7]\n\t"
    "v_exp_f32 v155, v155\n\t"
    "v_mfma_f32_16x16x32_bf16 v[8:11], a[40:43], v[144:147], v[8:11]\n\t"
    "v_exp_f32 v156, v156\n\t"
    "v_mfma_f32_16x16x32_bf16 v[12:15], a[44:47], v[144:147], v[12:15]\n\t"
    "s_waitcnt lgkmcnt(4)\n\t"
    "v_exp_f32 v157, v157\n\t"
    "v_mfma_f32_16x16x32_bf16 v[16:19], a[208:211], v[144:147], v[16:19]\n\t"
    "v_fma_f32 v158, v158, s60, v169\n\t"
    "v_fma_f32 v159, v159, s60, v169\n\t"
    "v_mfma_f32_16x16x32_bf16 v[20:23], a[212:215], v[144:147], v[20:23]\n\t"
    "s_waitcnt lgkmcnt(0)\n\t"
    "v_exp_f32 v158, v158\n\t"
    "v_mfma_f32_16x16x32_bf16 v[24:27], a[216:219], v[144:147], v[24:27]\n\t"
    "v_exp_f32 v159, v159\n\t"
    "v_mfma_f32_16x16x32_bf16 v[28:31], a[220:223], v[144:147], v[28:31]\n\t"
    "v_cvt_pk_bf16_f32 v152, v152, v153\n\t"
    "v_cvt_pk_bf16_f32 v153, v154, v155\n\t"
    "v_mfma_f32_16x16x32_bf16 v[136:139], a[160:163], a[112:115], 0\n\t"
    "s_waitcnt vmcnt(17) lgkmcnt(0)\n\t"
    "v_cvt_pk_bf16_f32 v154, v156, v157\n\t"
    "v_cvt_pk_bf16_f32 v155, v158, v159\n\t"
    "v_mfma_f32_16x16x32_bf16 v[136:139], a[164:167], a[116:119], v[136:139]\n\t"
    "s_barrier\n\t"
    "v_fma_f32 v128, v128, s60, v170\n\t"
    "v_fma_f32 v129, v129, s60, v170\n\t"
    "v_mfma_f32_16x16x32_bf16 v[136:139], a[168:171], a[120:123], v[136:139]\n\t"
    "ds_read_b64_tr_b16 a[192:193], v225 offset:16384\n\t"
    "v_fma_f32 v130, v130, s60, v170\n\t"
    "v_mfma_f32_16x16x32_bf16 v[136:139], a[172:175], a[124:127], v[136:139]\n\t"
    "s_xor_b32 s64, s52, 106496\n\t"
    "v_exp_f32 v128, v128\n\t"
    "v_mfma_f32_16x16x32_bf16 v[140:143], a[176:179], a[112:115], 0\n\t"
    "ds_read_b64_tr_b16 a[194:195], v225 offset:20480\n\t"
    "v_fma_f32 v131, v131, s60, v170\n\t"
    "v_mfma_f32_16x16x32_bf16 v[140:143], a[180:183], a[116:119], v[140:143]\n\t"
    "s_add_u32 m0, s63, 8192\n\t"
    "v_exp_f32 v129, v129\n\t"
    "v_mfma_f32_16x16x32_bf16 v[140:143], a[184:187], a[120:123], v[140:143]\n\t"
    "ds_read_b64_tr_b16 a[196:197], v225 offset:16392\n\t"
    "v_fma_f32 v132, v132, s60, v170\n\t"
    "v_mfma_f32_16x16x32_bf16 v[140:143], a[188:191], a[124:127], v[140:143]\n\t"
    "buffer_load_dwordx4 v226, s[36:39], s64 offen offset:0 lds\n\t"
    "v_mfma_f32_16x16x32_bf16 a[228:231], a[248:251], v[152:155], a[228:231]\n\t"
    "ds_read_b64_tr_b16 a[198:199], v225 offset:20488\n\t"
    "v_fma_f32 v133, v133, s60, v170\n\t"
    "v_mfma_f32_16x16x32_bf16 v[32:35], a[32:35], v[152:155], v[32:35]\n\t"
    "v_exp_f32 v130, v130\n\t"
    "v_mfma_f32_16x16x32_bf16 v[36:39], a[36:39], v[152:155], v[36:39]\n\t"
    "ds_read_b64_tr_b16 a[200:201], v225 offset:16896\n\t"
    "v_fma_f32 v134, v134, s60, v170\n\t"
    "v_mfma_f32_16x16x32_bf16 v[40:43], a[40:43], v[152:155], v[40:43]\n\t"
    "v_exp_f32 v131, v131\n\t"
    "v_mfma_f32_16x16x32_bf16 v[44:47], a[44:47], v[152:155], v[44:47]\n\t"
    "ds_read_b64_tr_b16 a[202:203], v225 offset:20992\n\t"
    "v_fma_f32 v135, v135, s60, v170\n\t"
    "v_mfma_f32_16x16x32_bf16 v[48:51], a[208:211], v[152:155], v[48:51]\n\t"
    "buffer_load_dwordx4 v226, s[36:39], s64 offen offset:2048 lds\n\t"
    "v_mfma_f32_16x16x32_bf16 v[52:55], a[212:215], v[152:155], v[52:55]\n\t"
    "ds_read_b64_tr_b16 a[204:205], v225 offset:16904\n\t"
    "v_cvt_pk_bf16_f32 v128, v128, v129\n\t"
    "v_mfma_f32_16x16x32_bf16 v[56:59], a[216:219], v[152:155], v[56:59]\n\t"
    "buffer_store_dwordx4 a[12:15], v228, s[48:51], s66 offen offset:3072 nt\n\t"
    "v_mfma_f32_16x16x32_bf16 v[60:63], a[220:223], v[152:155], v[60:63]\n\t"
    "ds_read_b64_tr_b16 a[206:207], v225 offset:21000\n\t"
    "v_cvt_pk_bf16_f32 v129, v130, v131\n\t"
    "v_mfma_f32_16x16x32_bf16 v[144:147], a[160:163], a[128:131], 0\n\t"
    "v_exp_f32 v132, v132\n\t"
    "v_mfma_f32_16x16x32_bf16 v[144:147], a[164:167], a[132:135], v[144:147]\n\t"
    "s_add_u32 m0, s63, 40960\n\t"
    "v_exp_f32 v133, v133\n\t"
    "v_mfma_f32_16x16x32_bf16 v[144:147], a[168:171], a[136:139], v[144:147]\n\t"
    "buffer_load_dwordx4 v226, s[40:43], s64 offen offset:0 lds\n\t"
    "v_mfma_f32_16x16x32_bf16 v[144:147], a[172:175], a[140:143], v[144:147]\n\t"
    "v_exp_f32 v134, v134\n\t"
    "v_mfma_f32_16x16x32_bf16 v[148:151], a[176:179], a[128:131], 0\n\t"
    "v_exp_f32 v135, v135\n\t"
    "v_mfma_f32_16x16x32_bf16 v[148:151], a[180:183], a[132:135], v[148:151]\n\t"
    "v_cvt_pk_bf16_f32 v130, v132, v133\n\t"
    "v_cvt_pk_bf16_f32 v131, v134, v135\n\t"
    "v_mfma_f32_16x16x32_bf16 v[148:151], a[184:187], a[136:139], v[148:151]\n\t"
    "v_fma_f32 v136, v136, s60, v171\n\t"
    "v_fma_f32 v137, v137, s60, v171\n\t"
    "v_mfma_f32_16x16x32_bf16 v[148:151], a[188:191], a[140:143], v[148:151]\n\t"
    "buffer_load_dwordx4 v226, s[40:43], s64 offen offset:2048 lds\n\t"
    "v_mfma_f32_16x16x32_bf16 a[232:235], a[248:251], v[128:131], a[232:235]\n\t"
    "v_exp_f32 v136, v136\n\t"
    "v_mfma_f32_16x16x32_bf16 v[64:67], a[32:35], v[128:131], v[64:67]\n\t"
    "v_exp_f32 v137, v137\n\t"
    "v_mfma_f32_16x16x32_bf16 v[68:71], a[36:39], v[128:131], v[68:71]\n\t"
    "v_fma_f32 v138, v138, s60, v171\n\t"
    "v_fma_f32 v139, v139, s60, v171\n\t"
    "v_mfma_f32_16x16x32_bf16 v[72:75], a[40:43], v[128:131], v[72:75]\n\t"
    "v_exp_f32 v138, v138\n\t"
    "v_mfma_f32_16x16x32_bf16 v[76:79], a[44:47], v[128:131], v[76:79]\n\t"
    "v_exp_f32 v139, v139\n\t"
    "v_mfma_f32_16x16x32_bf16 v[80:83], a[208:211], v[128:131], v[80:83]\n\t"
    "v_fma_f32 v140, v140, s60, v171\n\t"
    "v_fma_f32 v141, v141, s60, v171\n\t"
    "v_mfma_f32_16x16x32_bf16 v[84:87], a[212:215], v[128:131], v[84:87]\n\t"
    "v_exp_f32 v140, v140\n\t"
    "v_mfma_f32_16x16x32_bf16 v[88:91], a[216:219], v[128:131], v[88:91]\n\t"
    "v_exp_f32 v141, v141\n\t"
    "v_mfma_f32_16x16x32_bf16 v[92:95], a[220:223], v[128:131], v[92:95]\n\t"
    "v_fma_f32 v142, v142, s60, v171\n\t"
    "v_fma_f32 v143, v143, s60, v171\n\t"
    "v_mfma_f32_16x16x32_bf16 v[152:155], a[160:163], a[144:147], 0\n\t"
    "ds_read_b128 a[160:163], v224 offset:16384\n\t"
    "v_cvt_pk_bf16_f32 v136, v136, v137\n\t"
    "v_mfma_f32_16x16x32_bf16 v[152:155], a[164:167], a[148:151], v[152:155]\n\t"
    "v_exp_f32 v142, v142\n\t"
    "v_mfma_f32_16x16x32_bf16 v[152:155], a[168:171], a[152:155], v[152:155]\n\t"
    "ds_read_b128 a[164:167], v224 offset:16896\n\t"
    "v_cvt_pk_bf16_f32 v137, v138, v139\n\t"
    "v_mfma_f32_16x16x32_bf16 v[152:155], a[172:175], a[156:159], v[152:155]\n\t"
    "v_exp_f32 v143, v143\n\t"
    "v_mfma_f32_16x16x32_bf16 v[156:159], a[176:179], a[144:147], 0\n\t"
    "ds_read_b128 a[168:171], v224 offset:17408\n\t"
    "v_cvt_pk_bf16_f32 v138, v140, v141\n\t"
    "v_mfma_f32_16x16x32_bf16 v[156:159], a[180:183], a[148:151], v[156:159]\n\t"
    "v_cvt_pk_bf16_f32 v139, v142, v143\n\t"
    "v_fma_f32 v144, v144, s60, v172\n\t"
    "v_mfma_f32_16x16x32_bf16 v[156:159], a[184:187], a[152:155], v[156:159]\n\t"
    "ds_read_b128 a[172:175], v224 offset:17920\n\t"
    "v_fma_f32 v145, v145, s60, v172\n\t"
    "v_mfma_f32_16x16x32_bf16 v[156:159], a[188:191], a[156:159], v[156:159]\n\t"
    "v_exp_f32 v144, v144\n\t"
    "v_mfma_f32_16x16x32_bf16 a[236:239], a[248:251], v[136:139], a[236:239]\n\t"
    "ds_read_b128 a[176:179], v224 offset:20480\n\t"
    "v_fma_f32 v146, v146, s60, v172\n\t"
    "v_mfma_f32_16x16x32_bf16 v[96:99], a[32:35], v[136:139], v[96:99]\n\t"
    "v_exp_f32 v145, v145\n\t"
    "v_mfma_f32_16x16x32_bf16 v[100:103], a[36:39], v[136:139], v[100:103]\n\t"
    "ds_read_b128 a[180:183], v224 offset:20992\n\t"
    "v_fma_f32 v147, v147, s60, v172\n\t"
    "v_mfma_f32_16x16x32_bf16 v[104:107], a[40:43], v[136:139], v[104:107]\n\t"
    "v_exp_f32 v146, v146\n\t"
    "v_mfma_f32_16x16x32_bf16 v[108:111], a[44:47], v[136:139], v[108:111]\n\t"
    "ds_read_b128 a[184:187], v224 offset:21504\n\t"
    "v_fma_f32 v148, v148, s60, v172\n\t"
    "v_mfma_f32_16x16x32_bf16 v[112:115], a[208:211], v[136:139], v[112:115]\n\t"
    "ds_read_b128 a[188:191], v224 offset:22016\n\t"
    "v_fma_f32 v149, v149, s60, v172\n\t"
    "v_mfma_f32_16x16x32_bf16 v[116:119], a[212:215], v[136:139], v[116:119]\n\t"
    "v_exp_f32 v147, v147\n\t"
    "v_mfma_f32_16x16x32_bf16 v[120:123], a[216:219], v[136:139], v[120:123]\n\t"
    "v_exp_f32 v148, v148\n\t"
    "v_mfma_f32_16x16x32_bf16 v[124:127], a[220:223], v[136:139], v[124:127]\n\t"
    "s_waitcnt lgkmcnt(6)\n\t"
    "v_exp_f32 v149, v149\n\t"
    "v_mfma_f32_16x16x32_bf16 v[128:131], a[160:163], a[64:67], 0\n\t"
    "v_fma_f32 v150, v150, s60, v172\n\t"
    "v_fma_f32 v151, v151, s60, v172\n\t"
    "v_mfma_f32_16x16x32_bf16 v[128:131], a[164:167], a[68:71], v[128:131]\n\t"
    "s_waitcnt lgkmcnt(4)\n\t"
    "v_exp_f32 v150, v150\n\t"
    "v_mfma_f32_16x16x32_bf16 v[128:131], a[168:171], a[72:75], v[128:131]\n\t"
    "v_exp_f32 v151, v151\n\t"
    "v_mfma_f32_16x16x32_bf16 v[128:131], a[172:175], a[76:79], v[128:131]\n\t"
    "s_waitcnt lgkmcnt(2)\n\t"
    "v_cvt_pk_bf16_f32 v144, v144, v145\n\t"
    "v_cvt_pk_bf16_f32 v145, v146, v147\n\t"
    "v_mfma_f32_16x16x32_bf16 v[132:135], a[176:179], a[64:67], 0\n\t"
    "v_cvt_pk_bf16_f32 v146, v148, v149\n\t"
    "v_cvt_pk_bf16_f32 v147, v150, v151\n\t"
    "v_mfma_f32_16x16x32_bf16 v[132:135], a[180:183], a[68:71], v[132:135]\n\t"
    "s_waitcnt lgkmcnt(0)\n\t"
    "v_fma_f32 v152, v152, s60, v173\n\t"
    "v_fma_f32 v153, v153, s60, v173\n\t"
    "v_mfma_f32_16x16x32_bf16 v[132:135], a[184:187], a[72:75], v[132:135]\n\t"
    "v_exp_f32 v152, v152\n\t"
    "v_mfma_f32_16x16x32_bf16 v[132:135], a[188:191], a[76:79], v[132:135]\n\t"
    "v_exp_f32 v153, v153\n\t"
    "v_mfma_f32_16x16x32_bf16 a[240:243], a[248:251], v[144:147], a[240:243]\n\t"
    "v_fma_f32 v154, v154, s60, v173\n\t"
    "v_fma_f32 v155, v155, s60, v173\n\t"
    "v_mfma_f32_16x16x32_bf16 v[208:211], a[32:35], v[144:147], v[208:211]\n\t"
    "v_exp_f32 v154, v154\n\t"
    "v_mfma_f32_16x16x32_bf16 v[212:215], a[36:39], v[144:147], v[212:215]\n\t"
    "v_exp_f32 v155, v155\n\t"
    "v_mfma_f32_16x16x32_bf16 v[216:219], a[40:43], v[144:147], v[216:219]\n\t"
    "v_fma_f32 v156, v156, s60, v173\n\t"
    "v_fma_f32 v157, v157, s60, v173\n\t"
    "v_mfma_f32_16x16x32_bf16 v[220:223], a[44:47], v[144:147], v[220:223]\n\t"
    "v_exp_f32 v156, v156\n\t"
    "v_mfma_f32_16x16x32_bf16 v[240:243], a[208:211], v[144:147], v[240:243]\n\t"
    "v_exp_f32 v157, v157\n\t"
    "v_mfma_f32_16x16x32_bf16 v[244:247], a[212:215], v[144:147], v[244:247]\n\t"
    "v_fma_f32 v158, v158, s60, v173\n\t"
    "v_fma_f32 v159, v159, s60, v173\n\t"
    "v_mfma_f32_16x16x32_bf16 v[248:251], a[216:219], v[144:147], v[248:251]\n\t"
    "v_exp_f32 v158, v158\n\t"
    "v_mfma_f32_16x16x32_bf16 v[252:255], a[220:223], v[144:147], v[252:255]\n\t"
    "v_exp_f32 v159, v159\n\t"
    "v_mfma_f32_16x16x32_bf16 v[136:139], a[160:163], a[80:83], 0\n\t"
    "ds_read_b128 a[0:3], v232 offset:16384\n\t"
    "v_cvt_pk_bf16_f32 v152, v152, v153\n\t"
    "v_mfma_f32_16x16x32_bf16 v[136:139], a[164:167], a[84:87], v[136:139]\n\t"
    "v_cvt_pk_bf16_f32 v153, v154, v155\n\t"
    "v_cvt_pk_bf16_f32 v154, v156, v157\n\t"
    "v_mfma_f32_16x16x32_bf16 v[136:139], a[168:171], a[88:91], v[136:139]\n\t"
    "ds_read_b128 a[4:7], v232 offset:17408\n\t"
    "v_cvt_pk_bf16_f32 v155, v158, v159\n\t"
    "v_mfma_f32_16x16x32_bf16 v[136:139], a[172:175], a[92:95], v[136:139]\n\t"
    "v_fma_f32 v128, v128, s60, v168\n\t"
    "v_fma_f32 v129, v129, s60, v168\n\t"
    "v_mfma_f32_16x16x32_bf16 v[140:143], a[176:179], a[80:83], 0\n\t"
    "ds_read_b128 a[8:11], v232 offset:18432\n\t"
    "v_fma_f32 v130, v130, s60, v168\n\t"
    "v_mfma_f32_16x16x32_bf16 v[140:143], a[180:183], a[84:87], v[140:143]\n\t"
    "v_exp_f32 v128, v128\n\t"
    "v_mfma_f32_16x16x32_bf16 v[140:143], a[184:187], a[88:91], v[140:143]\n\t"
    "ds_read_b128 a[12:15], v232 offset:19456\n\t"
    "v_fma_f32 v131, v131, s60, v168\n\t"
    "v_mfma_f32_16x16x32_bf16 v[140:143], a[188:191], a[92:95], v[140:143]\n\t"
    "v_exp_f32 v129, v129\n\t"
    "v_mfma_f32_16x16x32_bf16 a[244:247], a[248:251], v[152:155], a[244:247]\n\t"
    "v_exp_f32 v130, v130\n\t"
    "v_mfma_f32_16x16x32_bf16 v[176:179], a[32:35], v[152:155], v[176:179]\n\t"
    "v_exp_f32 v131, v131\n\t"
    "v_mfma_f32_16x16x32_bf16 v[180:183], a[36:39], v[152:155], v[180:183]\n\t"
    "v_fma_f32 v132, v132, s60, v168\n\t"
    "v_fma_f32 v133, v133, s60, v168\n\t"
    "v_mfma_f32_16x16x32_bf16 v[184:187], a[40:43], v[152:155], v[184:187]\n\t"
    "v_exp_f32 v132, v132\n\t"
    "v_mfma_f32_16x16x32_bf16 v[188:191], a[44:47], v[152:155], v[188:191]\n\t"
    "v_exp_f32 v133, v133\n\t"
    "v_mfma_f32_16x16x32_bf16 v[192:195], a[208:211], v[152:155], v[192:195]\n\t"
    "ds_read_b64_tr_b16 a[208:209], v225 offset:17408\n\t"
    "v_fma_f32 v134, v134, s60, v168\n\t"
    "v_fma_f32 v135, v135, s60, v168\n\t"
    "v_mfma_f32_16x16x32_bf16 v[196:199], a[212:215], v[152:155], v[196:199]\n\t"
    "v_exp_f32 v134, v134\n\t"
    "v_exp_f32 v135, v135\n\t"
    "v_mfma_f32_16x16x32_bf16 v[200:203], a[216:219], v[152:155], v[200:203]\n\t"
    "ds_read_b64_tr_b16 a[210:211], v225 offset:21504\n\t"
    "v_cvt_pk_bf16_f32 v128, v128, v129\n\t"
    "v_mfma_f32_16x16x32_bf16 v[204:207], a[220:223], v[152:155], v[204:207]\n\t"
    "v_cvt_pk_bf16_f32 v129, v130, v131\n\t"
    "v_cvt_pk_bf16_f32 v130, v132, v133\n\t"
    "v_mfma_f32_16x16x32_bf16 v[144:147], a[160:163], a[96:99], 0\n\t"
    "ds_read_b64_tr_b16 a[212:213], v225 offset:17416\n\t"
    "v_cvt_pk_bf16_f32 v131, v134, v135\n\t"
    "v_mfma_f32_16x16x32_bf16 v[144:147], a[164:167], a[100:103], v[144:147]\n\t"
    "s_waitcnt lgkmcnt(3)\n\t"
    "v_fma_f32 v136, v136, s60, v169\n\t"
    "v_fma_f32 v137, v137, s60, v169\n\t"
    "v_mfma_f32_16x16x32_bf16 v[144:147], a[168:171], a[104:107], v[144:147]\n\t"
    "ds_read_b64_tr_b16 a[214:215], v225 offset:21512\n\t"
    "s_add_u32 s65, s53, 16384\n\t"
    "v_mfma_f32_16x16x32_bf16 v[144:147], a[172:175], a[108:111], v[144:147]\n\t"
    "ds_read_b64_tr_b16 a[216:217], v225 offset:17920\n\t"
    "s_add_u32 m0, s69, 16384\n\t"
    "v_mfma_f32_16x16x32_bf16 v[148:151], a[176:179], a[96:99], 0\n\t"
    "ds_read_b64_tr_b16 a[218:219], v225 offset:22016\n\t"
    "s_add_u32 s66, s54, 16384\n\t"
    "v_mfma_f32_16x16x32_bf16 v[148:151], a[180:183], a[100:103], v[148:151]\n\t"
    "ds_read_b64_tr_b16 a[220:221], v225 offset:17928\n\t"
    "v_fma_f32 v138, v138, s60, v169\n\t"
    "v_mfma_f32_16x16x32_bf16 v[148:151], a[184:187], a[104:107], v[148:151]\n\t"
    "ds_read_b64_tr_b16 a[222:223], v225 offset:22024\n\t"
    "v_fma_f32 v139, v139, s60, v169\n\t"
    "v_mfma_f32_16x16x32_bf16 v[148:151], a[188:191], a[108:111], v[148:151]\n\t"
    "buffer_load_dwordx4 v226, s[44:47], s65 offen offset:0 nt lds\n\t"
    "v_mfma_f32_16x16x32_bf16 a[224:227], a[248:251], v[128:131], a[224:227]\n\t"
    "s_waitcnt lgkmcnt(15)\n\t"
    "v_exp_f32 v136, v136\n\t"
    "v_mfma_f32_16x16x32_bf16 v[0:3], a[192:195], v[128:131], v[0:3]\n\t"
    "buffer_load_dwordx4 v226, s[44:47], s65 offen offset:2048 nt lds\n\t"
    "v_mfma_f32_16x16x32_bf16 v[4:7], a[196:199], v[128:131], v[4:7]\n\t"
    "buffer_store_dwordx4 a[0:3], v227, s[48:51], s66 offen offset:0 nt\n\t"
    "v_mfma_f32_16x16x32_bf16 v[8:11], a[200:203], v[128:131], v[8:11]\n\t"
    "v_exp_f32 v137, v137\n\t"
    "v_mfma_f32_16x16x32_bf16 v[12:15], a[204:207], v[128:131], v[12:15]\n\t"
    "s_waitcnt lgkmcnt(4)\n\t"
    "v_exp_f32 v138, v138\n\t"
    "v_mfma_f32_16x16x32_bf16 v[16:19], a[208:211], v[128:131], v[16:19]\n\t"
    "v_exp_f32 v139, v139\n\t"
    "v_fma_f32 v140, v140, s60, v169\n\t"
    "v_fma_f32 v141, v141, s60, v169\n\t"
    "v_fma_f32 v142, v142, s60, v169\n\t"
    "v_fma_f32 v143, v143, s60, v169\n\t"
    "v_mfma_f32_16x16x32_bf16 v[20:23], a[212:215], v[128:131], v[20:23]\n\t"
    "s_waitcnt lgkmcnt(0)\n\t"
    "v_exp_f32 v140, v140\n\t"
    "v_exp_f32 v141, v141\n\t"
    "v_mfma_f32_16x16x32_bf16 v[24:27], a[216:219], v[128:131], v[24:27]\n\t"
    "v_exp_f32 v142, v142\n\t"
    "v_mfma_f32_16x16x32_bf16 v[28:31], a[220:223], v[128:131], v[28:31]\n\t"
    "v_exp_f32 v143, v143\n\t"
    "v_mfma_f32_16x16x32_bf16 v[152:155], a[160:163], a[112:115], 0\n\t"
    "s_waitcnt vmcnt(19) lgkmcnt(0)\n\t"
    "v_cvt_pk_bf16_f32 v136, v136, v137\n\t"
    "v_cvt_pk_bf16_f32 v137, v138, v139\n\t"
    "v_mfma_f32_16x16x32_bf16 v[152:155], a[164:167], a[116:119], v[152:155]\n\t"
    "s_barrier\n\t"
    "v_cvt_pk_bf16_f32 v138, v140, v141\n\t"
    "v_cvt_pk_bf16_f32 v139, v142, v143\n\t"
    "v_mfma_f32_16x16x32_bf16 v[152:155], a[168:171], a[120:123], v[152:155]\n\t"
    "ds_read_b64_tr_b16 a[32:33], v225 offset:24576\n\t"
    "v_fma_f32 v144, v144, s60, v170\n\t"
    "v_mfma_f32_16x16x32_bf16 v[152:155], a[172:175], a[124:127], v[152:155]\n\t"
    "s_xor_b32 s64, s52, 114688\n\t"
    "v_exp_f32 v144, v144\n\t"
    "v_mfma_f32_16x16x32_bf16 v[156:159], a[176:179], a[112:115], 0\n\t"
    "ds_read_b64_tr_b16 a[34:35], v225 offset:28672\n\t"
    "v_fma_f32 v145, v145, s60, v170\n\t"
    "v_mfma_f32_16x16x32_bf16 v[156:159], a[180:183], a[116:119], v[156:159]\n\t"
    "s_add_u32 m0, s63, 16384\n\t"
    "v_exp_f32 v145, v145\n\t"
    "v_mfma_f32_16x16x32_bf16 v[156:159], a[184:187], a[120:123], v[156:159]\n\t"
    "ds_read_b64_tr_b16 a[36:37], v225 offset:24584\n\t"
    "v_fma_f32 v146, v146, s60, v170\n\t"
    "v_mfma_f32_16x16x32_bf16 v[156:159], a[188:191], a[124:127], v[156:159]\n\t"
    "buffer_load_dwordx4 v226, s[36:39], s64 offen offset:0 lds\n\t"
    "v_mfma_f32_16x16x32_bf16 a[228:231], a[248:251], v[136:139], a[228:231]\n\t"
    "ds_read_b64_tr_b16 a[38:39], v225 offset:28680\n\t"
    "v_fma_f32 v147, v147, s60, v170\n\t"
    "v_mfma_f32_16x16x32_bf16 v[32:35], a[192:195], v[136:139], v[32:35]\n\t"
    "v_exp_f32 v146, v146\n\t"
    "v_mfma_f32_16x16x32_bf16 v[36:39], a[196:199], v[136:139], v[36:39]\n\t"
    "ds_read_b64_tr_b16 a[40:41], v225 offset:25088\n\t"
    "v_fma_f32 v148, v148, s60, v170\n\t"
    "v_mfma_f32_16x16x32_bf16 v[40:43], a[200:203], v[136:139], v[40:43]\n\t"
    "v_exp_f32 v147, v147\n\t"
    "v_mfma_f32_16x16x32_bf16 v[44:47], a[204:207], v[136:139], v[44:47]\n\t"
    "ds_read_b64_tr_b16 a[42:43], v225 offset:29184\n\t"
    "v_fma_f32 v149, v149, s60, v170\n\t"
    "v_mfma_f32_16x16x32_bf16 v[48:51], a[208:211], v[136:139], v[48:51]\n\t"
    "buffer_load_dwordx4 v226, s[36:39], s64 offen offset:2048 lds\n\t"
    "v_fma_f32 v150, v150, s60, v170\n\t"
    "v_mfma_f32_16x16x32_bf16 v[52:55], a[212:215], v[136:139], v[52:55]\n\t"
    "ds_read_b64_tr_b16 a[44:45], v225 offset:25096\n\t"
    "v_fma_f32 v151, v151, s60, v170\n\t"
    "v_exp_f32 v148, v148\n\t"
    "v_mfma_f32_16x16x32_bf16 v[56:59], a[216:219], v[136:139], v[56:59]\n\t"
    "v_exp_f32 v149, v149\n\t"
    "v_mfma_f32_16x16x32_bf16 v[60:63], a[220:223], v[136:139], v[60:63]\n\t"
    "ds_read_b64_tr_b16 a[46:47], v225 offset:29192\n\t"
    "v_cvt_pk_bf16_f32 v144, v144, v145\n\t"
    "v_mfma_f32_16x16x32_bf16 v[128:131], a[160:163], a[128:131], 0\n\t"
    "v_exp_f32 v150, v150\n\t"
    "v_mfma_f32_16x16x32_bf16 v[128:131], a[164:167], a[132:135], v[128:131]\n\t"
    "s_add_u32 m0, s63, 49152\n\t"
    "v_exp_f32 v151, v151\n\t"
    "v_mfma_f32_16x16x32_bf16 v[128:131], a[168:171], a[136:139], v[128:131]\n\t"
    "buffer_load_dwordx4 v226, s[40:43], s64 offen offset:0 lds\n\t"
    "v_mfma_f32_16x16x32_bf16 v[128:131], a[172:175], a[140:143], v[128:131]\n\t"
    "v_cvt_pk_bf16_f32 v145, v146, v147\n\t"
    "v_cvt_pk_bf16_f32 v146, v148, v149\n\t"
    "v_mfma_f32_16x16x32_bf16 v[132:135], a[176:179], a[128:131], 0\n\t"
    "v_cvt_pk_bf16_f32 v147, v150, v151\n\t"
    "v_fma_f32 v152, v152, s60, v171\n\t"
    "v_mfma_f32_16x16x32_bf16 v[132:135], a[180:183], a[132:135], v[132:135]\n\t"
    "v_exp_f32 v152, v152\n\t"
    "v_mfma_f32_16x16x32_bf16 v[132:135], a[184:187], a[136:139], v[132:135]\n\t"
    "v_fma_f32 v153, v153, s60, v171\n\t"
    "v_fma_f32 v154, v154, s60, v171\n\t"
    "v_mfma_f32_16x16x32_bf16 v[132:135], a[188:191], a[140:143], v[132:135]\n\t"
    "buffer_load_dwordx4 v226, s[40:43], s64 offen offset:2048 lds\n\t"
    "v_mfma_f32_16x16x32_bf16 a[232:235], a[248:251], v[144:147], a[232:235]\n\t"
    "s_add_u32 s65, s53, 16512\n\t"
    "v_exp_f32 v153, v153\n\t"
    "v_mfma_f32_16x16x32_bf16 v[64:67], a[192:195], v[144:147], v[64:67]\n\t"
    "s_add_u32 m0, s69, 17408\n\t"
    "v_exp_f32 v154, v154\n\t"
    "v_mfma_f32_16x16x32_bf16 v[68:71], a[196:199], v[144:147], v[68:71]\n\t"
    "buffer_load_dwordx4 v226, s[44:47], s65 offen offset:0 nt lds\n\t"
    "v_mfma_f32_16x16x32_bf16 v[72:75], a[200:203], v[144:147], v[72:75]\n\t"
    "v_fma_f32 v155, v155, s60, v171\n\t"
    "v_fma_f32 v156, v156, s60, v171\n\t"
    "v_mfma_f32_16x16x32_bf16 v[76:79], a[204:207], v[144:147], v[76:79]\n\t"
    "buffer_load_dwordx4 v226, s[44:47], s65 offen offset:2048 nt lds\n\t"
    "v_mfma_f32_16x16x32_bf16 v[80:83], a[208:211], v[144:147], v[80:83]\n\t"
    "v_exp_f32 v155, v155\n\t"
    "v_mfma_f32_16x16x32_bf16 v[84:87], a[212:215], v[144:147], v[84:87]\n\t"
    "v_exp_f32 v156, v156\n\t"
    "v_fma_f32 v157, v157, s60, v171\n\t"
    "v_mfma_f32_16x16x32_bf16 v[88:91], a[216:219], v[144:147], v[88:91]\n\t"
    "v_exp_f32 v157, v157\n\t"
    "v_mfma_f32_16x16x32_bf16 v[92:95], a[220:223], v[144:147], v[92:95]\n\t"
    "v_fma_f32 v158, v158, s60, v171\n\t"
    "v_fma_f32 v159, v159, s60, v171\n\t"
    "v_mfma_f32_16x16x32_bf16 v[136:139], a[160:163], a[144:147], 0\n\t"
    "ds_read_b128 a[160:163], v224 offset:24576\n\t"
    "v_cvt_pk_bf16_f32 v152, v152, v153\n\t"
    "v_mfma_f32_16x16x32_bf16 v[136:139], a[164:167], a[148:151], v[136:139]\n\t"
    "v_exp_f32 v158, v158\n\t"
    "v_mfma_f32_16x16x32_bf16 v[136:139], a[168:171], a[152:155], v[136:139]\n\t"
    "ds_read_b128 a[164:167], v224 offset:25088\n\t"
    "v_cvt_pk_bf16_f32 v153, v154, v155\n\t"
    "v_mfma_f32_16x16x32_bf16 v[136:139], a[172:175], a[156:159], v[136:139]\n\t"
    "v_exp_f32 v159, v159\n\t"
    "v_mfma_f32_16x16x32_bf16 v[140:143], a[176:179], a[144:147], 0\n\t"
    "ds_read_b128 a[168:171], v224 offset:25600\n\t"
    "v_cvt_pk_bf16_f32 v154, v156, v157\n\t"
    "v_mfma_f32_16x16x32_bf16 v[140:143], a[180:183], a[148:151], v[140:143]\n\t"
    "v_cvt_pk_bf16_f32 v155, v158, v159\n\t"
    "v_fma_f32 v128, v128, s60, v172\n\t"
    "v_mfma_f32_16x16x32_bf16 v[140:143], a[184:187], a[152:155], v[140:143]\n\t"
    "ds_read_b128 a[172:175], v224 offset:26112\n\t"
    "v_fma_f32 v129, v129, s60, v172\n\t"
    "v_mfma_f32_16x16x32_bf16 v[140:143], a[188:191], a[156:159], v[140:143]\n\t"
    "v_exp_f32 v128, v128\n\t"
    "v_mfma_f32_16x16x32_bf16 a[236:239], a[248:251], v[152:155], a[236:239]\n\t"
    "ds_read_b128 a[176:179], v224 offset:28672\n\t"
    "v_fma_f32 v130, v130, s60, v172\n\t"
    "v_mfma_f32_16x16x32_bf16 v[96:99], a[192:195], v[152:155], v[96:99]\n\t"
    "buffer_store_dwordx4 a[4:7], v228, s[48:51], s66 offen offset:1024 nt\n\t"
    "v_mfma_f32_16x16x32_bf16 v[100:103], a[196:199], v[152:155], v[100:103]\n\t"
    "ds_read_b128 a[180:183], v224 offset:29184\n\t"
    "v_fma_f32 v131, v131, s60, v172\n\t"
    "v_mfma_f32_16x16x32_bf16 v[104:107], a[200:203], v[152:155], v[104:107]\n\t"
    "v_exp_f32 v129, v129\n\t"
    "v_mfma_f32_16x16x32_bf16 v[108:111], a[204:207], v[152:155], v[108:111]\n\t"
    "ds_read_b128 a[184:187], v224 offset:29696\n\t"
    "v_fma_f32 v132, v132, s60, v172\n\t"
    "v_mfma_f32_16x16x32_bf16 v[112:115], a[208:211], v[152:155], v[112:115]\n\t"
    "ds_read_b128 a[188:191], v224 offset:30208\n\t"
    "v_fma_f32 v133, v133, s60, v172\n\t"
    "v_mfma_f32_16x16x32_bf16 v[116:119], a[212:215], v[152:155], v[116:119]\n\t"
    "v_exp_f32 v130, v130\n\t"
    "v_mfma_f32_16x16x32_bf16 v[120:123], a[216:219], v[152:155], v[120:123]\n\t"
    "v_exp_f32 v131, v131\n\t"
    "v_mfma_f32_16x16x32_bf16 v[124:127], a[220:223], v[152:155], v[124:127]\n\t"
    "s_waitcnt lgkmcnt(6)\n\t"
    "v_exp_f32 v132, v132\n\t"
    "v_mfma_f32_16x16x32_bf16 v[144:147], a[160:163], a[64:67], 0\n\t"
    "v_exp_f32 v133, v133\n\t"
    "v_mfma_f32_16x16x32_bf16 v[144:147], a[164:167], a[68:71], v[144:147]\n\t"
    "s_waitcnt lgkmcnt(4)\n\t"
    "v_fma_f32 v134, v134, s60, v172\n\t"
    "v_fma_f32 v135, v135, s60, v172\n\t"
    "v_mfma_f32_16x16x32_bf16 v[144:147], a[168:171], a[72:75], v[144:147]\n\t"
    "v_exp_f32 v134, v134\n\t"
    "v_mfma_f32_16x16x32_bf16 v[144:147], a[172:175], a[76:79], v[144:147]\n\t"
    "s_waitcnt lgkmcnt(2)\n\t"
    "v_exp_f32 v135, v135\n\t"
    "v_mfma_f32_16x16x32_bf16 v[148:151], a[176:179], a[64:67], 0\n\t"
    "v_cvt_pk_bf16_f32 v128, v128, v129\n\t"
    "v_cvt_pk_bf16_f32 v129, v130, v131\n\t"
    "v_mfma_f32_16x16x32_bf16 v[148:151], a[180:183], a[68:71], v[148:151]\n\t"
    "s_waitcnt lgkmcnt(0)\n\t"
    "v_cvt_pk_bf16_f32 v130, v132, v133\n\t"
    "v_cvt_pk_bf16_f32 v131, v134, v135\n\t"
    "v_mfma_f32_16x16x32_bf16 v[148:151], a[184:187], a[72:75], v[148:151]\n\t"
    "v_fma_f32 v136, v136, s60, v173\n\t"
    "v_fma_f32 v137, v137, s60, v173\n\t"
    "v_mfma_f32_16x16x32_bf16 v[148:151], a[188:191], a[76:79], v[148:151]\n\t"
    "v_exp_f32 v136, v136\n\t"
    "v_mfma_f32_16x16x32_bf16 a[240:243], a[248:251], v[128:131], a[240:243]\n\t"
    "v_exp_f32 v137, v137\n\t"
    "v_mfma_f32_16x16x32_bf16 v[208:211], a[192:195], v[128:131], v[208:211]\n\t"
    "v_fma_f32 v138, v138, s60, v173\n\t"
    "v_fma_f32 v139, v139, s60, v173\n\t"
    "v_mfma_f32_16x16x32_bf16 v[212:215], a[196:199], v[128:131], v[212:215]\n\t"
    "v_exp_f32 v138, v138\n\t"
    "v_mfma_f32_16x16x32_bf16 v[216:219], a[200:203], v[128:131], v[216:219]\n\t"
    "v_exp_f32 v139, v139\n\t"
    "v_mfma_f32_16x16x32_bf16 v[220:223], a[204:207], v[128:131], v[220:223]\n\t"
    "v_fma_f32 v140, v140, s60, v173\n\t"
    "v_fma_f32 v141, v141, s60, v173\n\t"
    "v_mfma_f32_16x16x32_bf16 v[240:243], a[208:211], v[128:131], v[240:243]\n\t"
    "v_exp_f32 v140, v140\n\t"
    "v_mfma_f32_16x16x32_bf16 v[244:247], a[212:215], v[128:131], v[244:247]\n\t"
    "v_exp_f32 v141, v141\n\t"
    "v_mfma_f32_16x16x32_bf16 v[248:251], a[216:219], v[128:131], v[248:251]\n\t"
    "v_fma_f32 v142, v142, s60, v173\n\t"
    "v_fma_f32 v143, v143, s60, v173\n\t"
    "v_mfma_f32_16x16x32_bf16 v[252:255], a[220:223], v[128:131], v[252:255]\n\t"
    "v_exp_f32 v142, v142\n\t"
    "v_mfma_f32_16x16x32_bf16 v[152:155], a[160:163], a[80:83], 0\n\t"
    "v_exp_f32 v143, v143\n\t"
    "v_mfma_f32_16x16x32_bf16 v[152:155], a[164:167], a[84:87], v[152:155]\n\t"
    "v_cvt_pk_bf16_f32 v136, v136, v137\n\t"
    "v_cvt_pk_bf16_f32 v137, v138, v139\n\t"
    "v_mfma_f32_16x16x32_bf16 v[152:155], a[168:171], a[88:91], v[152:155]\n\t"
    "v_cvt_pk_bf16_f32 v138, v140, v141\n\t"
    "v_cvt_pk_bf16_f32 v139, v142, v143\n\t"
    "v_mfma_f32_16x16x32_bf16 v[152:155], a[172:175], a[92:95], v[152:155]\n\t"
    "v_fma_f32 v144, v144, s60, v168\n\t"
    "v_fma_f32 v145, v145, s60, v168\n\t"
    "v_mfma_f32_16x16x32_bf16 v[156:159], a[176:179], a[80:83], 0\n\t"
    "v_exp_f32 v144, v144\n\t"
    "v_mfma_f32_16x16x32_bf16 v[156:159], a[180:183], a[84:87], v[156:159]\n\t"
    "v_exp_f32 v145, v145\n\t"
    "v_mfma_f32_16x16x32_bf16 v[156:159], a[184:187], a[88:91], v[156:159]\n\t"
    "v_fma_f32 v146, v146, s60, v168\n\t"
    "v_fma_f32 v147, v147, s60, v168\n\t"
    "v_mfma_f32_16x16x32_bf16 v[156:159], a[188:191], a[92:95], v[156:159]\n\t"
    "v_exp_f32 v146, v146\n\t"
    "v_mfma_f32_16x16x32_bf16 a[244:247], a[248:251], v[136:139], a[244:247]\n\t"
    "v_exp_f32 v147, v147\n\t"
    "v_mfma_f32_16x16x32_bf16 v[176:179], a[192:195], v[136:139], v[176:179]\n\t"
    "v_fma_f32 v148, v148, s60, v168\n\t"
    "v_fma_f32 v149, v149, s60, v168\n\t"
    "v_mfma_f32_16x16x32_bf16 v[180:183], a[196:199], v[136:139], v[180:183]\n\t"
    "v_exp_f32 v148, v148\n\t"
    "v_mfma_f32_16x16x32_bf16 v[184:187], a[200:203], v[136:139], v[184:187]\n\t"
    "v_exp_f32 v149, v149\n\t"
    "v_mfma_f32_16x16x32_bf16 v[188:191], a[204:207], v[136:139], v[188:191]\n\t"
    "v_fma_f32 v150, v150, s60, v168\n\t"
    "v_fma_f32 v151, v151, s60, v168\n\t"
    "v_mfma_f32_16x16x32_bf16 v[192:195], a[208:211], v[136:139], v[192:195]\n\t"
    "ds_read_b64_tr_b16 a[208:209], v225 offset:25600\n\t"
    "v_cvt_pk_bf16_f32 v144, v144, v145\n\t"
    "v_mfma_f32_16x16x32_bf16 v[196:199], a[212:215], v[136:139], v[196:199]\n\t"
    "v_exp_f32 v150, v150\n\t"
    "v_mfma_f32_16x16x32_bf16 v[200:203], a[216:219], v[136:139], v[200:203]\n\t"
    "ds_read_b64_tr_b16 a[210:211], v225 offset:29696\n\t"
    "v_cvt_pk_bf16_f32 v145, v146, v147\n\t"
    "v_mfma_f32_16x16x32_bf16 v[204:207], a[220:223], v[136:139], v[204:207]\n\t"
    "v_exp_f32 v151, v151\n\t"
    "v_mfma_f32_16x16x32_bf16 v[128:131], a[160:163], a[96:99], 0\n\t"
    "ds_read_b64_tr_b16 a[212:213], v225 offset:25608\n\t"
    "v_cvt_pk_bf16_f32 v146, v148, v149\n\t"
    "v_mfma_f32_16x16x32_bf16 v[128:131], a[164:167], a[100:103], v[128:131]\n\t"
    "v_cvt_pk_bf16_f32 v147, v150, v151\n\t"
    "v_fma_f32 v152, v152, s60, v169\n\t"
    "v_mfma_f32_16x16x32_bf16 v[128:131], a[168:171], a[104:107], v[128:131]\n\t"
    "ds_read_b64_tr_b16 a[214:215], v225 offset:29704\n\t"
    "v_fma_f32 v153, v153, s60, v169\n\t"
    "v_mfma_f32_16x16x32_bf16 v[128:131], a[172:175], a[108:111], v[128:131]\n\t"
    "ds_read_b64_tr_b16 a[216:217], v225 offset:26112\n\t"
    "v_fma_f32 v154, v154, s60, v169\n\t"
    "v_mfma_f32_16x16x32_bf16 v[132:135], a[176:179], a[96:99], 0\n\t"
    "ds_read_b64_tr_b16 a[218:219], v225 offset:30208\n\t"
    "v_fma_f32 v155, v155, s60, v169\n\t"
    "v_mfma_f32_16x16x32_bf16 v[132:135], a[180:183], a[100:103], v[132:135]\n\t"
    "ds_read_b64_tr_b16 a[220:221], v225 offset:26120\n\t"
    "v_fma_f32 v156, v156, s60, v169\n\t"
    "v_mfma_f32_16x16x32_bf16 v[132:135], a[184:187], a[104:107], v[132:135]\n\t"
    "ds_read_b64_tr_b16 a[222:223], v225 offset:30216\n\t"
    "v_fma_f32 v157, v157, s60, v169\n\t"
    "v_mfma_f32_16x16x32_bf16 v[132:135], a[188:191], a[108:111], v[132:135]\n\t"
    "v_exp_f32 v152, v152\n\t"
    "v_mfma_f32_16x16x32_bf16 a[224:227], a[248:251], v[144:147], a[224:227]\n\t"
    "s_waitcnt lgkmcnt(15)\n\t"
    "v_exp_f32 v153, v153\n\t"
    "v_mfma_f32_16x16x32_bf16 v[0:3], a[32:35], v[144:147], v[0:3]\n\t"
    "v_exp_f32 v154, v154\n\t"
    "v_mfma_f32_16x16x32_bf16 v[4:7], a[36:39], v[144:147], v[4:7]\n\t"
    "v_exp_f32 v155, v155\n\t"
    "v_mfma_f32_16x16x32_bf16 v[8:11], a[40:43], v[144:147], v[8:11]\n\t"
    "buffer_store_dwordx4 a[8:11], v227, s[48:51], s66 offen offset:2048 nt\n\t"
    "v_mfma_f32_16x16x32_bf16 v[12:15], a[44:47], v[144:147], v[12:15]\n\t"
    "s_waitcnt lgkmcnt(4)\n\t"
    "v_exp_f32 v156, v156\n\t"
    "v_mfma_f32_16x16x32_bf16 v[16:19], a[208:211], v[144:147], v[16:19]\n\t"
    "v_exp_f32 v157, v157\n\t"
    "v_mfma_f32_16x16x32_bf16 v[20:23], a[212:215], v[144:147], v[20:23]\n\t"
    "s_waitcnt lgkmcnt(0)\n\t"
    "v_fma_f32 v158, v158, s60, v169\n\t"
    "v_fma_f32 v159, v159, s60, v169\n\t"
    "v_mfma_f32_16x16x32_bf16 v[24:27], a[216:219], v[144:147], v[24:27]\n\t"
    "v_exp_f32 v158, v158\n\t"
    "v_mfma_f32_16x16x32_bf16 v[28:31], a[220:223], v[144:147], v[28:31]\n\t"
    "v_exp_f32 v159, v159\n\t"
    "v_mfma_f32_16x16x32_bf16 v[136:139], a[160:163], a[112:115], 0\n\t"
    "s_waitcnt vmcnt(17) lgkmcnt(0)\n\t"
    "v_cvt_pk_bf16_f32 v152, v152, v153\n\t"
    "v_cvt_pk_bf16_f32 v153, v154, v155\n\t"
    "v_mfma_f32_16x16x32_bf16 v[136:139], a[164:167], a[116:119], v[136:139]\n\t"
    "s_barrier\n\t"
    "v_cvt_pk_bf16_f32 v154, v156, v157\n\t"
    "v_cvt_pk_bf16_f32 v155, v158, v159\n\t"
    "v_mfma_f32_16x16x32_bf16 v[136:139], a[168:171], a[120:123], v[136:139]\n\t"
    "ds_read_b64_tr_b16 a[192:193], v225 offset:0\n\t"
    "v_fma_f32 v128, v128, s60, v170\n\t"
    "v_mfma_f32_16x16x32_bf16 v[136:139], a[172:175], a[124:127], v[136:139]\n\t"
    "s_xor_b32 s64, s52, 122880\n\t"
    "v_exp_f32 v128, v128\n\t"
    "v_mfma_f32_16x16x32_bf16 v[140:143], a[176:179], a[112:115], 0\n\t"
    "ds_read_b64_tr_b16 a[194:195], v225 offset:4096\n\t"
    "v_fma_f32 v129, v129, s60, v170\n\t"
    "v_mfma_f32_16x16x32_bf16 v[140:143], a[180:183], a[116:119], v[140:143]\n\t"
    "s_add_u32 m0, s63, 24576\n\t"
    "v_exp_f32 v129, v129\n\t"
    "v_mfma_f32_16x16x32_bf16 v[140:143], a[184:187], a[120:123], v[140:143]\n\t"
    "ds_read_b64_tr_b16 a[196:197], v225 offset:8\n\t"
    "v_fma_f32 v130, v130, s60, v170\n\t"
    "v_mfma_f32_16x16x32_bf16 v[140:143], a[188:191], a[124:127], v[140:143]\n\t"
    "buffer_load_dwordx4 v226, s[36:39], s64 offen offset:0 lds\n\t"
    "v_mfma_f32_16x16x32_bf16 a[228:231], a[248:251], v[152:155], a[228:231]\n\t"
    "ds_read_b64_tr_b16 a[198:199], v225 offset:4104\n\t"
    "v_fma_f32 v131, v131, s60, v170\n\t"
    "v_mfma_f32_16x16x32_bf16 v[32:35], a[32:35], v[152:155], v[32:35]\n\t"
    "v_exp_f32 v130, v130\n\t"
    "v_mfma_f32_16x16x32_bf16 v[36:39], a[36:39], v[152:155], v[36:39]\n\t"
    "ds_read_b64_tr_b16 a[200:201], v225 offset:512\n\t"
    "v_fma_f32 v132, v132, s60, v170\n\t"
    "v_mfma_f32_16x16x32_bf16 v[40:43], a[40:43], v[152:155], v[40:43]\n\t"
    "v_exp_f32 v131, v131\n\t"
    "v_mfma_f32_16x16x32_bf16 v[44:47], a[44:47], v[152:155], v[44:47]\n\t"
    "ds_read_b64_tr_b16 a[202:203], v225 offset:4608\n\t"
    "v_fma_f32 v133, v133, s60, v170\n\t"
    "v_mfma_f32_16x16x32_bf16 v[48:51], a[208:211], v[152:155], v[48:51]\n\t"
    "buffer_load_dwordx4 v226, s[36:39], s64 offen offset:2048 lds\n\t"
    "v_mfma_f32_16x16x32_bf16 v[52:55], a[212:215], v[152:155], v[52:55]\n\t"
    "ds_read_b64_tr_b16 a[204:205], v225 offset:520\n\t"
    "v_fma_f32 v134, v134, s60, v170\n\t"
    "v_mfma_f32_16x16x32_bf16 v[56:59], a[216:219], v[152:155], v[56:59]\n\t"
    "v_exp_f32 v132, v132\n\t"
    "v_mfma_f32_16x16x32_bf16 v[60:63], a[220:223], v[152:155], v[60:63]\n\t"
    "ds_read_b64_tr_b16 a[206:207], v225 offset:4616\n\t"
    "v_fma_f32 v135, v135, s60, v170\n\t"
    "v_mfma_f32_16x16x32_bf16 v[144:147], a[160:163], a[128:131], 0\n\t"
    "v_exp_f32 v133, v133\n\t"
    "v_mfma_f32_16x16x32_bf16 v[144:147], a[164:167], a[132:135], v[144:147]\n\t"
    "s_add_u32 m0, s63, 57344\n\t"
    "v_exp_f32 v134, v134\n\t"
    "v_mfma_f32_16x16x32_bf16 v[144:147], a[168:171], a[136:139], v[144:147]\n\t"
    "buffer_load_dwordx4 v226, s[40:43], s64 offen offset:0 lds\n\t"
    "v_mfma_f32_16x16x32_bf16 v[144:147], a[172:175], a[140:143], v[144:147]\n\t"
    "v_exp_f32 v135, v135\n\t"
    "v_mfma_f32_16x16x32_bf16 v[148:151], a[176:179], a[128:131], 0\n\t"
    "v_cvt_pk_bf16_f32 v128, v128, v129\n\t"
    "v_cvt_pk_bf16_f32 v129, v130, v131\n\t"
    "v_mfma_f32_16x16x32_bf16 v[148:151], a[180:183], a[132:135], v[148:151]\n\t"
    "v_cvt_pk_bf16_f32 v130, v132, v133\n\t"
    "v_cvt_pk_bf16_f32 v131, v134, v135\n\t"
    "v_mfma_f32_16x16x32_bf16 v[148:151], a[184:187], a[136:139], v[148:151]\n\t"
    "v_fma_f32 v136, v136, s60, v171\n\t"
    "v_fma_f32 v137, v137, s60, v171\n\t"
    "v_mfma_f32_16x16x32_bf16 v[148:151], a[188:191], a[140:143], v[148:151]\n\t"
    "buffer_load_dwordx4 v226, s[40:43], s64 offen offset:2048 lds\n\t"
    "v_mfma_f32_16x16x32_bf16 a[232:235], a[248:251], v[128:131], a[232:235]\n\t"
    "s_mov_b32 s52, s71\n\t"
    "v_exp_f32 v136, v136\n\t"
    "v_mfma_f32_16x16x32_bf16 v[64:67], a[32:35], v[128:131], v[64:67]\n\t"
    "v_exp_f32 v137, v137\n\t"
    "v_mfma_f32_16x16x32_bf16 v[68:71], a[36:39], v[128:131], v[68:71]\n\t"
    "v_fma_f32 v138, v138, s60, v171\n\t"
    "v_fma_f32 v139, v139, s60, v171\n\t"
    "v_mfma_f32_16x16x32_bf16 v[72:75], a[40:43], v[128:131], v[72:75]\n\t"
    "v_exp_f32 v138, v138\n\t"
    "v_mfma_f32_16x16x32_bf16 v[76:79], a[44:47], v[128:131], v[76:79]\n\t"
    "v_exp_f32 v139, v139\n\t"
    "v_mfma_f32_16x16x32_bf16 v[80:83], a[208:211], v[128:131], v[80:83]\n\t"
    "v_fma_f32 v140, v140, s60, v171\n\t"
    "v_fma_f32 v141, v141, s60, v171\n\t"
    "v_mfma_f32_16x16x32_bf16 v[84:87], a[212:215], v[128:131], v[84:87]\n\t"
    "v_exp_f32 v140, v140\n\t"
    "v_mfma_f32_16x16x32_bf16 v[88:91], a[216:219], v[128:131], v[88:91]\n\t"
    "v_exp_f32 v141, v141\n\t"
    "v_mfma_f32_16x16x32_bf16 v[92:95], a[220:223], v[128:131], v[92:95]\n\t"
    "v_fma_f32 v142, v142, s60, v171\n\t"
    "v_fma_f32 v143, v143, s60, v171\n\t"
    "v_mfma_f32_16x16x32_bf16 v[152:155], a[160:163], a[144:147], 0\n\t"
    "ds_read_b128 a[160:163], v224 offset:0\n\t"
    "v_cvt_pk_bf16_f32 v136, v136, v137\n\t"
    "v_mfma_f32_16x16x32_bf16 v[152:155], a[164:167], a[148:151], v[152:155]\n\t"
    "v_exp_f32 v142, v142\n\t"
    "v_mfma_f32_16x16x32_bf16 v[152:155], a[168:171], a[152:155], v[152:155]\n\t"
    "ds_read_b128 a[164:167], v224 offset:512\n\t"
    "v_cvt_pk_bf16_f32 v137, v138, v139\n\t"
    "v_mfma_f32_16x16x32_bf16 v[152:155], a[172:175], a[156:159], v[152:155]\n\t"
    "v_exp_f32 v143, v143\n\t"
    "v_mfma_f32_16x16x32_bf16 v[156:159], a[176:179], a[144:147], 0\n\t"
    "ds_read_b128 a[168:171], v224 offset:1024\n\t"
    "v_cvt_pk_bf16_f32 v138, v140, v141\n\t"
    "v_mfma_f32_16x16x32_bf16 v[156:159], a[180:183], a[148:151], v[156:159]\n\t"
    "v_cvt_pk_bf16_f32 v139, v142, v143\n\t"
    "v_fma_f32 v144, v144, s60, v172\n\t"
    "v_mfma_f32_16x16x32_bf16 v[156:159], a[184:187], a[152:155], v[156:159]\n\t"
    "ds_read_b128 a[172:175], v224 offset:1536\n\t"
    "v_fma_f32 v145, v145, s60, v172\n\t"
    "v_mfma_f32_16x16x32_bf16 v[156:159], a[188:191], a[156:159], v[156:159]\n\t"
    "v_exp_f32 v144, v144\n\t"
    "v_mfma_f32_16x16x32_bf16 a[236:239], a[248:251], v[136:139], a[236:239]\n\t"
    "ds_read_b128 a[176:179], v224 offset:4096\n\t"
    "v_fma_f32 v146, v146, s60, v172\n\t"
    "v_mfma_f32_16x16x32_bf16 v[96:99], a[32:35], v[136:139], v[96:99]\n\t"
    "v_exp_f32 v145, v145\n\t"
    "v_mfma_f32_16x16x32_bf16 v[100:103], a[36:39], v[136:139], v[100:103]\n\t"
    "ds_read_b128 a[180:183], v224 offset:4608\n\t"
    "v_fma_f32 v147, v147, s60, v172\n\t"
    "v_mfma_f32_16x16x32_bf16 v[104:107], a[40:43], v[136:139], v[104:107]\n\t"
    "v_exp_f32 v146, v146\n\t"
    "v_mfma_f32_16x16x32_bf16 v[108:111], a[44:47], v[136:139], v[108:111]\n\t"
    "ds_read_b128 a[184:187], v224 offset:5120\n\t"
    "v_fma_f32 v148, v148, s60, v172\n\t"
    "v_mfma_f32_16x16x32_bf16 v[112:115], a[208:211], v[136:139], v[112:115]\n\t"
    "ds_read_b128 a[188:191], v224 offset:5632\n\t"
    "v_fma_f32 v149, v149, s60, v172\n\t"
    "v_mfma_f32_16x16x32_bf16 v[116:119], a[212:215], v[136:139], v[116:119]\n\t"
    "buffer_store_dwordx4 a[12:15], v228, s[48:51], s66 offen offset:3072 nt\n\t"
    "v_mfma_f32_16x16x32_bf16 v[120:123], a[216:219], v[136:139], v[120:123]\n\t"
    "v_exp_f32 v147, v147\n\t"
    "v_mfma_f32_16x16x32_bf16 v[124:127], a[220:223], v[136:139], v[124:127]\n\t"
    "s_waitcnt lgkmcnt(6)\n\t"
    "v_exp_f32 v148, v148\n\t"
    "v_mfma_f32_16x16x32_bf16 v[128:131], a[160:163], a[64:67], 0\n\t"
    "v_exp_f32 v149, v149\n\t"
    "v_mfma_f32_16x16x32_bf16 v[128:131], a[164:167], a[68:71], v[128:131]\n\t"
    "s_waitcnt lgkmcnt(4)\n\t"
    "v_fma_f32 v150, v150, s60, v172\n\t"
    "v_fma_f32 v151, v151, s60, v172\n\t"
    "v_mfma_f32_16x16x32_bf16 v[128:131], a[168:171], a[72:75], v[128:131]\n\t"
    "v_exp_f32 v150, v150\n\t"
    "v_mfma_f32_16x16x32_bf16 v[128:131], a[172:175], a[76:79], v[128:131]\n\t"
    "s_waitcnt lgkmcnt(2)\n\t"
    "v_exp_f32 v151, v151\n\t"
    "v_mfma_f32_16x16x32_bf16 v[132:135], a[176:179], a[64:67], 0\n\t"
    "v_cvt_pk_bf16_f32 v144, v144, v145\n\t"
    "v_cvt_pk_bf16_f32 v145, v146, v147\n\t"
    "v_mfma_f32_16x16x32_bf16 v[132:135], a[180:183], a[68:71], v[132:135]\n\t"
    "s_waitcnt lgkmcnt(0)\n\t"
    "v_cvt_pk_bf16_f32 v146, v148, v149\n\t"
    "v_cvt_pk_bf16_f32 v147, v150, v151\n\t"
    "v_mfma_f32_16x16x32_bf16 v[132:135], a[184:187], a[72:75], v[132:135]\n\t"
    "v_fma_f32 v152, v152, s60, v173\n\t"
    "v_fma_f32 v153, v153, s60, v173\n\t"
    "v_mfma_f32_16x16x32_bf16 v[132:135], a[188:191], a[76:79], v[132:135]\n\t"
    "v_exp_f32 v152, v152\n\t"
    "v_mfma_f32_16x16x32_bf16 a[240:243], a[248:251], v[144:147], a[240:243]\n\t"
    "v_exp_f32 v153, v153\n\t"
    "v_mfma_f32_16x16x32_bf16 v[208:211], a[32:35], v[144:147], v[208:211]\n\t"
    "v_fma_f32 v154, v154, s60, v173\n\t"
    "v_fma_f32 v155, v155, s60, v173\n\t"
    "v_mfma_f32_16x16x32_bf16 v[212:215], a[36:39], v[144:147], v[212:215]\n\t"
    "v_exp_f32 v154, v154\n\t"
    "v_mfma_f32_16x16x32_bf16 v[216:219], a[40:43], v[144:147], v[216:219]\n\t"
    "v_exp_f32 v155, v155\n\t"
    "v_mfma_f32_16x16x32_bf16 v[220:223], a[44:47], v[144:147], v[220:223]\n\t"
    "v_fma_f32 v156, v156, s60, v173\n\t"
    "v_fma_f32 v157, v157, s60, v173\n\t"
    "v_mfma_f32_16x16x32_bf16 v[240:243], a[208:211], v[144:147], v[240:243]\n\t"
    "v_exp_f32 v156, v156\n\t"
    "v_mfma_f32_16x16x32_bf16 v[244:247], a[212:215], v[144:147], v[244:247]\n\t"
    "v_exp_f32 v157, v157\n\t"
    "v_mfma_f32_16x16x32_bf16 v[248:251], a[216:219], v[144:147], v[248:251]\n\t"
    "v_fma_f32 v158, v158, s60, v173\n\t"
    "v_fma_f32 v159, v159, s60, v173\n\t"
    "v_mfma_f32_16x16x32_bf16 v[252:255], a[220:223], v[144:147], v[252:255]\n\t"
    "v_exp_f32 v158, v158\n\t"
    "v_mfma_f32_16x16x32_bf16 v[136:139], a[160:163], a[80:83], 0\n\t"
    "v_exp_f32 v159, v159\n\t"
    "v_mfma_f32_16x16x32_bf16 v[136:139], a[164:167], a[84:87], v[136:139]\n\t"
    "v_cvt_pk_bf16_f32 v152, v152, v153\n\t"
    "v_cvt_pk_bf16_f32 v153, v154, v155\n\t"
    "v_mfma_f32_16x16x32_bf16 v[136:139], a[168:171], a[88:91], v[136:139]\n\t"
    "v_cvt_pk_bf16_f32 v154, v156, v157\n\t"
    "v_cvt_pk_bf16_f32 v155, v158, v159\n\t"
    "v_mfma_f32_16x16x32_bf16 v[136:139], a[172:175], a[92:95], v[136:139]\n\t"
    "v_fma_f32 v128, v128, s60, v168\n\t"
    "v_fma_f32 v129, v129, s60, v168\n\t"
    "v_mfma_f32_16x16x32_bf16 v[140:143], a[176:179], a[80:83], 0\n\t"
    "v_exp_f32 v128, v128\n\t"
    "v_mfma_f32_16x16x32_bf16 v[140:143], a[180:183], a[84:87], v[140:143]\n\t"
    "v_exp_f32 v129, v129\n\t"
    "v_mfma_f32_16x16x32_bf16 v[140:143], a[184:187], a[88:91], v[140:143]\n\t"
    "v_fma_f32 v130, v130, s60, v168\n\t"
    "v_fma_f32 v131, v131, s60, v168\n\t"
    "v_mfma_f32_16x16x32_bf16 v[140:143], a[188:191], a[92:95], v[140:143]\n\t"
    "v_exp_f32 v130, v130\n\t"
    "v_mfma_f32_16x16x32_bf16 a[244:247], a[248:251], v[152:155], a[244:247]\n\t"
    "v_exp_f32 v131, v131\n\t"
    "v_mfma_f32_16x16x32_bf16 v[176:179], a[32:35], v[152:155], v[176:179]\n\t"
    "v_fma_f32 v132, v132, s60, v168\n\t"
    "v_fma_f32 v133, v133, s60, v168\n\t"
    "v_mfma_f32_16x16x32_bf16 v[180:183], a[36:39], v[152:155], v[180:183]\n\t"
    "v_exp_f32 v132, v132\n\t"
    "v_mfma_f32_16x16x32_bf16 v[184:187], a[40:43], v[152:155], v[184:187]\n\t"
    "v_exp_f32 v133, v133\n\t"
    "v_mfma_f32_16x16x32_bf16 v[188:191], a[44:47], v[152:155], v[188:191]\n\t"
    "v_fma_f32 v134, v134, s60, v168\n\t"
    "v_fma_f32 v135, v135, s60, v168\n\t"
    "v_mfma_f32_16x16x32_bf16 v[192:195], a[208:211], v[152:155], v[192:195]\n\t"
    "ds_read_b64_tr_b16 a[208:209], v225 offset:1024\n\t"
    "v_cvt_pk_bf16_f32 v128, v128, v129\n\t"
    "v_mfma_f32_16x16x32_bf16 v[196:199], a[212:215], v[152:155], v[196:199]\n\t"
    "v_exp_f32 v134, v134\n\t"
    "v_mfma_f32_16x16x32_bf16 v[200:203], a[216:219], v[152:155], v[200:203]\n\t"
    "ds_read_b64_tr_b16 a[210:211], v225 offset:5120\n\t"
    "v_cvt_pk_bf16_f32 v129, v130, v131\n\t"
    "v_mfma_f32_16x16x32_bf16 v[204:207], a[220:223], v[152:155], v[204:207]\n\t"
    "v_exp_f32 v135, v135\n\t"
    "v_mfma_f32_16x16x32_bf16 v[144:147], a[160:163], a[96:99], 0\n\t"
    "ds_read_b64_tr_b16 a[212:213], v225 offset:1032\n\t"
    "v_cvt_pk_bf16_f32 v130, v132, v133\n\t"
    "v_mfma_f32_16x16x32_bf16 v[144:147], a[164:167], a[100:103], v[144:147]\n\t"
    "v_cvt_pk_bf16_f32 v131, v134, v135\n\t"
    "v_fma_f32 v136, v136, s60, v169\n\t"
    "v_mfma_f32_16x16x32_bf16 v[144:147], a[168:171], a[104:107], v[144:147]\n\t"
    "ds_read_b64_tr_b16 a[214:215], v225 offset:5128\n\t"
    "v_fma_f32 v137, v137, s60, v169\n\t"
    "v_mfma_f32_16x16x32_bf16 v[144:147], a[172:175], a[108:111], v[144:147]\n\t"
    "ds_read_b64_tr_b16 a[216:217], v225 offset:1536\n\t"
    "v_fma_f32 v138, v138, s60, v169\n\t"
    "v_mfma_f32_16x16x32_bf16 v[148:151], a[176:179], a[96:99], 0\n\t"
    "ds_read_b64_tr_b16 a[218:219], v225 offset:5632\n\t"
    "v_fma_f32 v139, v139, s60, v169\n\t"
    "v_mfma_f32_16x16x32_bf16 v[148:151], a[180:183], a[100:103], v[148:151]\n\t"
    "ds_read_b64_tr_b16 a[220:221], v225 offset:1544\n\t"
    "v_fma_f32 v140, v140, s60, v169\n\t"
    "v_mfma_f32_16x16x32_bf16 v[148:151], a[184:187], a[104:107], v[148:151]\n\t"
    "ds_read_b64_tr_b16 a[222:223], v225 offset:5640\n\t"
    "v_fma_f32 v141, v141, s60, v169\n\t"
    "v_mfma_f32_16x16x32_bf16 v[148:151], a[188:191], a[108:111], v[148:151]\n\t"
    "v_exp_f32 v136, v136\n\t"
    "v_mfma_f32_16x16x32_bf16 a[224:227], a[248:251], v[128:131], a[224:227]\n\t"
    "s_waitcnt lgkmcnt(15)\n\t"
    "v_exp_f32 v137, v137\n\t"
    "v_mfma_f32_16x16x32_bf16 v[0:3], a[192:195], v[128:131], v[0:3]\n\t"
    "v_exp_f32 v138, v138\n\t"
    "v_mfma_f32_16x16x32_bf16 v[4:7], a[196:199], v[128:131], v[4:7]\n\t"
    "v_exp_f32 v139, v139\n\t"
    "v_mfma_f32_16x16x32_bf16 v[8:11], a[200:203], v[128:131], v[8:11]\n\t"
    "v_exp_f32 v140, v140\n\t"
    "v_mfma_f32_16x16x32_bf16 v[12:15], a[204:207], v[128:131], v[12:15]\n\t"
    "s_waitcnt lgkmcnt(4)\n\t"
    "v_exp_f32 v141, v141\n\t"
    "v_mfma_f32_16x16x32_bf16 v[16:19], a[208:211], v[128:131], v[16:19]\n\t"
    "v_fma_f32 v142, v142, s60, v169\n\t"
    "v_fma_f32 v143, v143, s60, v169\n\t"
    "v_cvt_pk_bf16_f32 v136, v136, v137\n\t"
    "v_cvt_pk_bf16_f32 v137, v138, v139\n\t"
    "v_mfma_f32_16x16x32_bf16 v[20:23], a[212:215], v[128:131], v[20:23]\n\t"
    "s_waitcnt lgkmcnt(0)\n\t"
    "v_exp_f32 v142, v142\n\t"
    "v_exp_f32 v143, v143\n\t"
    "v_mfma_f32_16x16x32_bf16 v[24:27], a[216:219], v[128:131], v[24:27]\n\t"
    "v_cvt_pk_bf16_f32 v138, v140, v141\n\t"
    "v_cvt_pk_bf16_f32 v139, v142, v143\n\t"
    "v_mfma_f32_16x16x32_bf16 v[28:31], a[220:223], v[128:131], v[28:31]\n\t"
    "v_fma_f32 v144, v144, s60, v170\n\t"
    "v_fma_f32 v145, v145, s60, v170\n\t"
    "v_mfma_f32_16x16x32_bf16 v[152:155], a[160:163], a[112:115], 0\n\t"
    "ds_read_b128 a[0:3], v232 offset:20480\n\t"
    "s_waitcnt vmcnt(16) lgkmcnt(1)\n\t"
    "v_mfma_f32_16x16x32_bf16 v[152:155], a[164:167], a[116:119], v[152:155]\n\t"
    "s_barrier\n\t"
    "v_exp_f32 v144, v144\n\t"
    "v_mfma_f32_16x16x32_bf16 v[152:155], a[168:171], a[120:123], v[152:155]\n\t"
    "ds_read_b64_tr_b16 a[32:33], v225 offset:8192\n\t"
    "v_fma_f32 v146, v146, s60, v170\n\t"
    "v_mfma_f32_16x16x32_bf16 v[152:155], a[172:175], a[124:127], v[152:155]\n\t"
    "ds_read_b128 a[4:7], v232 offset:21504\n\t"
    "s_xor_b32 s64, s52, 0\n\t"
    "v_mfma_f32_16x16x32_bf16 v[156:159], a[176:179], a[112:115], 0\n\t"
    "ds_read_b64_tr_b16 a[34:35], v225 offset:12288\n\t"
    "v_fma_f32 v147, v147, s60, v170\n\t"
    "v_mfma_f32_16x16x32_bf16 v[156:159], a[180:183], a[116:119], v[156:159]\n\t"
    "s_add_u32 m0, s63, 0\n\t"
    "v_exp_f32 v145, v145\n\t"
    "v_mfma_f32_16x16x32_bf16 v[156:159], a[184:187], a[120:123], v[156:159]\n\t"
    "ds_read_b64_tr_b16 a[36:37], v225 offset:8200\n\t"
    "v_fma_f32 v148, v148, s60, v170\n\t"
    "v_mfma_f32_16x16x32_bf16 v[156:159], a[188:191], a[124:127], v[156:159]\n\t"
    "buffer_load_dwordx4 v226, s[36:39], s64 offen offset:0 lds\n\t"
    "v_mfma_f32_16x16x32_bf16 a[228:231], a[248:251], v[136:139], a[228:231]\n\t"
    "ds_read_b64_tr_b16 a[38:39], v225 offset:12296\n\t"
    "v_fma_f32 v149, v149, s60, v170\n\t"
    "v_mfma_f32_16x16x32_bf16 v[32:35], a[192:195], v[136:139], v[32:35]\n\t"
    "ds_read_b128 a[8:11], v232 offset:22528\n\t"
    "v_fma_f32 v150, v150, s60, v170\n\t"
    "v_mfma_f32_16x16x32_bf16 v[36:39], a[196:199], v[136:139], v[36:39]\n\t"
    "ds_read_b64_tr_b16 a[40:41], v225 offset:8704\n\t"
    "v_fma_f32 v151, v151, s60, v170\n\t"
    "v_mfma_f32_16x16x32_bf16 v[40:43], a[200:203], v[136:139], v[40:43]\n\t"
    "ds_read_b128 a[12:15], v232 offset:23552\n\t"
    "v_cvt_pk_bf16_f32 v144, v144, v145\n\t"
    "v_mfma_f32_16x16x32_bf16 v[44:47], a[204:207], v[136:139], v[44:47]\n\t"
    "ds_read_b64_tr_b16 a[42:43], v225 offset:12800\n\t"
    "v_fma_f32 v152, v152, s60, v171\n\t"
    "v_exp_f32 v146, v146\n\t"
    "v_mfma_f32_16x16x32_bf16 v[48:51], a[208:211], v[136:139], v[48:51]\n\t"
    "buffer_load_dwordx4 v226, s[36:39], s64 offen offset:2048 lds\n\t"
    "v_exp_f32 v147, v147\n\t"
    "v_mfma_f32_16x16x32_bf16 v[52:55], a[212:215], v[136:139], v[52:55]\n\t"
    "ds_read_b64_tr_b16 a[44:45], v225 offset:8712\n\t"
    "v_cvt_pk_bf16_f32 v145, v146, v147\n\t"
    "v_exp_f32 v148, v148\n\t"
    "v_mfma_f32_16x16x32_bf16 v[56:59], a[216:219], v[136:139], v[56:59]\n\t"
    "v_exp_f32 v149, v149\n\t"
    "v_mfma_f32_16x16x32_bf16 v[60:63], a[220:223], v[136:139], v[60:63]\n\t"
    "ds_read_b64_tr_b16 a[46:47], v225 offset:12808\n\t"
    "v_cvt_pk_bf16_f32 v146, v148, v149\n\t"
    "v_mfma_f32_16x16x32_bf16 v[128:131], a[160:163], a[128:131], 0\n\t"
    "v_exp_f32 v150, v150\n\t"
    "v_mfma_f32_16x16x32_bf16 v[128:131], a[164:167], a[132:135], v[128:131]\n\t"
    "s_add_u32 m0, s63, 32768\n\t"
    "v_exp_f32 v151, v151\n\t"
    "v_mfma_f32_16x16x32_bf16 v[128:131], a[168:171], a[136:139], v[128:131]\n\t"
    "buffer_load_dwordx4 v226, s[40:43], s64 offen offset:0 lds\n\t"
    "v_mfma_f32_16x16x32_bf16 v[128:131], a[172:175], a[140:143], v[128:131]\n\t"
    "v_exp_f32 v152, v152\n\t"
    "v_mfma_f32_16x16x32_bf16 v[132:135], a[176:179], a[128:131], 0\n\t"
    "v_cvt_pk_bf16_f32 v147, v150, v151\n\t"
    "v_fma_f32 v153, v153, s60, v171\n\t"
    "v_mfma_f32_16x16x32_bf16 v[132:135], a[180:183], a[132:135], v[132:135]\n\t"
    "v_exp_f32 v153, v153\n\t"
    "v_mfma_f32_16x16x32_bf16 v[132:135], a[184:187], a[136:139], v[132:135]\n\t"
    "s_waitcnt lgkmcnt(3)\n\t"
    "v_fma_f32 v154, v154, s60, v171\n\t"
    "v_fma_f32 v155, v155, s60, v171\n\t"
    "v_mfma_f32_16x16x32_bf16 v[132:135], a[188:191], a[140:143], v[132:135]\n\t"
    "buffer_load_dwordx4 v226, s[40:43], s64 offen offset:2048 lds\n\t"
    "v_mfma_f32_16x16x32_bf16 a[232:235], a[248:251], v[144:147], a[232:235]\n\t"
    "s_add_u32 s65, s53, 20480\n\t"
    "v_exp_f32 v154, v154\n\t"
    "v_mfma_f32_16x16x32_bf16 v[64:67], a[192:195], v[144:147], v[64:67]\n\t"
    "s_add_u32 m0, s69, 20480\n\t"
    "v_exp_f32 v155, v155\n\t"
    "v_mfma_f32_16x16x32_bf16 v[68:71], a[196:199], v[144:147], v[68:71]\n\t"
    "buffer_load_dwordx4 v226, s[44:47], s65 offen offset:0 nt lds\n\t"
    "v_mfma_f32_16x16x32_bf16 v[72:75], a[200:203], v[144:147], v[72:75]\n\t"
    "s_add_u32 s66, s54, 20480\n\t"
    "v_fma_f32 v156, v156, s60, v171\n\t"
    "v_fma_f32 v157, v157, s60, v171\n\t"
    "v_mfma_f32_16x16x32_bf16 v[76:79], a[204:207], v[144:147], v[76:79]\n\t"
    "buffer_load_dwordx4 v226, s[44:47], s65 offen offset:2048 nt lds\n\t"
    "v_mfma_f32_16x16x32_bf16 v[80:83], a[208:211], v[144:147], v[80:83]\n\t"
    "buffer_store_dwordx4 a[0:3], v227, s[48:51], s66 offen offset:0 nt\n\t"
    "v_mfma_f32_16x16x32_bf16 v[84:87], a[212:215], v[144:147], v[84:87]\n\t"
    "v_exp_f32 v156, v156\n\t"
    "v_fma_f32 v158, v158, s60, v171\n\t"
    "v_fma_f32 v159, v159, s60, v171\n\t"
    "v_mfma_f32_16x16x32_bf16 v[88:91], a[216:219], v[144:147], v[88:91]\n\t"
    "v_exp_f32 v157, v157\n\t"
    "v_mfma_f32_16x16x32_bf16 v[92:95], a[220:223], v[144:147], v[92:95]\n\t"
    "v_exp_f32 v158, v158\n\t"
    "v_mfma_f32_16x16x32_bf16 v[136:139], a[160:163], a[144:147], 0\n\t"
    "ds_read_b128 a[160:163], v224 offset:8192\n\t"
    "v_cvt_pk_bf16_f32 v152, v152, v153\n\t"
    "v_mfma_f32_16x16x32_bf16 v[136:139], a[164:167], a[148:151], v[136:139]\n\t"
    "v_exp_f32 v159, v159\n\t"
    "v_mfma_f32_16x16x32_bf16 v[136:139], a[168:171], a[152:155], v[136:139]\n\t"
    "ds_read_b128 a[164:167], v224 offset:8704\n\t"
    "v_cvt_pk_bf16_f32 v153, v154, v155\n\t"
    "v_mfma_f32_16x16x32_bf16 v[136:139], a[172:175], a[156:159], v[136:139]\n\t"
    "v_cvt_pk_bf16_f32 v154, v156, v157\n\t"
    "v_cvt_pk_bf16_f32 v155, v158, v159\n\t"
    "v_mfma_f32_16x16x32_bf16 v[140:143], a[176:179], a[144:147], 0\n\t"
    "ds_read_b128 a[168:171], v224 offset:9216\n\t"
    "v_fma_f32 v128, v128, s60, v172\n\t"
    "v_mfma_f32_16x16x32_bf16 v[140:143], a[180:183], a[148:151], v[140:143]\n\t"
    "s_add_u32 s65, s53, 20608\n\t"
    "v_exp_f32 v128, v128\n\t"
    "v_mfma_f32_16x16x32_bf16 v[140:143], a[184:187], a[152:155], v[140:143]\n\t"
    "ds_read_b128 a[172:175], v224 offset:9728\n\t"
    "v_fma_f32 v129, v129, s60, v172\n\t"
    "v_mfma_f32_16x16x32_bf16 v[140:143], a[188:191], a[156:159], v[140:143]\n\t"
    "s_add_u32 m0, s69, 21504\n\t"
    "v_exp_f32 v129, v129\n\t"
    "v_mfma_f32_16x16x32_bf16 a[236:239], a[248:251], v[152:155], a[236:239]\n\t"
    "ds_read_b128 a[176:179], v224 offset:12288\n\t"
    "v_fma_f32 v130, v130, s60, v172\n\t"
    "v_mfma_f32_16x16x32_bf16 v[96:99], a[192:195], v[152:155], v[96:99]\n\t"
    "buffer_load_dwordx4 v226, s[44:47], s65 offen offset:0 nt lds\n\t"
    "v_mfma_f32_16x16x32_bf16 v[100:103], a[196:199], v[152:155], v[100:103]\n\t"
    "ds_read_b128 a[180:183], v224 offset:12800\n\t"
    "v_fma_f32 v131, v131, s60, v172\n\t"
    "v_mfma_f32_16x16x32_bf16 v[104:107], a[200:203], v[152:155], v[104:107]\n\t"
    "buffer_load_dwordx4 v226, s[44:47], s65 offen offset:2048 nt lds\n\t"
    "v_mfma_f32_16x16x32_bf16 v[108:111], a[204:207], v[152:155], v[108:111]\n\t"
    "ds_read_b128 a[184:187], v224 offset:13312\n\t"
    "v_fma_f32 v132, v132, s60, v172\n\t"
    "v_mfma_f32_16x16x32_bf16 v[112:115], a[208:211], v[152:155], v[112:115]\n\t"
    "ds_read_b128 a[188:191], v224 offset:13824\n\t"
    "v_fma_f32 v133, v133, s60, v172\n\t"
    "v_mfma_f32_16x16x32_bf16 v[116:119], a[212:215], v[152:155], v[116:119]\n\t"
    "v_exp_f32 v130, v130\n\t"
    "v_mfma_f32_16x16x32_bf16 v[120:123], a[216:219], v[152:155], v[120:123]\n\t"
    "v_exp_f32 v131, v131\n\t"
    "v_mfma_f32_16x16x32_bf16 v[124:127], a[220:223], v[152:155], v[124:127]\n\t"
    "s_waitcnt lgkmcnt(6)\n\t"
    "v_exp_f32 v132, v132\n\t"
    "v_mfma_f32_16x16x32_bf16 v[144:147], a[160:163], a[64:67], 0\n\t"
    "v_exp_f32 v133, v133\n\t"
    "v_mfma_f32_16x16x32_bf16 v[144:147], a[164:167], a[68:71], v[144:147]\n\t"
    "s_waitcnt lgkmcnt(4)\n\t"
    "v_fma_f32 v134, v134, s60, v172\n\t"
    "v_fma_f32 v135, v135, s60, v172\n\t"
    "v_mfma_f32_16x16x32_bf16 v[144:147], a[168:171], a[72:75], v[144:147]\n\t"
    "v_exp_f32 v134, v134\n\t"
    "v_mfma_f32_16x16x32_bf16 v[144:147], a[172:175], a[76:79], v[144:147]\n\t"
    "s_waitcnt lgkmcnt(2)\n\t"
    "v_exp_f32 v135, v135\n\t"
    "v_mfma_f32_16x16x32_bf16 v[148:151], a[176:179], a[64:67], 0\n\t"
    "v_cvt_pk_bf16_f32 v128, v128, v129\n\t"
    "v_cvt_pk_bf16_f32 v129, v130, v131\n\t"
    "v_mfma_f32_16x16x32_bf16 v[148:151], a[180:183], a[68:71], v[148:151]\n\t"
    "s_waitcnt lgkmcnt(0)\n\t"
    "v_cvt_pk_bf16_f32 v130, v132, v133\n\t"
    "v_cvt_pk_bf16_f32 v131, v134, v135\n\t"
    "v_mfma_f32_16x16x32_bf16 v[148:151], a[184:187], a[72:75], v[148:151]\n\t"
    "v_fma_f32 v136, v136, s60, v173\n\t"
    "v_fma_f32 v137, v137, s60, v173\n\t"
    "v_mfma_f32_16x16x32_bf16 v[148:151], a[188:191], a[76:79], v[148:151]\n\t"
    "v_exp_f32 v136, v136\n\t"
    "v_mfma_f32_16x16x32_bf16 a[240:243], a[248:251], v[128:131], a[240:243]\n\t"
    "v_exp_f32 v137, v137\n\t"
    "v_mfma_f32_16x16x32_bf16 v[208:211], a[192:195], v[128:131], v[208:211]\n\t"
    "v_fma_f32 v138, v138, s60, v173\n\t"
    "v_fma_f32 v139, v139, s60, v173\n\t"
    "v_mfma_f32_16x16x32_bf16 v[212:215], a[196:199], v[128:131], v[212:215]\n\t"
    "v_exp_f32 v138, v138\n\t"
    "v_mfma_f32_16x16x32_bf16 v[216:219], a[200:203], v[128:131], v[216:219]\n\t"
    "v_exp_f32 v139, v139\n\t"
    "v_mfma_f32_16x16x32_bf16 v[220:223], a[204:207], v[128:131], v[220:223]\n\t"
    "v_fma_f32 v140, v140, s60, v173\n\t"
    "v_fma_f32 v141, v141, s60, v173\n\t"
    "v_mfma_f32_16x16x32_bf16 v[240:243], a[208:211], v[128:131], v[240:243]\n\t"
    "v_exp_f32 v140, v140\n\t"
    "v_mfma_f32_16x16x32_bf16 v[244:247], a[212:215], v[128:131], v[244:247]\n\t"
    "v_exp_f32 v141, v141\n\t"
    "v_mfma_f32_16x16x32_bf16 v[248:251], a[216:219], v[128:131], v[248:251]\n\t"
    "v_fma_f32 v142, v142, s60, v173\n\t"
    "v_fma_f32 v143, v143, s60, v173\n\t"
    "v_mfma_f32_16x16x32_bf16 v[252:255], a[220:223], v[128:131], v[252:255]\n\t"
    "v_exp_f32 v142, v142\n\t"
    "v_mfma_f32_16x16x32_bf16 v[152:155], a[160:163], a[80:83], 0\n\t"
    "v_exp_f32 v143, v143\n\t"
    "v_mfma_f32_16x16x32_bf16 v[152:155], a[164:167], a[84:87], v[152:155]\n\t"
    "v_cvt_pk_bf16_f32 v136, v136, v137\n\t"
    "v_cvt_pk_bf16_f32 v137, v138, v139\n\t"
    "v_mfma_f32_16x16x32_bf16 v[152:155], a[168:171], a[88:91], v[152:155]\n\t"
    "v_cvt_pk_bf16_f32 v138, v140, v141\n\t"
    "v_cvt_pk_bf16_f32 v139, v142, v143\n\t"
    "v_mfma_f32_16x16x32_bf16 v[152:155], a[172:175], a[92:95], v[152:155]\n\t"
    "v_fma_f32 v144, v144, s60, v168\n\t"
    "v_fma_f32 v145, v145, s60, v168\n\t"
    "v_mfma_f32_16x16x32_bf16 v[156:159], a[176:179], a[80:83], 0\n\t"
    "v_exp_f32 v144, v144\n\t"
    "v_mfma_f32_16x16x32_bf16 v[156:159], a[180:183], a[84:87], v[156:159]\n\t"
    "v_exp_f32 v145, v145\n\t"
    "v_mfma_f32_16x16x32_bf16 v[156:159], a[184:187], a[88:91], v[156:159]\n\t"
    "v_fma_f32 v146, v146, s60, v168\n\t"
    "v_fma_f32 v147, v147, s60, v168\n\t"
    "v_mfma_f32_16x16x32_bf16 v[156:159], a[188:191], a[92:95], v[156:159]\n\t"
    "v_exp_f32 v146, v146\n\t"
    "v_mfma_f32_16x16x32_bf16 a[244:247], a[248:251], v[136:139], a[244:247]\n\t"
    "v_exp_f32 v147, v147\n\t"
    "v_mfma_f32_16x16x32_bf16 v[176:179], a[192:195], v[136:139], v[176:179]\n\t"
    "v_fma_f32 v148, v148, s60, v168\n\t"
    "v_fma_f32 v149, v149, s60, v168\n\t"
    "v_mfma_f32_16x16x32_bf16 v[180:183], a[196:199], v[136:139], v[180:183]\n\t"
    "v_exp_f32 v148, v148\n\t"
    "v_mfma_f32_16x16x32_bf16 v[184:187], a[200:203], v[136:139], v[184:187]\n\t"
    "v_exp_f32 v149, v149\n\t"
    "v_mfma_f32_16x16x32_bf16 v[188:191], a[204:207], v[136:139], v[188:191]\n\t"
    "v_fma_f32 v150, v150, s60, v168\n\t"
    "v_fma_f32 v151, v151, s60, v168\n\t"
    "v_mfma_f32_16x16x32_bf16 v[192:195], a[208:211], v[136:139], v[192:195]\n\t"
    "ds_read_b64_tr_b16 a[208:209], v225 offset:9216\n\t"
    "v_cvt_pk_bf16_f32 v144, v144, v145\n\t"
    "v_mfma_f32_16x16x32_bf16 v[196:199], a[212:215], v[136:139], v[196:199]\n\t"
    "buffer_store_dwordx4 a[4:7], v228, s[48:51], s66 offen offset:1024 nt\n\t"
    "v_mfma_f32_16x16x32_bf16 v[200:203], a[216:219], v[136:139], v[200:203]\n\t"
    "ds_read_b64_tr_b16 a[210:211], v225 offset:13312\n\t"
    "v_cvt_pk_bf16_f32 v145, v146, v147\n\t"
    "v_mfma_f32_16x16x32_bf16 v[204:207], a[220:223], v[136:139], v[204:207]\n\t"
    "v_exp_f32 v150, v150\n\t"
    "v_mfma_f32_16x16x32_bf16 v[128:131], a[160:163], a[96:99], 0\n\t"
    "ds_read_b64_tr_b16 a[212:213], v225 offset:9224\n\t"
    "v_cvt_pk_bf16_f32 v146, v148, v149\n\t"
    "v_mfma_f32_16x16x32_bf16 v[128:131], a[164:167], a[100:103], v[128:131]\n\t"
    "v_exp_f32 v151, v151\n\t"
    "v_mfma_f32_16x16x32_bf16 v[128:131], a[168:171], a[104:107], v[128:131]\n\t"
    "ds_read_b64_tr_b16 a[214:215], v225 offset:13320\n\t"
    "v_cvt_pk_bf16_f32 v147, v150, v151\n\t"
    "v_mfma_f32_16x16x32_bf16 v[128:131], a[172:175], a[108:111], v[128:131]\n\t"
    "ds_read_b64_tr_b16 a[216:217], v225 offset:9728\n\t"
    "v_fma_f32 v152, v152, s60, v169\n\t"
    "v_mfma_f32_16x16x32_bf16 v[132:135], a[176:179], a[96:99], 0\n\t"
    "ds_read_b64_tr_b16 a[218:219], v225 offset:13824\n\t"
    "v_fma_f32 v153, v153, s60, v169\n\t"
    "v_mfma_f32_16x16x32_bf16 v[132:135], a[180:183], a[100:103], v[132:135]\n\t"
    "ds_read_b64_tr_b16 a[220:221], v225 offset:9736\n\t"
    "v_fma_f32 v154, v154, s60, v169\n\t"
    "v_mfma_f32_16x16x32_bf16 v[132:135], a[184:187], a[104:107], v[132:135]\n\t"
    "ds_read_b64_tr_b16 a[222:223], v225 offset:13832\n\t"
    "v_fma_f32 v155, v155, s60, v169\n\t"
    "v_mfma_f32_16x16x32_bf16 v[132:135], a[188:191], a[108:111], v[132:135]\n\t"
    "v_exp_f32 v152, v152\n\t"
    "v_mfma_f32_16x16x32_bf16 a[224:227], a[248:251], v[144:147], a[224:227]\n\t"
    "s_waitcnt lgkmcnt(15)\n\t"
    "v_exp_f32 v153, v153\n\t"
    "v_mfma_f32_16x16x32_bf16 v[0:3], a[32:35], v[144:147], v[0:3]\n\t"
    "v_exp_f32 v154, v154\n\t"
    "v_mfma_f32_16x16x32_bf16 v[4:7], a[36:39], v[144:147], v[4:7]\n\t"
    "v_exp_f32 v155, v155\n\t"
    "v_mfma_f32_16x16x32_bf16 v[8:11], a[40:43], v[144:147], v[8:11]\n\t"
    "v_fma_f32 v156, v156, s60, v169\n\t"
    "v_fma_f32 v157, v157, s60, v169\n\t"
    "v_mfma_f32_16x16x32_bf16 v[12:15], a[44:47], v[144:147], v[12:15]\n\t"
    "s_waitcnt lgkmcnt(4)\n\t"
    "v_exp_f32 v156, v156\n\t"
    "v_mfma_f32_16x16x32_bf16 v[16:19], a[208:211], v[144:147], v[16:19]\n\t"
    "v_exp_f32 v157, v157\n\t"
    "v_mfma_f32_16x16x32_bf16 v[20:23], a[212:215], v[144:147], v[20:23]\n\t"
    "s_waitcnt lgkmcnt(0)\n\t"
    "v_fma_f32 v158, v158, s60, v169\n\t"
    "v_fma_f32 v159, v159, s60, v169\n\t"
    "v_mfma_f32_16x16x32_bf16 v[24:27], a[216:219], v[144:147], v[24:27]\n\t"
    "v_exp_f32 v158, v158\n\t"
    "v_mfma_f32_16x16x32_bf16 v[28:31], a[220:223], v[144:147], v[28:31]\n\t"
    "v_exp_f32 v159, v159\n\t"
    "v_mfma_f32_16x16x32_bf16 v[136:139], a[160:163], a[112:115], 0\n\t"
    "s_waitcnt vmcnt(19) lgkmcnt(0)\n\t"
    "v_cvt_pk_bf16_f32 v152, v152, v153\n\t"
    "v_cvt_pk_bf16_f32 v153, v154, v155\n\t"
    "v_mfma_f32_16x16x32_bf16 v[136:139], a[164:167], a[116:119], v[136:139]\n\t"
    "s_barrier\n\t"
    "v_cvt_pk_bf16_f32 v154, v156, v157\n\t"
    "v_cvt_pk_bf16_f32 v155, v158, v159\n\t"
    "v_mfma_f32_16x16x32_bf16 v[136:139], a[168:171], a[120:123], v[136:139]\n\t"
    "ds_read_b64_tr_b16 a[192:193], v225 offset:16384\n\t"
    "v_fma_f32 v128, v128, s60, v170\n\t"
    "v_mfma_f32_16x16x32_bf16 v[136:139], a[172:175], a[124:127], v[136:139]\n\t"
    "s_xor_b32 s64, s52, 8192\n\t"
    "v_exp_f32 v128, v128\n\t"
    "v_mfma_f32_16x16x32_bf16 v[140:143], a[176:179], a[112:115], 0\n\t"
    "ds_read_b64_tr_b16 a[194:195], v225 offset:20480\n\t"
    "v_fma_f32 v129, v129, s60, v170\n\t"
    "v_mfma_f32_16x16x32_bf16 v[140:143], a[180:183], a[116:119], v[140:143]\n\t"
    "s_add_u32 m0, s63, 8192\n\t"
    "v_exp_f32 v129, v129\n\t"
    "v_mfma_f32_16x16x32_bf16 v[140:143], a[184:187], a[120:123], v[140:143]\n\t"
    "ds_read_b64_tr_b16 a[196:197], v225 offset:16392\n\t"
    "v_fma_f32 v130, v130, s60, v170\n\t"
    "v_mfma_f32_16x16x32_bf16 v[140:143], a[188:191], a[124:127], v[140:143]\n\t"
    "buffer_load_dwordx4 v226, s[36:39], s64 offen offset:0 lds\n\t"
    "v_mfma_f32_16x16x32_bf16 a[228:231], a[248:251], v[152:155], a[228:231]\n\t"
    "ds_read_b64_tr_b16 a[198:199], v225 offset:20488\n\t"
    "v_fma_f32 v131, v131, s60, v170\n\t"
    "v_mfma_f32_16x16x32_bf16 v[32:35], a[32:35], v[152:155], v[32:35]\n\t"
    "v_exp_f32 v130, v130\n\t"
    "v_mfma_f32_16x16x32_bf16 v[36:39], a[36:39], v[152:155], v[36:39]\n\t"
    "ds_read_b64_tr_b16 a[200:201], v225 offset:16896\n\t"
    "v_fma_f32 v132, v132, s60, v170\n\t"
    "v_mfma_f32_16x16x32_bf16 v[40:43], a[40:43], v[152:155], v[40:43]\n\t"
    "v_exp_f32 v131, v131\n\t"
    "v_mfma_f32_16x16x32_bf16 v[44:47], a[44:47], v[152:155], v[44:47]\n\t"
    "ds_read_b64_tr_b16 a[202:203], v225 offset:20992\n\t"
    "v_fma_f32 v133, v133, s60, v170\n\t"
    "v_mfma_f32_16x16x32_bf16 v[48:51], a[208:211], v[152:155], v[48:51]\n\t"
    "buffer_load_dwordx4 v226, s[36:39], s64 offen offset:2048 lds\n\t"
    "v_mfma_f32_16x16x32_bf16 v[52:55], a[212:215], v[152:155], v[52:55]\n\t"
    "ds_read_b64_tr_b16 a[204:205], v225 offset:16904\n\t"
    "v_fma_f32 v134, v134, s60, v170\n\t"
    "v_fma_f32 v135, v135, s60, v170\n\t"
    "v_mfma_f32_16x16x32_bf16 v[56:59], a[216:219], v[152:155], v[56:59]\n\t"
    "v_exp_f32 v132, v132\n\t"
    "v_mfma_f32_16x16x32_bf16 v[60:63], a[220:223], v[152:155], v[60:63]\n\t"
    "ds_read_b64_tr_b16 a[206:207], v225 offset:21000\n\t"
    "v_cvt_pk_bf16_f32 v128, v128, v129\n\t"
    "v_mfma_f32_16x16x32_bf16 v[144:147], a[160:163], a[128:131], 0\n\t"
    "v_exp_f32 v133, v133\n\t"
    "v_mfma_f32_16x16x32_bf16 v[144:147], a[164:167], a[132:135], v[144:147]\n\t"
    "s_add_u32 m0, s63, 40960\n\t"
    "v_exp_f32 v134, v134\n\t"
    "v_mfma_f32_16x16x32_bf16 v[144:147], a[168:171], a[136:139], v[144:147]\n\t"
    "buffer_load_dwordx4 v226, s[40:43], s64 offen offset:0 lds\n\t"
    "v_mfma_f32_16x16x32_bf16 v[144:147], a[172:175], a[140:143], v[144:147]\n\t"
    "v_exp_f32 v135, v135\n\t"
    "v_mfma_f32_16x16x32_bf16 v[148:151], a[176:179], a[128:131], 0\n\t"
    "v_cvt_pk_bf16_f32 v129, v130, v131\n\t"
    "v_cvt_pk_bf16_f32 v130, v132, v133\n\t"
    "v_mfma_f32_16x16x32_bf16 v[148:151], a[180:183], a[132:135], v[148:151]\n\t"
    "v_cvt_pk_bf16_f32 v131, v134, v135\n\t"
    "v_fma_f32 v136, v136, s60, v171\n\t"
    "v_mfma_f32_16x16x32_bf16 v[148:151], a[184:187], a[136:139], v[148:151]\n\t"
    "v_exp_f32 v136, v136\n\t"
    "v_mfma_f32_16x16x32_bf16 v[148:151], a[188:191], a[140:143], v[148:151]\n\t"
    "buffer_load_dwordx4 v226, s[40:43], s64 offen offset:2048 lds\n\t"
    "v_mfma_f32_16x16x32_bf16 a[232:235], a[248:251], v[128:131], a[232:235]\n\t"
    "v_fma_f32 v137, v137, s60, v171\n\t"
    "v_fma_f32 v138, v138, s60, v171\n\t"
    "v_mfma_f32_16x16x32_bf16 v[64:67], a[32:35], v[128:131], v[64:67]\n\t"
    "v_exp_f32 v137, v137\n\t"
    "v_mfma_f32_16x16x32_bf16 v[68:71], a[36:39], v[128:131], v[68:71]\n\t"
    "v_exp_f32 v138, v138\n\t"
    "v_mfma_f32_16x16x32_bf16 v[72:75], a[40:43], v[128:131], v[72:75]\n\t"
    "v_fma_f32 v139, v139, s60, v171\n\t"
    "v_fma_f32 v140, v140, s60, v171\n\t"
    "v_mfma_f32_16x16x32_bf16 v[76:79], a[44:47], v[128:131], v[76:79]\n\t"
    "v_exp_f32 v139, v139\n\t"
    "v_mfma_f32_16x16x32_bf16 v[80:83], a[208:211], v[128:131], v[80:83]\n\t"
    "v_exp_f32 v140, v140\n\t"
    "v_mfma_f32_16x16x32_bf16 v[84:87], a[212:215], v[128:131], v[84:87]\n\t"
    "buffer_store_dwordx4 a[8:11], v227, s[48:51], s66 offen offset:2048 nt\n\t"
    "v_mfma_f32_16x16x32_bf16 v[88:91], a[216:219], v[128:131], v[88:91]\n\t"
    "v_fma_f32 v141, v141, s60, v171\n\t"
    "v_fma_f32 v142, v142, s60, v171\n\t"
    "v_mfma_f32_16x16x32_bf16 v[92:95], a[220:223], v[128:131], v[92:95]\n\t"
    "v_exp_f32 v141, v141\n\t"
    "v_mfma_f32_16x16x32_bf16 v[152:155], a[160:163], a[144:147], 0\n\t"
    "ds_read_b128 a[160:163], v224 offset:16384\n\t"
    "v_fma_f32 v143, v143, s60, v171\n\t"
    "v_mfma_f32_16x16x32_bf16 v[152:155], a[164:167], a[148:151], v[152:155]\n\t"
    "v_exp_f32 v142, v142\n\t"
    "v_mfma_f32_16x16x32_bf16 v[152:155], a[168:171], a[152:155], v[152:155]\n\t"
    "ds_read_b128 a[164:167], v224 offset:16896\n\t"
    "v_cvt_pk_bf16_f32 v136, v136, v137\n\t"
    "v_mfma_f32_16x16x32_bf16 v[152:155], a[172:175], a[156:159], v[152:155]\n\t"
    "v_exp_f32 v143, v143\n\t"
    "v_mfma_f32_16x16x32_bf16 v[156:159], a[176:179], a[144:147], 0\n\t"
    "ds_read_b128 a[168:171], v224 offset:17408\n\t"
    "v_cvt_pk_bf16_f32 v137, v138, v139\n\t"
    "v_mfma_f32_16x16x32_bf16 v[156:159], a[180:183], a[148:151], v[156:159]\n\t"
    "v_cvt_pk_bf16_f32 v138, v140, v141\n\t"
    "v_cvt_pk_bf16_f32 v139, v142, v143\n\t"
    "v_mfma_f32_16x16x32_bf16 v[156:159], a[184:187], a[152:155], v[156:159]\n\t"
    "ds_read_b128 a[172:175], v224 offset:17920\n\t"
    "v_fma_f32 v144, v144, s60, v172\n\t"
    "v_mfma_f32_16x16x32_bf16 v[156:159], a[188:191], a[156:159], v[156:159]\n\t"
    "v_exp_f32 v144, v144\n\t"
    "v_mfma_f32_16x16x32_bf16 a[236:239], a[248:251], v[136:139], a[236:239]\n\t"
    "ds_read_b128 a[176:179], v224 offset:20480\n\t"
    "v_fma_f32 v145, v145, s60, v172\n\t"
    "v_mfma_f32_16x16x32_bf16 v[96:99], a[32:35], v[136:139], v[96:99]\n\t"
    "v_exp_f32 v145, v145\n\t"
    "v_mfma_f32_16x16x32_bf16 v[100:103], a[36:39], v[136:139], v[100:103]\n\t"
    "ds_read_b128 a[180:183], v224 offset:20992\n\t"
    "v_fma_f32 v146, v146, s60, v172\n\t"
    "v_mfma_f32_16x16x32_bf16 v[104:107], a[40:43], v[136:139], v[104:107]\n\t"
    "v_exp_f32 v146, v146\n\t"
    "v_mfma_f32_16x16x32_bf16 v[108:111], a[44:47], v[136:139], v[108:111]\n\t"
    "ds_read_b128 a[184:187], v224 offset:21504\n\t"
    "v_fma_f32 v147, v147, s60, v172\n\t"
    "v_mfma_f32_16x16x32_bf16 v[112:115], a[208:211], v[136:139], v[112:115]\n\t"
    "ds_read_b128 a[188:191], v224 offset:22016\n\t"
    "v_fma_f32 v148, v148, s60, v172\n\t"
    "v_mfma_f32_16x16x32_bf16 v[116:119], a[212:215], v[136:139], v[116:119]\n\t"
    "v_exp_f32 v147, v147\n\t"
    "v_mfma_f32_16x16x32_bf16 v[120:123], a[216:219], v[136:139], v[120:123]\n\t"
    "v_exp_f32 v148, v148\n\t"
    "v_mfma_f32_16x16x32_bf16 v[124:127], a[220:223], v[136:139], v[124:127]\n\t"
    "s_waitcnt lgkmcnt(6)\n\t"
    "v_fma_f32 v149, v149, s60, v172\n\t"
    "v_fma_f32 v150, v150, s60, v172\n\t"
    "v_mfma_f32_16x16x32_bf16 v[128:131], a[160:163], a[64:67], 0\n\t"
    "v_exp_f32 v149, v149\n\t"
    "v_mfma_f32_16x16x32_bf16 v[128:131], a[164:167], a[68:71], v[128:131]\n\t"
    "s_waitcnt lgkmcnt(4)\n\t"
    "v_exp_f32 v150, v150\n\t"
    "v_mfma_f32_16x16x32_bf16 v[128:131], a[168:171], a[72:75], v[128:131]\n\t"
    "v_fma_f32 v151, v151, s60, v172\n\t"
    "v_cvt_pk_bf16_f32 v144, v144, v145\n\t"
    "v_mfma_f32_16x16x32_bf16 v[128:131], a[172:175], a[76:79], v[128:131]\n\t"
    "s_waitcnt lgkmcnt(2)\n\t"
    "v_exp_f32 v151, v151\n\t"
    "v_mfma_f32_16x16x32_bf16 v[132:135], a[176:179], a[64:67], 0\n\t"
    "v_cvt_pk_bf16_f32 v145, v146, v147\n\t"
    "v_cvt_pk_bf16_f32 v146, v148, v149\n\t"
    "v_mfma_f32_16x16x32_bf16 v[132:135], a[180:183], a[68:71], v[132:135]\n\t"
    "s_waitcnt lgkmcnt(0)\n\t"
    "v_cvt_pk_bf16_f32 v147, v150, v151\n\t"
    "v_fma_f32 v152, v152, s60, v173\n\t"
    "v_mfma_f32_16x16x32_bf16 v[132:135], a[184:187], a[72:75], v[132:135]\n\t"
    "v_exp_f32 v152, v152\n\t"
    "v_mfma_f32_16x16x32_bf16 v[132:135], a[188:191], a[76:79], v[132:135]\n\t"
    "v_fma_f32 v153, v153, s60, v173\n\t"
    "v_fma_f32 v154, v154, s60, v173\n\t"
    "v_mfma_f32_16x16x32_bf16 a[240:243], a[248:251], v[144:147], a[240:243]\n\t"
    "v_exp_f32 v153, v153\n\t"
    "v_mfma_f32_16x16x32_bf16 v[208:211], a[32:35], v[144:147], v[208:211]\n\t"
    "v_exp_f32 v154, v154\n\t"
    "v_mfma_f32_16x16x32_bf16 v[212:215], a[36:39], v[144:147], v[212:215]\n\t"
    "v_fma_f32 v155, v155, s60, v173\n\t"
    "v_fma_f32 v156, v156, s60, v173\n\t"
    "v_mfma_f32_16x16x32_bf16 v[216:219], a[40:43], v[144:147], v[216:219]\n\t"
    "v_exp_f32 v155, v155\n\t"
    "v_mfma_f32_16x16x32_bf16 v[220:223], a[44:47], v[144:147], v[220:223]\n\t"
    "v_exp_f32 v156, v156\n\t"
    "v_mfma_f32_16x16x32_bf16 v[240:243], a[208:211], v[144:147], v[240:243]\n\t"
    "v_fma_f32 v157, v157, s60, v173\n\t"
    "v_fma_f32 v158, v158, s60, v173\n\t"
    "v_mfma_f32_16x16x32_bf16 v[244:247], a[212:215], v[144:147], v[244:247]\n\t"
    "v_exp_f32 v157, v157\n\t"
    "v_mfma_f32_16x16x32_bf16 v[248:251], a[216:219], v[144:147], v[248:251]\n\t"
    "v_exp_f32 v158, v158\n\t"
    "v_mfma_f32_16x16x32_bf16 v[252:255], a[220:223], v[144:147], v[252:255]\n\t"
    "v_fma_f32 v159, v159, s60, v173\n\t"
    "v_cvt_pk_bf16_f32 v152, v152, v153\n\t"
    "v_mfma_f32_16x16x32_bf16 v[136:139], a[160:163], a[80:83], 0\n\t"
    "v_exp_f32 v159, v159\n\t"
    "v_mfma_f32_16x16x32_bf16 v[136:139], a[164:167], a[84:87], v[136:139]\n\t"
    "v_cvt_pk_bf16_f32 v153, v154, v155\n\t"
    "v_cvt_pk_bf16_f32 v154, v156, v157\n\t"
    "v_mfma_f32_16x16x32_bf16 v[136:139], a[168:171], a[88:91], v[136:139]\n\t"
    "v_cvt_pk_bf16_f32 v155, v158, v159\n\t"
    "v_fma_f32 v128, v128, s60, v168\n\t"
    "v_mfma_f32_16x16x32_bf16 v[136:139], a[172:175], a[92:95], v[136:139]\n\t"
    "v_exp_f32 v128, v128\n\t"
    "v_mfma_f32_16x16x32_bf16 v[140:143], a[176:179], a[80:83], 0\n\t"
    "v_fma_f32 v129, v129, s60, v168\n\t"
    "v_fma_f32 v130, v130, s60, v168\n\t"
    "v_mfma_f32_16x16x32_bf16 v[140:143], a[180:183], a[84:87], v[140:143]\n\t"
    "v_exp_f32 v129, v129\n\t"
    "v_mfma_f32_16x16x32_bf16 v[140:143], a[184:187], a[88:91], v[140:143]\n\t"
    "v_exp_f32 v130, v130\n\t"
    "v_mfma_f32_16x16x32_bf16 v[140:143], a[188:191], a[92:95], v[140:143]\n\t"
    "v_fma_f32 v131, v131, s60, v168\n\t"
    "v_fma_f32 v132, v132, s60, v168\n\t"
    "v_mfma_f32_16x16x32_bf16 a[244:247], a[248:251], v[152:155], a[244:247]\n\t"
    "v_exp_f32 v131, v131\n\t"
    "v_mfma_f32_16x16x32_bf16 v[176:179], a[32:35], v[152:155], v[176:179]\n\t"
    "v_exp_f32 v132, v132\n\t"
    "v_mfma_f32_16x16x32_bf16 v[180:183], a[36:39], v[152:155], v[180:183]\n\t"
    "v_fma_f32 v133, v133, s60, v168\n\t"
    "v_fma_f32 v134, v134, s60, v168\n\t"
    "v_mfma_f32_16x16x32_bf16 v[184:187], a[40:43], v[152:155], v[184:187]\n\t"
    "v_exp_f32 v133, v133\n\t"
    "v_mfma_f32_16x16x32_bf16 v[188:191], a[44:47], v[152:155], v[188:191]\n\t"
    "v_exp_f32 v134, v134\n\t"
    "v_mfma_f32_16x16x32_bf16 v[192:195], a[208:211], v[152:155], v[192:195]\n\t"
    "ds_read_b64_tr_b16 a[208:209], v225 offset:17408\n\t"
    "v_fma_f32 v135, v135, s60, v168\n\t"
    "v_mfma_f32_16x16x32_bf16 v[196:199], a[212:215], v[152:155], v[196:199]\n\t"
    "v_exp_f32 v135, v135\n\t"
    "v_mfma_f32_16x16x32_bf16 v[200:203], a[216:219], v[152:155], v[200:203]\n\t"
    "ds_read_b64_tr_b16 a[210:211], v225 offset:21504\n\t"
    "v_cvt_pk_bf16_f32 v128, v128, v129\n\t"
    "v_mfma_f32_16x16x32_bf16 v[204:207], a[220:223], v[152:155], v[204:207]\n\t"
    "buffer_store_dwordx4 a[12:15], v228, s[48:51], s66 offen offset:3072 nt\n\t"
    "v_mfma_f32_16x16x32_bf16 v[144:147], a[160:163], a[96:99], 0\n\t"
    "ds_read_b64_tr_b16 a[212:213], v225 offset:17416\n\t"
    "v_cvt_pk_bf16_f32 v129, v130, v131\n\t"
    "v_mfma_f32_16x16x32_bf16 v[144:147], a[164:167], a[100:103], v[144:147]\n\t"
    "s_cmp_lg_u64 s[58:59], 0\n\t"
    "s_cselect_b32 s66, s55, 0\n\t"
    "v_cvt_pk_bf16_f32 v130, v132, v133\n\t"
    "v_mfma_f32_16x16x32_bf16 v[144:147], a[168:171], a[104:107], v[144:147]\n\t"
    "ds_read_b64_tr_b16 a[214:215], v225 offset:21512\n\t"
    "v_cvt_pk_bf16_f32 v131, v134, v135\n\t"
    "v_mfma_f32_16x16x32_bf16 v[144:147], a[172:175], a[108:111], v[144:147]\n\t"
    "ds_read_b64_tr_b16 a[216:217], v225 offset:17920\n\t"
    "v_fma_f32 v136, v136, s60, v169\n\t"
    "v_mfma_f32_16x16x32_bf16 v[148:151], a[176:179], a[96:99], 0\n\t"
    "ds_read_b64_tr_b16 a[218:219], v225 offset:22016\n\t"
    "v_fma_f32 v137, v137, s60, v169\n\t"
    "v_mfma_f32_16x16x32_bf16 v[148:151], a[180:183], a[100:103], v[148:151]\n\t"
    "ds_read_b64_tr_b16 a[220:221], v225 offset:17928\n\t"
    "v_fma_f32 v138, v138, s60, v169\n\t"
    "v_mfma_f32_16x16x32_bf16 v[148:151], a[184:187], a[104:107], v[148:151]\n\t"
    "ds_read_b64_tr_b16 a[222:223], v225 offset:22024\n\t"
    "v_fma_f32 v139, v139, s60, v169\n\t"
    "v_mfma_f32_16x16x32_bf16 v[148:151], a[188:191], a[108:111], v[148:151]\n\t"
    "s_or_b32 s56, s56, s66\n\t"
    "v_exp_f32 v136, v136\n\t"
    "v_mfma_f32_16x16x32_bf16 a[224:227], a[248:251], v[128:131], a[224:227]\n\t"
    "s_waitcnt lgkmcnt(15)\n\t"
    "v_exp_f32 v137, v137\n\t"
    "v_mfma_f32_16x16x32_bf16 v[0:3], a[192:195], v[128:131], v[0:3]\n\t"
    "s_mov_b64 s[58:59], 0\n\t"
    "v_exp_f32 v138, v138\n\t"
    "v_mfma_f32_16x16x32_bf16 v[4:7], a[196:199], v[128:131], v[4:7]\n\t"
    "s_mov_b32 s55, s68\n\t"
    "v_exp_f32 v139, v139\n\t"
    "v_mfma_f32_16x16x32_bf16 v[8:11], a[200:203], v[128:131], v[8:11]\n\t"
    "s_lshl_b32 s68, s68, 1\n\t"
    "v_fma_f32 v140, v140, s60, v169\n\t"
    "v_fma_f32 v141, v141, s60, v169\n\t"
    "v_mfma_f32_16x16x32_bf16 v[12:15], a[204:207], v[128:131], v[12:15]\n\t"
    "s_waitcnt lgkmcnt(4)\n\t"
    "v_exp_f32 v140, v140\n\t"
    "v_mfma_f32_16x16x32_bf16 v[16:19], a[208:211], v[128:131], v[16:19]\n\t"
    "s_mov_b32 s54, s76\n\t"
    "v_exp_f32 v141, v141\n\t"
    "v_fma_f32 v142, v142, s60, v169\n\t"
    "v_mfma_f32_16x16x32_bf16 v[20:23], a[212:215], v[128:131], v[20:23]\n\t"
    "s_waitcnt lgkmcnt(0)\n\t"
    "v_exp_f32 v142, v142\n\t"
    "v_fma_f32 v143, v143, s60, v169\n\t"
    "v_cvt_pk_bf16_f32 v136, v136, v137\n\t"
    "v_mfma_f32_16x16x32_bf16 v[24:27], a[216:219], v[128:131], v[24:27]\n\t"
    "s_mov_b32 s53, s78\n\t"
    "v_exp_f32 v143, v143\n\t"
    "v_mfma_f32_16x16x32_bf16 v[28:31], a[220:223], v[128:131], v[28:31]\n\t"
    "s_sub_u32 s57, s57, 1\n\t"
    "v_cvt_pk_bf16_f32 v137, v138, v139\n\t"
    "v_cvt_pk_bf16_f32 v138, v140, v141\n\t"
    "v_mfma_f32_16x16x32_bf16 v[152:155], a[160:163], a[112:115], 0\n\t"
    "s_waitcnt vmcnt(17) lgkmcnt(0)\n\t"
    "s_mov_b32 s71, s72\n\t"
    "v_cvt_pk_bf16_f32 v139, v142, v143\n\t"
    "v_mfma_f32_16x16x32_bf16 v[152:155], a[164:167], a[116:119], v[152:155]\n\t"
    "s_barrier\n\t"
    "v_fma_f32 v144, v144, s60, v170\n\t"
    "v_fma_f32 v145, v145, s60, v170\n\t"
    "v_mfma_f32_16x16x32_bf16 v[152:155], a[168:171], a[120:123], v[152:155]\n\t"
    "ds_read_b64_tr_b16 a[32:33], v225 offset:24576\n\t"
    "s_mov_b32 s72, s73\n\t"
    "v_mfma_f32_16x16x32_bf16 v[152:155], a[172:175], a[124:127], v[152:155]\n\t"
    "s_xor_b32 s64, s52, 16384\n\t"
    "s_mov_b32 s73, 0xc0000000\n\t"
    "v_fma_f32 v146, v146, s60, v170\n\t"
    "v_mfma_f32_16x16x32_bf16 v[156:159], a[176:179], a[112:115], 0\n\t"
    "ds_read_b64_tr_b16 a[34:35], v225 offset:28672\n\t"
    "v_fma_f32 v147, v147, s60, v170\n\t"
    "v_mfma_f32_16x16x32_bf16 v[156:159], a[180:183], a[116:119], v[156:159]\n\t"
    "s_add_u32 m0, s63, 16384\n\t"
    "v_exp_f32 v144, v144\n\t"
    "v_mfma_f32_16x16x32_bf16 v[156:159], a[184:187], a[120:123], v[156:159]\n\t"
    "ds_read_b64_tr_b16 a[36:37], v225 offset:24584\n\t"
    "v_fma_f32 v148, v148, s60, v170\n\t"
    "v_mfma_f32_16x16x32_bf16 v[156:159], a[188:191], a[124:127], v[156:159]\n\t"
    "buffer_load_dwordx4 v226, s[36:39], s64 offen offset:0 lds\n\t"
    "v_mfma_f32_16x16x32_bf16 a[228:231], a[248:251], v[136:139], a[228:231]\n\t"
    "ds_read_b64_tr_b16 a[38:39], v225 offset:28680\n\t"
    "v_fma_f32 v149, v149, s60, v170\n\t"
    "v_mfma_f32_16x16x32_bf16 v[32:35], a[192:195], v[136:139], v[32:35]\n\t"
    "s_mov_b32 s76, s77\n\t"
    "v_exp_f32 v145, v145\n\t"
    "v_mfma_f32_16x16x32_bf16 v[36:39], a[196:199], v[136:139], v[36:39]\n\t"
    "ds_read_b64_tr_b16 a[40:41], v225 offset:25088\n\t"
    "v_fma_f32 v150, v150, s60, v170\n\t"
    "v_mfma_f32_16x16x32_bf16 v[40:43], a[200:203], v[136:139], v[40:43]\n\t"
    "s_mov_b32 s77, s78\n\t"
    "v_exp_f32 v146, v146\n\t"
    "v_mfma_f32_16x16x32_bf16 v[44:47], a[204:207], v[136:139], v[44:47]\n\t"
    "ds_read_b64_tr_b16 a[42:43], v225 offset:29184\n\t"
    "v_fma_f32 v151, v151, s60, v170\n\t"
    "v_mfma_f32_16x16x32_bf16 v[48:51], a[208:211], v[136:139], v[48:51]\n\t"
    "buffer_load_dwordx4 v226, s[36:39], s64 offen offset:2048 lds\n\t"
    "v_mfma_f32_16x16x32_bf16 v[52:55], a[212:215], v[136:139], v[52:55]\n\t"
    "ds_read_b64_tr_b16 a[44:45], v225 offset:25096\n\t"
    "v_cvt_pk_bf16_f32 v144, v144, v145\n\t"
    "v_mfma_f32_16x16x32_bf16 v[56:59], a[216:219], v[136:139], v[56:59]\n\t"
    "s_mov_b32 s78, s79\n\t"
    "v_exp_f32 v147, v147\n\t"
    "v_mfma_f32_16x16x32_bf16 v[60:63], a[220:223], v[136:139], v[60:63]\n\t"
    "ds_read_b64_tr_b16 a[46:47], v225 offset:29192\n\t"
    "v_cvt_pk_bf16_f32 v145, v146, v147\n\t"
    "v_mfma_f32_16x16x32_bf16 v[128:131], a[160:163], a[128:131], 0\n\t"
    "s_mov_b32 s79, 0xc0000000\n\t"
    "v_exp_f32 v148, v148\n\t"
    "v_mfma_f32_16x16x32_bf16 v[128:131], a[164:167], a[132:135], v[128:131]\n\t"
    "s_add_u32 m0, s63, 49152\n\t"
    "v_exp_f32 v149, v149\n\t"
    "v_mfma_f32_16x16x32_bf16 v[128:131], a[168:171], a[136:139], v[128:131]\n\t"
    "buffer_load_dwordx4 v226, s[40:43], s64 offen offset:0 lds\n\t"
    "v_mfma_f32_16x16x32_bf16 v[128:131], a[172:175], a[140:143], v[128:131]\n\t"
    "v_exp_f32 v150, v150\n\t"
    "v_mfma_f32_16x16x32_bf16 v[132:135], a[176:179], a[128:131], 0\n\t"
    "v_exp_f32 v151, v151\n\t"
    "v_mfma_f32_16x16x32_bf16 v[132:135], a[180:183], a[132:135], v[132:135]\n\t"
    "v_cvt_pk_bf16_f32 v146, v148, v149\n\t"
    "v_cvt_pk_bf16_f32 v147, v150, v151\n\t"
    "v_mfma_f32_16x16x32_bf16 v[132:135], a[184:187], a[136:139], v[132:135]\n\t"
    "v_fma_f32 v152, v152, s60, v171\n\t"
    "v_fma_f32 v153, v153, s60, v171\n\t"
    "v_mfma_f32_16x16x32_bf16 v[132:135], a[188:191], a[140:143], v[132:135]\n\t"
    "buffer_load_dwordx4 v226, s[40:43], s64 offen offset:2048 lds\n\t"
    "v_mfma_f32_16x16x32_bf16 a[232:235], a[248:251], v[144:147], a[232:235]\n\t"
    "v_exp_f32 v152, v152\n\t"
    "v_mfma_f32_16x16x32_bf16 v[64:67], a[192:195], v[144:147], v[64:67]\n\t"
    "v_exp_f32 v153, v153\n\t"
    "v_mfma_f32_16x16x32_bf16 v[68:71], a[196:199], v[144:147], v[68:71]\n\t"
    "v_fma_f32 v154, v154, s60, v171\n\t"
    "v_fma_f32 v155, v155, s60, v171\n\t"
    "v_mfma_f32_16x16x32_bf16 v[72:75], a[200:203], v[144:147], v[72:75]\n\t"
    "v_exp_f32 v154, v154\n\t"
    "v_mfma_f32_16x16x32_bf16 v[76:79], a[204:207], v[144:147], v[76:79]\n\t"
    "v_exp_f32 v155, v155\n\t"
    "v_mfma_f32_16x16x32_bf16 v[80:83], a[208:211], v[144:147], v[80:83]\n\t"
    "v_fma_f32 v156, v156, s60, v171\n\t"
    "v_fma_f32 v157, v157, s60, v171\n\t"
    "v_mfma_f32_16x16x32_bf16 v[84:87], a[212:215], v[144:147], v[84:87]\n\t"
    "v_exp_f32 v156, v156\n\t"
    "v_mfma_f32_16x16x32_bf16 v[88:91], a[216:219], v[144:147], v[88:91]\n\t"
    "v_exp_f32 v157, v157\n\t"
    "v_mfma_f32_16x16x32_bf16 v[92:95], a[220:223], v[144:147], v[92:95]\n\t"
    "v_fma_f32 v158, v158, s60, v171\n\t"
    "v_fma_f32 v159, v159, s60, v171\n\t"
    "v_mfma_f32_16x16x32_bf16 v[136:139], a[160:163], a[144:147], 0\n\t"
    "ds_read_b128 a[160:163], v224 offset:24576\n\t"
    "v_cvt_pk_bf16_f32 v152, v152, v153\n\t"
    "v_mfma_f32_16x16x32_bf16 v[136:139], a[164:167], a[148:151], v[136:139]\n\t"
    "v_exp_f32 v158, v158\n\t"
    "v_mfma_f32_16x16x32_bf16 v[136:139], a[168:171], a[152:155], v[136:139]\n\t"
    "ds_read_b128 a[164:167], v224 offset:25088\n\t"
    "v_cvt_pk_bf16_f32 v153, v154, v155\n\t"
    "v_mfma_f32_16x16x32_bf16 v[136:139], a[172:175], a[156:159], v[136:139]\n\t"
    "v_exp_f32 v159, v159\n\t"
    "v_mfma_f32_16x16x32_bf16 v[140:143], a[176:179], a[144:147], 0\n\t"
    "ds_read_b128 a[168:171], v224 offset:25600\n\t"
    "v_cvt_pk_bf16_f32 v154, v156, v157\n\t"
    "v_mfma_f32_16x16x32_bf16 v[140:143], a[180:183], a[148:151], v[140:143]\n\t"
    "v_cvt_pk_bf16_f32 v155, v158, v159\n\t"
    "v_fma_f32 v128, v128, s60, v172\n\t"
    "v_mfma_f32_16x16x32_bf16 v[140:143], a[184:187], a[152:155], v[140:143]\n\t"
    "ds_read_b128 a[172:175], v224 offset:26112\n\t"
    "v_fma_f32 v129, v129, s60, v172\n\t"
    "v_mfma_f32_16x16x32_bf16 v[140:143], a[188:191], a[156:159], v[140:143]\n\t"
    "v_exp_f32 v128, v128\n\t"
    "v_mfma_f32_16x16x32_bf16 a[236:239], a[248:251], v[152:155], a[236:239]\n\t"
    "ds_read_b128 a[176:179], v224 offset:28672\n\t"
    "v_fma_f32 v130, v130, s60, v172\n\t"
    "v_mfma_f32_16x16x32_bf16 v[96:99], a[192:195], v[152:155], v[96:99]\n\t"
    "v_exp_f32 v129, v129\n\t"
    "v_mfma_f32_16x16x32_bf16 v[100:103], a[196:199], v[152:155], v[100:103]\n\t"
    "ds_read_b128 a[180:183], v224 offset:29184\n\t"
    "v_fma_f32 v131, v131, s60, v172\n\t"
    "v_mfma_f32_16x16x32_bf16 v[104:107], a[200:203], v[152:155], v[104:107]\n\t"
    "v_exp_f32 v130, v130\n\t"
    "v_mfma_f32_16x16x32_bf16 v[108:111], a[204:207], v[152:155], v[108:111]\n\t"
    "ds_read_b128 a[184:187], v224 offset:29696\n\t"
    "v_fma_f32 v132, v132, s60, v172\n\t"
    "v_mfma_f32_16x16x32_bf16 v[112:115], a[208:211], v[152:155], v[112:115]\n\t"
    "ds_read_b128 a[188:191], v224 offset:30208\n\t"
    "v_fma_f32 v133, v133, s60, v172\n\t"
    "v_mfma_f32_16x16x32_bf16 v[116:119], a[212:215], v[152:155], v[116:119]\n\t"
    "v_exp_f32 v131, v131\n\t"
    "v_mfma_f32_16x16x32_bf16 v[120:123], a[216:219], v[152:155], v[120:123]\n\t"
    "v_exp_f32 v132, v132\n\t"
    "v_mfma_f32_16x16x32_bf16 v[124:127], a[220:223], v[152:155], v[124:127]\n\t"
    "s_waitcnt lgkmcnt(6)\n\t"
    "v_exp_f32 v133, v133\n\t"
    "v_mfma_f32_16x16x32_bf16 v[144:147], a[160:163], a[64:67], 0\n\t"
    "s_waitcnt vmcnt(63)\n\t"
    "v_fma_f32 v134, v134, s60, v172\n\t"
    "v_fma_f32 v135, v135, s60, v172\n\t"
    "v_mfma_f32_16x16x32_bf16 v[144:147], a[164:167], a[68:71], v[144:147]\n\t"
    "s_waitcnt lgkmcnt(4)\n\t"
    "v_exp_f32 v134, v134\n\t"
    "v_mfma_f32_16x16x32_bf16 v[144:147], a[168:171], a[72:75], v[144:147]\n\t"
    "v_exp_f32 v135, v135\n\t"
    "v_mfma_f32_16x16x32_bf16 v[144:147], a[172:175], a[76:79], v[144:147]\n\t"
    "s_waitcnt lgkmcnt(2)\n\t"
    "v_cvt_pk_bf16_f32 v128, v128, v129\n\t"
    "v_cvt_pk_bf16_f32 v129, v130, v131\n\t"
    "v_mfma_f32_16x16x32_bf16 v[148:151], a[176:179], a[64:67], 0\n\t"
    "ds_read_b128 a[64:67], v229 offset:0\n\t"
    "v_cvt_pk_bf16_f32 v130, v132, v133\n\t"
    "v_mfma_f32_16x16x32_bf16 v[148:151], a[180:183], a[68:71], v[148:151]\n\t"
    "s_waitcnt lgkmcnt(1)\n\t"
    "v_cvt_pk_bf16_f32 v131, v134, v135\n\t"
    "v_fma_f32 v136, v136, s60, v173\n\t"
    "v_mfma_f32_16x16x32_bf16 v[148:151], a[184:187], a[72:75], v[148:151]\n\t"
    "ds_read_b128 a[68:71], v229 offset:512\n\t"
    "v_fma_f32 v137, v137, s60, v173\n\t"
    "v_mfma_f32_16x16x32_bf16 v[148:151], a[188:191], a[76:79], v[148:151]\n\t"
    "ds_read_b128 a[72:75], v229 offset:1024\n\t"
    "v_fma_f32 v138, v138, s60, v173\n\t"
    "v_mfma_f32_16x16x32_bf16 a[240:243], a[248:251], v[128:131], a[240:243]\n\t"
    "ds_read_b128 a[76:79], v229 offset:1536\n\t"
    "v_fma_f32 v139, v139, s60, v173\n\t"
    "v_mfma_f32_16x16x32_bf16 v[208:211], a[192:195], v[128:131], v[208:211]\n\t"
    "v_exp_f32 v136, v136\n\t"
    "v_mfma_f32_16x16x32_bf16 v[212:215], a[196:199], v[128:131], v[212:215]\n\t"
    "v_exp_f32 v137, v137\n\t"
    "v_mfma_f32_16x16x32_bf16 v[216:219], a[200:203], v[128:131], v[216:219]\n\t"
    "v_exp_f32 v138, v138\n\t"
    "v_mfma_f32_16x16x32_bf16 v[220:223], a[204:207], v[128:131], v[220:223]\n\t"
    "v_exp_f32 v139, v139\n\t"
    "v_mfma_f32_16x16x32_bf16 v[240:243], a[208:211], v[128:131], v[240:243]\n\t"
    "v_fma_f32 v140, v140, s60, v173\n\t"
    "v_fma_f32 v141, v141, s60, v173\n\t"
    "v_mfma_f32_16x16x32_bf16 v[244:247], a[212:215], v[128:131], v[244:247]\n\t"
    "v_exp_f32 v140, v140\n\t"
    "v_fma_f32 v142, v142, s60, v173\n\t"
    "v_mfma_f32_16x16x32_bf16 v[248:251], a[216:219], v[128:131], v[248:251]\n\t"
    "v_exp_f32 v141, v141\n\t"
    "v_mfma_f32_16x16x32_bf16 v[252:255], a[220:223], v[128:131], v[252:255]\n\t"
    "s_waitcnt vmcnt(63)\n\t"
    "v_exp_f32 v142, v142\n\t"
    "v_mfma_f32_16x16x32_bf16 v[152:155], a[160:163], a[80:83], 0\n\t"
    "v_fma_f32 v143, v143, s60, v173\n\t"
    "v_cvt_pk_bf16_f32 v136, v136, v137\n\t"
    "v_mfma_f32_16x16x32_bf16 v[152:155], a[164:167], a[84:87], v[152:155]\n\t"
    "v_exp_f32 v143, v143\n\t"
    "v_mfma_f32_16x16x32_bf16 v[152:155], a[168:171], a[88:91], v[152:155]\n\t"
    "v_cvt_pk_bf16_f32 v137, v138, v139\n\t"
    "v_cvt_pk_bf16_f32 v138, v140, v141\n\t"
    "v_mfma_f32_16x16x32_bf16 v[152:155], a[172:175], a[92:95], v[152:155]\n\t"
    "v_cvt_pk_bf16_f32 v139, v142, v143\n\t"
    "v_fma_f32 v144, v144, s60, v168\n\t"
    "v_mfma_f32_16x16x32_bf16 v[156:159], a[176:179], a[80:83], 0\n\t"
    "ds_read_b128 a[80:83], v229 offset:4096\n\t"
    "v_fma_f32 v145, v145, s60, v168\n\t"
    "v_mfma_f32_16x16x32_bf16 v[156:159], a[180:183], a[84:87], v[156:159]\n\t"
    "ds_read_b128 a[84:87], v229 offset:4608\n\t"
    "v_fma_f32 v146, v146, s60, v168\n\t"
    "v_mfma_f32_16x16x32_bf16 v[156:159], a[184:187], a[88:91], v[156:159]\n\t"
    "ds_read_b128 a[88:91], v229 offset:5120\n\t"
    "v_fma_f32 v147, v147, s60, v168\n\t"
    "v_mfma_f32_16x16x32_bf16 v[156:159], a[188:191], a[92:95], v[156:159]\n\t"
    "ds_read_b128 a[92:95], v229 offset:5632\n\t"
    "v_fma_f32 v148, v148, s60, v168\n\t"
    "v_mfma_f32_16x16x32_bf16 a[244:247], a[248:251], v[136:139], a[244:247]\n\t"
    "v_exp_f32 v144, v144\n\t"
    "v_mfma_f32_16x16x32_bf16 v[176:179], a[192:195], v[136:139], v[176:179]\n\t"
    "v_exp_f32 v145, v145\n\t"
    "v_mfma_f32_16x16x32_bf16 v[180:183], a[196:199], v[136:139], v[180:183]\n\t"
    "v_exp_f32 v146, v146\n\t"
    "v_mfma_f32_16x16x32_bf16 v[184:187], a[200:203], v[136:139], v[184:187]\n\t"
    "v_exp_f32 v147, v147\n\t"
    "v_mfma_f32_16x16x32_bf16 v[188:191], a[204:207], v[136:139], v[188:191]\n\t"
    "v_exp_f32 v148, v148\n\t"
    "v_mfma_f32_16x16x32_bf16 v[192:195], a[208:211], v[136:139], v[192:195]\n\t"
    "ds_read_b64_tr_b16 a[208:209], v225 offset:25600\n\t"
    "v_fma_f32 v149, v149, s60, v168\n\t"
    "v_fma_f32 v150, v150, s60, v168\n\t"
    "v_mfma_f32_16x16x32_bf16 v[196:199], a[212:215], v[136:139], v[196:199]\n\t"
    "v_exp_f32 v149, v149\n\t"
    "v_fma_f32 v151, v151, s60, v168\n\t"
    "v_exp_f32 v150, v150\n\t"
    "v_mfma_f32_16x16x32_bf16 v[200:203], a[216:219], v[136:139], v[200:203]\n\t"
    "ds_read_b64_tr_b16 a[210:211], v225 offset:29696\n\t"
    "v_cvt_pk_bf16_f32 v144, v144, v145\n\t"
    "v_mfma_f32_16x16x32_bf16 v[204:207], a[220:223], v[136:139], v[204:207]\n\t"
    "s_waitcnt vmcnt(63)\n\t"
    "v_exp_f32 v151, v151\n\t"
    "v_mfma_f32_16x16x32_bf16 v[128:131], a[160:163], a[96:99], 0\n\t"
    "ds_read_b64_tr_b16 a[212:213], v225 offset:25608\n\t"
    "v_cvt_pk_bf16_f32 v145, v146, v147\n\t"
    "v_mfma_f32_16x16x32_bf16 v[128:131], a[164:167], a[100:103], v[128:131]\n\t"
    "v_cvt_pk_bf16_f32 v146, v148, v149\n\t"
    "v_cvt_pk_bf16_f32 v147, v150, v151\n\t"
    "v_mfma_f32_16x16x32_bf16 v[128:131], a[168:171], a[104:107], v[128:131]\n\t"
    "ds_read_b64_tr_b16 a[214:215], v225 offset:29704\n\t"
    "v_fma_f32 v152, v152, s60, v169\n\t"
    "v_mfma_f32_16x16x32_bf16 v[128:131], a[172:175], a[108:111], v[128:131]\n\t"
    "ds_read_b64_tr_b16 a[216:217], v225 offset:26112\n\t"
    "v_fma_f32 v153, v153, s60, v169\n\t"
    "v_mfma_f32_16x16x32_bf16 v[132:135], a[176:179], a[96:99], 0\n\t"
    "ds_read_b64_tr_b16 a[218:219], v225 offset:30208\n\t"
    "v_fma_f32 v154, v154, s60, v169\n\t"
    "v_mfma_f32_16x16x32_bf16 v[132:135], a[180:183], a[100:103], v[132:135]\n\t"
    "ds_read_b64_tr_b16 a[220:221], v225 offset:26120\n\t"
    "v_fma_f32 v155, v155, s60, v169\n\t"
    "v_mfma_f32_16x16x32_bf16 v[132:135], a[184:187], a[104:107], v[132:135]\n\t"
    "ds_read_b64_tr_b16 a[222:223], v225 offset:30216\n\t"
    "v_fma_f32 v156, v156, s60, v169\n\t"
    "v_mfma_f32_16x16x32_bf16 v[132:135], a[188:191], a[108:111], v[132:135]\n\t"
    "ds_read_b128 a[96:99], v229 offset:8192\n\t"
    "v_fma_f32 v157, v157, s60, v169\n\t"
    "v_mfma_f32_16x16x32_bf16 a[224:227], a[248:251], v[144:147], a[224:227]\n\t"
    "s_waitcnt lgkmcnt(15)\n\t"
    "v_exp_f32 v152, v152\n\t"
    "v_mfma_f32_16x16x32_bf16 v[0:3], a[32:35], v[144:147], v[0:3]\n\t"
    "ds_read_b128 a[100:103], v229 offset:8704\n\t"
    "v_fma_f32 v158, v158, s60, v169\n\t"
    "v_mfma_f32_16x16x32_bf16 v[4:7], a[36:39], v[144:147], v[4:7]\n\t"
    "ds_read_b128 a[104:107], v229 offset:9216\n\t"
    "v_fma_f32 v159, v159, s60, v169\n\t"
    "v_mfma_f32_16x16x32_bf16 v[8:11], a[40:43], v[144:147], v[8:11]\n\t"
    "ds_read_b128 a[108:111], v229 offset:9728\n\t"
    "v_fma_f32 v128, v128, s60, v170\n\t"
    "v_mfma_f32_16x16x32_bf16 v[12:15], a[44:47], v[144:147], v[12:15]\n\t"
    "s_waitcnt lgkmcnt(8)\n\t"
    "v_exp_f32 v153, v153\n\t"
    "v_mfma_f32_16x16x32_bf16 v[16:19], a[208:211], v[144:147], v[16:19]\n\t"
    "v_exp_f32 v154, v154\n\t"
    "v_exp_f32 v155, v155\n\t"
    "v_exp_f32 v156, v156\n\t"
    "v_exp_f32 v157, v157\n\t"
    "v_exp_f32 v158, v158\n\t"
    "v_exp_f32 v159, v159\n\t"
    "v_accvgpr_read_b32 v238, a224\n\t"
    "v_subrev_u32 v236, 0x35800000, v238\n\t"
    "v_mfma_f32_16x16x32_bf16 v[20:23], a[212:215], v[144:147], v[20:23]\n\t"
    "s_waitcnt lgkmcnt(4)\n\t"
    "v_rcp_f32 v238, v238\n\t"
    "v_cvt_pk_bf16_f32 v152, v152, v153\n\t"
    "v_cvt_pk_bf16_f32 v153, v154, v155\n\t"
    "v_mfma_f32_16x16x32_bf16 v[24:27], a[216:219], v[144:147], v[24:27]\n\t"
    "v_exp_f32 v128, v128\n\t"
    "v_mfma_f32_16x16x32_bf16 v[28:31], a[220:223], v[144:147], v[28:31]\n\t"
    "s_waitcnt vmcnt(47)\n\t"
    "v_cvt_pk_bf16_f32 v154, v156, v157\n\t"
    "v_cvt_pk_bf16_f32 v155, v158, v159\n\t"
    "v_mfma_f32_16x16x32_bf16 v[136:139], a[160:163], a[112:115], 0\n\t"
    "s_waitcnt vmcnt(16) lgkmcnt(4)\n\t"
    "v_fma_f32 v129, v129, s60, v170\n\t"
    "v_fma_f32 v130, v130, s60, v170\n\t"
    "v_mfma_f32_16x16x32_bf16 v[136:139], a[164:167], a[116:119], v[136:139]\n\t"
    "s_barrier\n\t"
    "v_exp_f32 v129, v129\n\t"
    "v_mfma_f32_16x16x32_bf16 v[136:139], a[168:171], a[120:123], v[136:139]\n\t"
    "ds_read_b128 a[48:51], v239 offset:0\n\t"
    "v_fma_f32 v131, v131, s60, v170\n\t"
    "v_mfma_f32_16x16x32_bf16 v[136:139], a[172:175], a[124:127], v[136:139]\n\t"
    "s_xor_b32 s64, s52, 24576\n\t"
    "v_exp_f32 v130, v130\n\t"
    "v_mfma_f32_16x16x32_bf16 v[140:143], a[176:179], a[112:115], 0\n\t"
    "ds_read_b128 a[52:55], v239 offset:512\n\t"
    "s_add_u32 m0, s63, 24576\n\t"
    "v_fma_f32 v132, v132, s60, v170\n\t"
    "v_fma_f32 v133, v133, s60, v170\n\t"
    "v_fma_f32 v134, v134, s60, v170\n\t"
    "v_pk_mul_f32 v[0:1], v[0:1], v[238:239] op_sel_hi:[1,0]\n\t"
    "v_pk_mul_f32 v[2:3], v[2:3], v[238:239] op_sel_hi:[1,0]\n\t"
    "v_pk_mul_f32 v[4:5], v[4:5], v[238:239] op_sel_hi:[1,0]\n\t"
    "v_pk_mul_f32 v[6:7], v[6:7], v[238:239] op_sel_hi:[1,0]\n\t"
    "v_pk_mul_f32 v[8:9], v[8:9], v[238:239] op_sel_hi:[1,0]\n\t"
    "v_pk_mul_f32 v[10:11], v[10:11], v[238:239] op_sel_hi:[1,0]\n\t"
    "v_pk_mul_f32 v[12:13], v[12:13], v[238:239] op_sel_hi:[1,0]\n\t"
    "v_pk_mul_f32 v[14:15], v[14:15], v[238:239] op_sel_hi:[1,0]\n\t"
    "v_pk_mul_f32 v[16:17], v[16:17], v[238:239] op_sel_hi:[1,0]\n\t"
    "v_pk_mul_f32 v[18:19], v[18:19], v[238:239] op_sel_hi:[1,0]\n\t"
    "v_pk_mul_f32 v[20:21], v[20:21], v[238:239] op_sel_hi:[1,0]\n\t"
    "v_pk_mul_f32 v[22:23], v[22:23], v[238:239] op_sel_hi:[1,0]\n\t"
    "v_pk_mul_f32 v[24:25], v[24:25], v[238:239] op_sel_hi:[1,0]\n\t"
    "v_pk_mul_f32 v[26:27], v[26:27], v[238:239] op_sel_hi:[1,0]\n\t"
    "v_pk_mul_f32 v[28:29], v[28:29], v[238:239] op_sel_hi:[1,0]\n\t"
    "v_mfma_f32_16x16x32_bf16 v[140:143], a[180:183], a[116:119], v[140:143]\n\t"
    "ds_read_b128 a[112:115], v229 offset:12288\n\t"
    "v_fma_f32 v135, v135, s60, v170\n\t"
    "v_exp_f32 v131, v131\n\t"
    "v_cvt_pk_bf16_f32 v128, v128, v129\n\t"
    "v_pk_mul_f32 v[30:31], v[30:31], v[238:239] op_sel_hi:[1,0]\n\t"
    "v_mfma_f32_16x16x32_bf16 v[140:143], a[184:187], a[120:123], v[140:143]\n\t"
    "ds_read_b128 a[56:59], v239 offset:1024\n\t"
    "v_cvt_pk_bf16_f32 v129, v130, v131\n\t"
    "v_mfma_f32_16x16x32_bf16 v[140:143], a[188:191], a[124:127], v[140:143]\n\t"
    "ds_read_b128 a[116:119], v229 offset:12800\n\t"
    "v_cmp_le_u32 vcc, s62, v236\n\t"
    "v_mfma_f32_16x16x32_bf16 a[228:231], a[248:251], v[152:155], a[228:231]\n\t"
    "ds_read_b128 a[60:63], v239 offset:1536\n\t"
    "v_fma_f32 v136, v136, s60, v171\n\t"
    "v_mfma_f32_16x16x32_bf16 v[32:35], a[32:35], v[152:155], v[32:35]\n\t"
    "ds_read_b128 a[120:123], v229 offset:13312\n\t"
    "v_fma_f32 v137, v137, s60, v171\n\t"
    "v_mfma_f32_16x16x32_bf16 v[36:39], a[36:39], v[152:155], v[36:39]\n\t"
    "ds_read_b64_tr_b16 a[192:193], v225 offset:0\n\t"
    "v_fma_f32 v138, v138, s60, v171\n\t"
    "v_mfma_f32_16x16x32_bf16 v[40:43], a[40:43], v[152:155], v[40:43]\n\t"
    "ds_read_b128 a[124:127], v229 offset:13824\n\t"
    "v_fma_f32 v139, v139, s60, v171\n\t"
    "v_mfma_f32_16x16x32_bf16 v[44:47], a[44:47], v[152:155], v[44:47]\n\t"
    "ds_read_b64_tr_b16 a[194:195], v225 offset:4096\n\t"
    "v_fma_f32 v140, v140, s60, v171\n\t"
    "v_mfma_f32_16x16x32_bf16 v[48:51], a[208:211], v[152:155], v[48:51]\n\t"
    "buffer_load_dwordx4 v226, s[36:39], s64 offen offset:0 lds\n\t"
    "v_exp_f32 v132, v132\n\t"
    "v_exp_f32 v133, v133\n\t"
    "v_mfma_f32_16x16x32_bf16 v[52:55], a[212:215], v[152:155], v[52:55]\n\t"
    "ds_read_b64_tr_b16 a[196:197], v225 offset:8\n\t"
    "v_accvgpr_read_b32 v239, a228\n\t"
    "v_mfma_f32_16x16x32_bf16 v[56:59], a[216:219], v[152:155], v[56:59]\n\t"
    "s_or_b64 s[58:59], s[58:59], vcc\n\t"
    "v_exp_f32 v134, v134\n\t"
    "v_mfma_f32_16x16x32_bf16 v[60:63], a[220:223], v[152:155], v[60:63]\n\t"
    "ds_read_b64_tr_b16 a[198:199], v225 offset:4104\n\t"
    "v_subrev_u32 v237, 0x35800000, v239\n\t"
    "v_mfma_f32_16x16x32_bf16 v[144:147], a[160:163], a[128:131], 0\n\t"
    "s_waitcnt vmcnt(28)\n\t"
    "v_exp_f32 v135, v135\n\t"
    "v_mfma_f32_16x16x32_bf16 v[144:147], a[164:167], a[132:135], v[144:147]\n\t"
    "ds_read_b64_tr_b16 a[200:201], v225 offset:512\n\t"
    "v_cvt_pk_bf16_f32 v130, v132, v133\n\t"
    "v_mfma_f32_16x16x32_bf16 v[144:147], a[168:171], a[136:139], v[144:147]\n\t"
    "buffer_load_dwordx4 v226, s[36:39], s64 offen offset:2048 lds\n\t"
    "v_mfma_f32_16x16x32_bf16 v[144:147], a[172:175], a[140:143], v[144:147]\n\t"
    "ds_read_b64_tr_b16 a[202:203], v225 offset:4608\n\t"
    "v_cvt_pk_bf16_f32 v131, v134, v135\n\t"
    "v_rcp_f32 v239, v239\n\t"
    "v_mfma_f32_16x16x32_bf16 v[148:151], a[176:179], a[128:131], 0\n\t"
    "ds_read_b128 a[128:131], v229 offset:16384\n\t"
    "v_fma_f32 v141, v141, s60, v171\n\t"
    "v_fma_f32 v142, v142, s60, v171\n\t"
    "v_fma_f32 v143, v143, s60, v171\n\t"
    "v_cmp_le_u32 vcc, s62, v237\n\t"
    "v_pk_mul_f32 v[32:33], v[32:33], v[238:239] op_sel:[0,1] op_sel_hi:[1,1]\n\t"
    "v_pk_mul_f32 v[34:35], v[34:35], v[238:239] op_sel:[0,1] op_sel_hi:[1,1]\n\t"
    "v_pk_mul_f32 v[36:37], v[36:37], v[238:239] op_sel:[0,1] op_sel_hi:[1,1]\n\t"
    "v_pk_mul_f32 v[38:39], v[38:39], v[238:239] op_sel:[0,1] op_sel_hi:[1,1]\n\t"
    "v_pk_mul_f32 v[40:41], v[40:41], v[238:239] op_sel:[0,1] op_sel_hi:[1,1]\n\t"
    "v_pk_mul_f32 v[42:43], v[42:43], v[238:239] op_sel:[0,1] op_sel_hi:[1,1]\n\t"
    "v_pk_mul_f32 v[44:45], v[44:45], v[238:239] op_sel:[0,1] op_sel_hi:[1,1]\n\t"
    "v_pk_mul_f32 v[46:47], v[46:47], v[238:239] op_sel:[0,1] op_sel_hi:[1,1]\n\t"
    "v_pk_mul_f32 v[48:49], v[48:49], v[238:239] op_sel:[0,1] op_sel_hi:[1,1]\n\t"
    "v_pk_mul_f32 v[50:51], v[50:51], v[238:239] op_sel:[0,1] op_sel_hi:[1,1]\n\t"
    "v_pk_mul_f32 v[52:53], v[52:53], v[238:239] op_sel:[0,1] op_sel_hi:[1,1]\n\t"
    "v_pk_mul_f32 v[54:55], v[54:55], v[238:239] op_sel:[0,1] op_sel_hi:[1,1]\n\t"
    "v_pk_mul_f32 v[56:57], v[56:57], v[238:239] op_sel:[0,1] op_sel_hi:[1,1]\n\t"
    "v_pk_mul_f32 v[58:59], v[58:59], v[238:239] op_sel:[0,1] op_sel_hi:[1,1]\n\t"
    "v_pk_mul_f32 v[60:61], v[60:61], v[238:239] op_sel:[0,1] op_sel_hi:[1,1]\n\t"
    "v_mfma_f32_16x16x32_bf16 v[148:151], a[180:183], a[132:135], v[148:151]\n\t"
    "ds_read_b64_tr_b16 a[204:205], v225 offset:520\n\t"
    "s_or_b64 s[58:59], s[58:59], vcc\n\t"
    "v_exp_f32 v136, v136\n\t"
    "v_cvt_pk_bf16_f32 v0, v0, v1\n\t"
    "v_pk_mul_f32 v[62:63], v[62:63], v[238:239] op_sel:[0,1] op_sel_hi:[1,1]\n\t"
    "v_mfma_f32_16x16x32_bf16 v[148:151], a[184:187], a[136:139], v[148:151]\n\t"
    "ds_read_b128 a[132:135], v229 offset:16896\n\t"
    "v_cvt_pk_bf16_f32 v1, v2, v3\n\t"
    "v_mfma_f32_16x16x32_bf16 v[148:151], a[188:191], a[140:143], v[148:151]\n\t"
    "ds_read_b64_tr_b16 a[206:207], v225 offset:4616\n\t"
    "v_cvt_pk_bf16_f32 v2, v4, v5\n\t"
    "v_mfma_f32_16x16x32_bf16 a[232:235], a[248:251], v[128:131], a[232:235]\n\t"
    "ds_read_b128 a[136:139], v229 offset:17408\n\t"
    "v_fma_f32 v144, v144, s60, v172\n\t"
    "v_mfma_f32_16x16x32_bf16 v[64:67], a[32:35], v[128:131], v[64:67]\n\t"
    "ds_read_b128 a[140:143], v229 offset:17920\n\t"
    "v_fma_f32 v145, v145, s60, v172\n\t"
    "v_mfma_f32_16x16x32_bf16 v[68:71], a[36:39], v[128:131], v[68:71]\n\t"
    "s_add_u32 m0, s63, 57344\n\t"
    "v_exp_f32 v137, v137\n\t"
    "v_mfma_f32_16x16x32_bf16 v[72:75], a[40:43], v[128:131], v[72:75]\n\t"
    "buffer_load_dwordx4 v226, s[40:43], s64 offen offset:0 lds\n\t"
    "v_mfma_f32_16x16x32_bf16 v[76:79], a[44:47], v[128:131], v[76:79]\n\t"
    "v_exp_f32 v138, v138\n\t"
    "v_exp_f32 v139, v139\n\t"
    "v_exp_f32 v140, v140\n\t"
    "v_exp_f32 v141, v141\n\t"
    "v_exp_f32 v142, v142\n\t"
    "v_mfma_f32_16x16x32_bf16 v[80:83], a[208:211], v[128:131], v[80:83]\n\t"
    "v_exp_f32 v143, v143\n\t"
    "v_accvgpr_read_b32 v238, a232\n\t"
    "v_subrev_u32 v236, 0x35800000, v238\n\t"
    "v_rcp_f32 v238, v238\n\t"
    "v_cvt_pk_bf16_f32 v136, v136, v137\n\t"
    "v_cvt_pk_bf16_f32 v137, v138, v139\n\t"
    "v_cvt_pk_bf16_f32 v138, v140, v141\n\t"
    "v_fma_f32 v146, v146, s60, v172\n\t"
    "v_fma_f32 v147, v147, s60, v172\n\t"
    "v_fma_f32 v148, v148, s60, v172\n\t"
    "v_fma_f32 v149, v149, s60, v172\n\t"
    "v_fma_f32 v150, v150, s60, v172\n\t"
    "v_fma_f32 v151, v151, s60, v172\n\t"
    "v_mfma_f32_16x16x32_bf16 v[84:87], a[212:215], v[128:131], v[84:87]\n\t"
    "v_cvt_pk_bf16_f32 v139, v142, v143\n\t"
    "v_cmp_le_u32 vcc, s62, v236\n\t"
    "v_exp_f32 v144, v144\n\t"
    "v_mfma_f32_16x16x32_bf16 v[88:91], a[216:219], v[128:131], v[88:91]\n\t"
    "s_waitcnt lgkmcnt(15)\n\t"
    "s_or_b64 s[58:59], s[58:59], vcc\n\t"
    "v_cvt_pk_bf16_f32 v3, v6, v7\n\t"
    "v_exp_f32 v145, v145\n\t"
    "v_exp_f32 v146, v146\n\t"
    "v_exp_f32 v147, v147\n\t"
    "v_mfma_f32_16x16x32_bf16 v[92:95], a[220:223], v[128:131], v[92:95]\n\t"
    "s_waitcnt lgkmcnt(14)\n\t"
    "v_cvt_pk_bf16_f32 v144, v144, v145\n\t"
    "v_cvt_pk_bf16_f32 v145, v146, v147\n\t"
    "v_mfma_f32_16x16x32_bf16 v[160:163], a[48:51], a[64:67], 0\n\t"
    "buffer_load_dwordx4 v226, s[40:43], s64 offen offset:2048 lds\n\t"
    "v_mfma_f32_16x16x32_bf16 v[160:163], a[52:55], a[68:71], v[160:163]\n\t"
    "ds_write_b128 v230, v[0:3] offset:0\n\t"
    "v_mfma_f32_16x16x32_bf16 v[160:163], a[56:59], a[72:75], v[160:163]\n\t"
    "v_exp_f32 v148, v148\n\t"
    "v_mfma_f32_16x16x32_bf16 v[160:163], a[60:63], a[76:79], v[160:163]\n\t"
    "s_waitcnt vmcnt(15)\n\t"
    "v_cvt_pk_bf16_f32 v8, v8, v9\n\t"
    "v_cvt_pk_bf16_f32 v9, v10, v11\n\t"
    "v_mfma_f32_16x16x32_bf16 v[152:155], a[160:163], a[144:147], 0\n\t"
    "ds_read_b128 a[160:163], v224 offset:0\n\t"
    "v_cvt_pk_bf16_f32 v10, v12, v13\n\t"
    "v_exp_f32 v149, v149\n\t"
    "v_cvt_pk_bf16_f32 v11, v14, v15\n\t"
    "v_pk_mul_f32 v[64:65], v[64:65], v[238:239] op_sel_hi:[1,0]\n\t"
    "v_pk_mul_f32 v[66:67], v[66:67], v[238:239] op_sel_hi:[1,0]\n\t"
    "v_pk_mul_f32 v[68:69], v[68:69], v[238:239] op_sel_hi:[1,0]\n\t"
    "v_pk_mul_f32 v[70:71], v[70:71], v[238:239] op_sel_hi:[1,0]\n\t"
    "v_pk_mul_f32 v[72:73], v[72:73], v[238:239] op_sel_hi:[1,0]\n\t"
    "v_pk_mul_f32 v[74:75], v[74:75], v[238:239] op_sel_hi:[1,0]\n\t"
    "v_pk_mul_f32 v[76:77], v[76:77], v[238:239] op_sel_hi:[1,0]\n\t"
    "v_pk_mul_f32 v[78:79], v[78:79], v[238:239] op_sel_hi:[1,0]\n\t"
    "v_pk_mul_f32 v[80:81], v[80:81], v[238:239] op_sel_hi:[1,0]\n\t"
    "v_pk_mul_f32 v[82:83], v[82:83], v[238:239] op_sel_hi:[1,0]\n\t"
    "v_pk_mul_f32 v[84:85], v[84:85], v[238:239] op_sel_hi:[1,0]\n\t"
    "v_pk_mul_f32 v[86:87], v[86:87], v[238:239] op_sel_hi:[1,0]\n\t"
    "v_pk_mul_f32 v[88:89], v[88:89], v[238:239] op_sel_hi:[1,0]\n\t"
    "v_pk_mul_f32 v[90:91], v[90:91], v[238:239] op_sel_hi:[1,0]\n\t"
    "v_mfma_f32_16x16x32_bf16 v[152:155], a[164:167], a[148:151], v[152:155]\n\t"
    "ds_write_b128 v231, v[8:11] offset:0\n\t"
    "v_exp_f32 v150, v150\n\t"
    "v_exp_f32 v151, v151\n\t"
    "v_cvt_pk_bf16_f32 v146, v148, v149\n\t"
    "v_pk_mul_f32 v[92:93], v[92:93], v[238:239] op_sel_hi:[1,0]\n\t"
    "v_pk_mul_f32 v[94:95], v[94:95], v[238:239] op_sel_hi:[1,0]\n\t"
    "v_mfma_f32_16x16x32_bf16 v[152:155], a[168:171], a[152:155], v[152:155]\n\t"
    "ds_read_b128 a[164:167], v224 offset:512\n\t"
    "v_cvt_pk_bf16_f32 v147, v150, v151\n\t"
    "v_cvt_pk_bf16_f32 v16, v16, v17\n\t"
    "v_cvt_pk_bf16_f32 v17, v18, v19\n\t"
    "v_cvt_pk_bf16_f32 v18, v20, v21\n\t"
    "v_cvt_pk_bf16_f32 v19, v22, v23\n\t"
    "v_cvt_pk_bf16_f32 v24, v24, v25\n\t"
    "v_mfma_f32_16x16x32_bf16 v[152:155], a[172:175], a[156:159], v[152:155]\n\t"
    "v_cvt_pk_bf16_f32 v25, v26, v27\n\t"
    "v_cvt_pk_bf16_f32 v26, v28, v29\n\t"
    "v_mfma_f32_16x16x32_bf16 v[156:159], a[176:179], a[144:147], 0\n\t"
    "ds_read_b128 a[168:171], v224 offset:1024\n\t"
    "v_max3_f32 v160, v160, v161, v162\n\t"
    "v_max_f32 v160, v160, v163\n\t"
    "v_mul_f32 v168, s61, v160\n\t"
    "v_cvt_pk_bf16_f32 v27, v30, v31\n\t"
    "v_mfma_f32_16x16x32_bf16 v[156:159], a[180:183], a[148:151], v[156:159]\n\t"
    "ds_read_b128 a[144:147], v229 offset:20480\n\t"
    "v_cvt_pk_bf16_f32 v32, v32, v33\n\t"
    "v_cvt_pk_bf16_f32 v33, v34, v35\n\t"
    "v_cvt_pk_bf16_f32 v34, v36, v37\n\t"
    "v_cvt_pk_bf16_f32 v35, v38, v39\n\t"
    "v_cvt_pk_bf16_f32 v40, v40, v41\n\t"
    "v_cvt_pk_bf16_f32 v41, v42, v43\n\t"
    "v_mfma_f32_16x16x32_bf16 v[156:159], a[184:187], a[152:155], v[156:159]\n\t"
    "ds_read_b128 a[172:175], v224 offset:1536\n\t"
    "v_cvt_pk_bf16_f32 v42, v44, v45\n\t"
    "v_mfma_f32_16x16x32_bf16 v[156:159], a[188:191], a[156:159], v[156:159]\n\t"
    "ds_read_b128 a[148:151], v229 offset:20992\n\t"
    "v_cvt_pk_bf16_f32 v43, v46, v47\n\t"
    "v_mfma_f32_16x16x32_bf16 a[236:239], a[248:251], v[136:139], a[236:239]\n\t"
    "ds_read_b128 a[176:179], v224 offset:4096\n\t"
    "v_fma_f32 v152, v152, s60, v173\n\t"
    "v_fma_f32 v153, v153, s60, v173\n\t"
    "v_fma_f32 v154, v154, s60, v173\n\t"
    "v_fma_f32 v155, v155, s60, v173\n\t"
    "v_cvt_pk_bf16_f32 v48, v48, v49\n\t"
    "v_cvt_pk_bf16_f32 v49, v50, v51\n\t"
    "v_cvt_pk_bf16_f32 v50, v52, v53\n\t"
    "v_cvt_pk_bf16_f32 v51, v54, v55\n\t"
    "v_cvt_pk_bf16_f32 v56, v56, v57\n\t"
    "v_cvt_pk_bf16_f32 v57, v58, v59\n\t"
    "v_mfma_f32_16x16x32_bf16 v[96:99], a[32:35], v[136:139], v[96:99]\n\t"
    "ds_read_b128 a[152:155], v229 offset:21504\n\t"
    "v_cvt_pk_bf16_f32 v58, v60, v61\n\t"
    "v_exp_f32 v152, v152\n\t"
    "v_exp_f32 v153, v153\n\t"
    "v_exp_f32 v154, v154\n\t"
    "v_exp_f32 v155, v155\n\t"
    "v_mfma_f32_16x16x32_bf16 v[100:103], a[36:39], v[136:139], v[100:103]\n\t"
    "ds_read_b128 a[180:183], v224 offset:4608\n\t"
    "v_cvt_pk_bf16_f32 v152, v152, v153\n\t"
    "v_cvt_pk_bf16_f32 v153, v154, v155\n\t"
    "v_cvt_pk_bf16_f32 v59, v62, v63\n\t"
    "v_mfma_f32_16x16x32_bf16 v[104:107], a[40:43], v[136:139], v[104:107]\n\t"
    "ds_read_b128 a[156:159], v229 offset:22016\n\t"
    "v_cvt_pk_bf16_f32 v64, v64, v65\n\t"
    "v_cvt_pk_bf16_f32 v65, v66, v67\n\t"
    "v_cvt_pk_bf16_f32 v66, v68, v69\n\t"
    "v_cvt_pk_bf16_f32 v67, v70, v71\n\t"
    "v_cvt_pk_bf16_f32 v72, v72, v73\n\t"
    "v_cvt_pk_bf16_f32 v73, v74, v75\n\t"
    "v_mfma_f32_16x16x32_bf16 v[108:111], a[44:47], v[136:139], v[108:111]\n\t"
    "ds_read_b128 a[184:187], v224 offset:5120\n\t"
    "v_fma_f32 v156, v156, s60, v173\n\t"
    "v_fma_f32 v157, v157, s60, v173\n\t"
    "v_fma_f32 v158, v158, s60, v173\n\t"
    "v_fma_f32 v159, v159, s60, v173\n\t"
    "v_cvt_pk_bf16_f32 v74, v76, v77\n\t"
    "v_mfma_f32_16x16x32_bf16 v[112:115], a[208:211], v[136:139], v[112:115]\n\t"
    "ds_write_b128 v230, v[16:19] offset:128\n\t"
    "v_accvgpr_read_b32 v239, a236\n\t"
    "v_subrev_u32 v237, 0x35800000, v239\n\t"
    "v_rcp_f32 v239, v239\n\t"
    "v_exp_f32 v156, v156\n\t"
    "v_exp_f32 v157, v157\n\t"
    "v_exp_f32 v158, v158\n\t"
    "v_mfma_f32_16x16x32_bf16 v[116:119], a[212:215], v[136:139], v[116:119]\n\t"
    "ds_read_b128 a[188:191], v224 offset:5632\n\t"
    "v_cvt_pk_bf16_f32 v154, v156, v157\n\t"
    "v_exp_f32 v159, v159\n\t"
    "v_mfma_f32_16x16x32_bf16 v[120:123], a[216:219], v[136:139], v[120:123]\n\t"
    "v_cvt_pk_bf16_f32 v155, v158, v159\n\t"
    "v_cmp_le_u32 vcc, s62, v237\n\t"
    "v_mfma_f32_16x16x32_bf16 v[124:127], a[220:223], v[136:139], v[124:127]\n\t"
    "s_waitcnt lgkmcnt(15)\n\t"
    "v_cvt_pk_bf16_f32 v75, v78, v79\n\t"
    "v_cvt_pk_bf16_f32 v80, v80, v81\n\t"
    "s_cmp_lg_u32 s57, 0\n\t"
    "s_cbranch_scc1 .Lbody%=\n\t"
    ".Lfin%=:\n\t"
    "s_or_b64 s[58:59], s[58:59], vcc\n\t"
    "ds_write_b128 v231, v[24:27] offset:128\n\t"
    "ds_read_b128 a[64:67], v232 offset:0\n\t"
    "ds_read_b128 a[68:71], v232 offset:1024\n\t"
    "ds_read_b128 a[72:75], v232 offset:2048\n\t"
    "ds_read_b128 a[76:79], v232 offset:3072\n\t"
    "v_cvt_pk_bf16_f32 v81, v82, v83\n\t"
    "v_cvt_pk_bf16_f32 v82, v84, v85\n\t"
    "v_cvt_pk_bf16_f32 v83, v86, v87\n\t"
    "v_cvt_pk_bf16_f32 v88, v88, v89\n\t"
    "v_cvt_pk_bf16_f32 v89, v90, v91\n\t"
    "ds_write_b128 v230, v[32:35] offset:4096\n\t"
    "v_cvt_pk_bf16_f32 v90, v92, v93\n\t"
    "v_pk_mul_f32 v[96:97], v[96:97], v[238:239] op_sel:[0,1] op_sel_hi:[1,1]\n\t"
    "v_pk_mul_f32 v[98:99], v[98:99], v[238:239] op_sel:[0,1] op_sel_hi:[1,1]\n\t"
    "v_pk_mul_f32 v[100:101], v[100:101], v[238:239] op_sel:[0,1] op_sel_hi:[1,1]\n\t"
    "v_pk_mul_f32 v[102:103], v[102:103], v[238:239] op_sel:[0,1] op_sel_hi:[1,1]\n\t"
    "ds_write_b128 v231, v[40:43] offset:4096\n\t"
    "s_waitcnt lgkmcnt(2)\n\t"
    "s_add_u32 s66, s54, 0\n\t"
    "buffer_store_dwordx4 a[64:67], v227, s[48:51], s66 offen offset:0 nt\n\t"
    "v_pk_mul_f32 v[104:105], v[104:105], v[238:239] op_sel:[0,1] op_sel_hi:[1,1]\n\t"
    "v_pk_mul_f32 v[106:107], v[106:107], v[238:239] op_sel:[0,1] op_sel_hi:[1,1]\n\t"
    "v_pk_mul_f32 v[108:109], v[108:109], v[238:239] op_sel:[0,1] op_sel_hi:[1,1]\n\t"
    "v_pk_mul_f32 v[110:111], v[110:111], v[238:239] op_sel:[0,1] op_sel_hi:[1,1]\n\t"
    "v_pk_mul_f32 v[112:113], v[112:113], v[238:239] op_sel:[0,1] op_sel_hi:[1,1]\n\t"
    "buffer_store_dwordx4 a[68:71], v228, s[48:51], s66 offen offset:1024 nt\n\t"
    "ds_write_b128 v230, v[48:51] offset:4224\n\t"
    "v_pk_mul_f32 v[114:115], v[114:115], v[238:239] op_sel:[0,1] op_sel_hi:[1,1]\n\t"
    "v_pk_mul_f32 v[116:117], v[116:117], v[238:239] op_sel:[0,1] op_sel_hi:[1,1]\n\t"
    "v_pk_mul_f32 v[118:119], v[118:119], v[238:239] op_sel:[0,1] op_sel_hi:[1,1]\n\t"
    "v_pk_mul_f32 v[120:121], v[120:121], v[238:239] op_sel:[0,1] op_sel_hi:[1,1]\n\t"
    "buffer_store_dwordx4 a[72:75], v227, s[48:51], s66 offen offset:2048 nt\n\t"
    "v_pk_mul_f32 v[122:123], v[122:123], v[238:239] op_sel:[0,1] op_sel_hi:[1,1]\n\t"
    "ds_write_b128 v231, v[56:59] offset:4224\n\t"
    "ds_read_b128 a[80:83], v232 offset:4096\n\t"
    "ds_read_b128 a[84:87], v232 offset:5120\n\t"
    "ds_read_b128 a[88:91], v232 offset:6144\n\t"
    "ds_read_b128 a[92:95], v232 offset:7168\n\t"
    "v_cvt_pk_bf16_f32 v91, v94, v95\n\t"
    "v_cvt_pk_bf16_f32 v96, v96, v97\n\t"
    "v_cvt_pk_bf16_f32 v97, v98, v99\n\t"
    "buffer_store_dwordx4 a[76:79], v228, s[48:51], s66 offen offset:3072 nt\n\t"
    "v_cvt_pk_bf16_f32 v98, v100, v101\n\t"
    "v_cvt_pk_bf16_f32 v99, v102, v103\n\t"
    "ds_write_b128 v230, v[64:67] offset:8192\n\t"
    "v_cvt_pk_bf16_f32 v104, v104, v105\n\t"
    "v_cvt_pk_bf16_f32 v105, v106, v107\n\t"
    "v_cvt_pk_bf16_f32 v106, v108, v109\n\t"
    "v_cvt_pk_bf16_f32 v107, v110, v111\n\t"
    "v_cvt_pk_bf16_f32 v112, v112, v113\n\t"
    "ds_write_b128 v231, v[72:75] offset:8192\n\t"
    "v_cvt_pk_bf16_f32 v113, v114, v115\n\t"
    "v_cvt_pk_bf16_f32 v114, v116, v117\n\t"
    "v_pk_mul_f32 v[124:125], v[124:125], v[238:239] op_sel:[0,1] op_sel_hi:[1,1]\n\t"
    "s_waitcnt lgkmcnt(2)\n\t"
    "s_add_u32 s66, s54, 4096\n\t"
    "buffer_store_dwordx4 a[80:83], v227, s[48:51], s66 offen offset:0 nt\n\t"
    "v_pk_mul_f32 v[126:127], v[126:127], v[238:239] op_sel:[0,1] op_sel_hi:[1,1]\n\t"
    "v_cvt_pk_bf16_f32 v115, v118, v119\n\t"
    "ds_write_b128 v230, v[80:83] offset:8320\n\t"
    "v_cvt_pk_bf16_f32 v120, v120, v121\n\t"
    "v_cvt_pk_bf16_f32 v121, v122, v123\n\t"
    "buffer_store_dwordx4 a[84:87], v228, s[48:51], s66 offen offset:1024 nt\n\t"
    "v_cvt_pk_bf16_f32 v122, v124, v125\n\t"
    "v_cvt_pk_bf16_f32 v123, v126, v127\n\t"
    "v_mfma_f32_16x16x32_bf16 a[240:243], a[248:251], v[144:147], a[240:243]\n\t"
    "ds_write_b128 v231, v[88:91] offset:8320\n\t"
    "ds_read_b128 a[96:99], v232 offset:8192\n\t"
    "ds_read_b128 a[100:103], v232 offset:9216\n\t"
    "ds_read_b128 a[104:107], v232 offset:10240\n\t"
    "ds_read_b128 a[108:111], v232 offset:11264\n\t"
    "v_mfma_f32_16x16x32_bf16 v[208:211], a[32:35], v[144:147], v[208:211]\n\t"
    "buffer_store_dwordx4 a[88:91], v227, s[48:51], s66 offen offset:2048 nt\n\t"
    "v_mfma_f32_16x16x32_bf16 v[212:215], a[36:39], v[144:147], v[212:215]\n\t"
    "v_mfma_f32_16x16x32_bf16 v[216:219], a[40:43], v[144:147], v[216:219]\n\t"
    "v_mfma_f32_16x16x32_bf16 v[220:223], a[44:47], v[144:147], v[220:223]\n\t"
    "v_mfma_f32_16x16x32_bf16 v[240:243], a[208:211], v[144:147], v[240:243]\n\t"
    "ds_write_b128 v230, v[96:99] offset:12288\n\t"
    "buffer_store_dwordx4 a[92:95], v228, s[48:51], s66 offen offset:3072 nt\n\t"
    "v_accvgpr_read_b32 v238, a240\n\t"
    "v_subrev_u32 v236, 0x35800000, v238\n\t"
    "v_rcp_f32 v238, v238\n\t"
    "v_cmp_le_u32 vcc, s62, v236\n\t"
    "v_mfma_f32_16x16x32_bf16 v[244:247], a[212:215], v[144:147], v[244:247]\n\t"
    "ds_write_b128 v231, v[104:107] offset:12288\n\t"
    "s_or_b64 s[58:59], s[58:59], vcc\n\t"
    "v_mfma_f32_16x16x32_bf16 v[248:251], a[216:219], v[144:147], v[248:251]\n\t"
    "v_mfma_f32_16x16x32_bf16 v[252:255], a[220:223], v[144:147], v[252:255]\n\t"
    "v_pk_mul_f32 v[208:209], v[208:209], v[238:239] op_sel_hi:[1,0]\n\t"
    "v_pk_mul_f32 v[210:211], v[210:211], v[238:239] op_sel_hi:[1,0]\n\t"
    "ds_write_b128 v230, v[112:115] offset:12416\n\t"
    "s_waitcnt lgkmcnt(3)\n\t"
    "s_add_u32 s66, s54, 8192\n\t"
    "buffer_store_dwordx4 a[96:99], v227, s[48:51], s66 offen offset:0 nt\n\t"
    "v_pk_mul_f32 v[212:213], v[212:213], v[238:239] op_sel_hi:[1,0]\n\t"
    "v_pk_mul_f32 v[214:215], v[214:215], v[238:239] op_sel_hi:[1,0]\n\t"
    "v_pk_mul_f32 v[216:217], v[216:217], v[238:239] op_sel_hi:[1,0]\n\t"
    "v_pk_mul_f32 v[218:219], v[218:219], v[238:239] op_sel_hi:[1,0]\n\t"
    "v_pk_mul_f32 v[220:221], v[220:221], v[238:239] op_sel_hi:[1,0]\n\t"
    "buffer_store_dwordx4 a[100:103], v228, s[48:51], s66 offen offset:1024 nt\n\t"
    "ds_write_b128 v231, v[120:123] offset:12416\n\t"
    "ds_read_b128 a[112:115], v232 offset:12288\n\t"
    "ds_read_b128 a[116:119], v232 offset:13312\n\t"
    "ds_read_b128 a[120:123], v232 offset:14336\n\t"
    "ds_read_b128 a[124:127], v232 offset:15360\n\t"
    "v_pk_mul_f32 v[222:223], v[222:223], v[238:239] op_sel_hi:[1,0]\n\t"
    "v_pk_mul_f32 v[240:241], v[240:241], v[238:239] op_sel_hi:[1,0]\n\t"
    "v_pk_mul_f32 v[242:243], v[242:243], v[238:239] op_sel_hi:[1,0]\n\t"
    "v_pk_mul_f32 v[244:245], v[244:245], v[238:239] op_sel_hi:[1,0]\n\t"
    "buffer_store_dwordx4 a[104:107], v227, s[48:51], s66 offen offset:2048 nt\n\t"
    "v_pk_mul_f32 v[246:247], v[246:247], v[238:239] op_sel_hi:[1,0]\n\t"
    "v_pk_mul_f32 v[248:249], v[248:249], v[238:239] op_sel_hi:[1,0]\n\t"
    "v_pk_mul_f32 v[250:251], v[250:251], v[238:239] op_sel_hi:[1,0]\n\t"
    "v_pk_mul_f32 v[252:253], v[252:253], v[238:239] op_sel_hi:[1,0]\n\t"
    "v_pk_mul_f32 v[254:255], v[254:255], v[238:239] op_sel_hi:[1,0]\n\t"
    "buffer_store_dwordx4 a[108:111], v228, s[48:51], s66 offen offset:3072 nt\n\t"
    "v_cvt_pk_bf16_f32 v208, v208, v209\n\t"
    "v_cvt_pk_bf16_f32 v209, v210, v211\n\t"
    "v_cvt_pk_bf16_f32 v210, v212, v213\n\t"
    "v_cvt_pk_bf16_f32 v211, v214, v215\n\t"
    "ds_write_b128 v230, v[208:211] offset:16384\n\t"
    "v_cvt_pk_bf16_f32 v216, v216, v217\n\t"
    "v_cvt_pk_bf16_f32 v217, v218, v219\n\t"
    "v_cvt_pk_bf16_f32 v218, v220, v221\n\t"
    "v_cvt_pk_bf16_f32 v219, v222, v223\n\t"
    "v_cvt_pk_bf16_f32 v240, v240, v241\n\t"
    "ds_write_b128 v231, v[216:219] offset:16384\n\t"
    "v_cvt_pk_bf16_f32 v241, v242, v243\n\t"
    "s_waitcnt lgkmcnt(2)\n\t"
    "s_add_u32 s66, s54, 12288\n\t"
    "buffer_store_dwordx4 a[112:115], v227, s[48:51], s66 offen offset:0 nt\n\t"
    "v_cvt_pk_bf16_f32 v242, v244, v245\n\t"
    "v_cvt_pk_bf16_f32 v243, v246, v247\n\t"
    "v_cvt_pk_bf16_f32 v248, v248, v249\n\t"
    "v_cvt_pk_bf16_f32 v249, v250, v251\n\t"
    "ds_write_b128 v230, v[240:243] offset:16512\n\t"
    "buffer_store_dwordx4 a[116:119], v228, s[48:51], s66 offen offset:1024 nt\n\t"
    "v_cvt_pk_bf16_f32 v250, v252, v253\n\t"
    "v_cvt_pk_bf16_f32 v251, v254, v255\n\t"
    "v_mfma_f32_16x16x32_bf16 a[244:247], a[248:251], v[152:155], a[244:247]\n\t"
    "v_mfma_f32_16x16x32_bf16 v[176:179], a[32:35], v[152:155], v[176:179]\n\t"
    "v_mfma_f32_16x16x32_bf16 v[180:183], a[36:39], v[152:155], v[180:183]\n\t"
    "buffer_store_dwordx4 a[120:123], v227, s[48:51], s66 offen offset:2048 nt\n\t"
    "ds_write_b128 v231, v[248:251] offset:16512\n\t"
    "ds_read_b128 a[128:131], v232 offset:16384\n\t"
    "ds_read_b128 a[132:135], v232 offset:17408\n\t"
    "ds_read_b128 a[136:139], v232 offset:18432\n\t"
    "ds_read_b128 a[140:143], v232 offset:19456\n\t"
    "v_mfma_f32_16x16x32_bf16 v[184:187], a[40:43], v[152:155], v[184:187]\n\t"
    "v_mfma_f32_16x16x32_bf16 v[188:191], a[44:47], v[152:155], v[188:191]\n\t"
    "v_mfma_f32_16x16x32_bf16 v[192:195], a[208:211], v[152:155], v[192:195]\n\t"
    "v_accvgpr_read_b32 v239, a244\n\t"
    "buffer_store_dwordx4 a[124:127], v228, s[48:51], s66 offen offset:3072 nt\n\t"
    "v_subrev_u32 v237, 0x35800000, v239\n\t"
    "v_rcp_f32 v239, v239\n\t"
    "v_cmp_le_u32 vcc, s62, v237\n\t"
    "v_mfma_f32_16x16x32_bf16 v[196:199], a[212:215], v[152:155], v[196:199]\n\t"
    "s_nop 0\n\t"
    "s_or_b64 s[58:59], s[58:59], vcc\n\t"
    "v_mfma_f32_16x16x32_bf16 v[200:203], a[216:219], v[152:155], v[200:203]\n\t"
    "v_mfma_f32_16x16x32_bf16 v[204:207], a[220:223], v[152:155], v[204:207]\n\t"
    "v_pk_mul_f32 v[176:177], v[176:177], v[238:239] op_sel:[0,1] op_sel_hi:[1,1]\n\t"
    "v_pk_mul_f32 v[178:179], v[178:179], v[238:239] op_sel:[0,1] op_sel_hi:[1,1]\n\t"
    "v_pk_mul_f32 v[180:181], v[180:181], v[238:239] op_sel:[0,1] op_sel_hi:[1,1]\n\t"
    "v_pk_mul_f32 v[182:183], v[182:183], v[238:239] op_sel:[0,1] op_sel_hi:[1,1]\n\t"
    "v_pk_mul_f32 v[184:185], v[184:185], v[238:239] op_sel:[0,1] op_sel_hi:[1,1]\n\t"
    "s_waitcnt lgkmcnt(0)\n\t"
    "s_add_u32 s66, s54, 16384\n\t"
    "buffer_store_dwordx4 a[128:131], v227, s[48:51], s66 offen offset:0 nt\n\t"
    "v_pk_mul_f32 v[186:187], v[186:187], v[238:239] op_sel:[0,1] op_sel_hi:[1,1]\n\t"
    "v_pk_mul_f32 v[188:189], v[188:189], v[238:239] op_sel:[0,1] op_sel_hi:[1,1]\n\t"
    "v_pk_mul_f32 v[190:191], v[190:191], v[238:239] op_sel:[0,1] op_sel_hi:[1,1]\n\t"
    "v_pk_mul_f32 v[192:193], v[192:193], v[238:239] op_sel:[0,1] op_sel_hi:[1,1]\n\t"
    "v_pk_mul_f32 v[194:195], v[194:195], v[238:239] op_sel:[0,1] op_sel_hi:[1,1]\n\t"
    "buffer_store_dwordx4 a[132:135], v228, s[48:51], s66 offen offset:1024 nt\n\t"
    "v_pk_mul_f32 v[196:197], v[196:197], v[238:239] op_sel:[0,1] op_sel_hi:[1,1]\n\t"
    "v_pk_mul_f32 v[198:199], v[198:199], v[238:239] op_sel:[0,1] op_sel_hi:[1,1]\n\t"
    "v_pk_mul_f32 v[200:201], v[200:201], v[238:239] op_sel:[0,1] op_sel_hi:[1,1]\n\t"
    "v_pk_mul_f32 v[202:203], v[202:203], v[238:239] op_sel:[0,1] op_sel_hi:[1,1]\n\t"
    "v_pk_mul_f32 v[204:205], v[204:205], v[238:239] op_sel:[0,1] op_sel_hi:[1,1]\n\t"
    "buffer_store_dwordx4 a[136:139], v227, s[48:51], s66 offen offset:2048 nt\n\t"
    "v_pk_mul_f32 v[206:207], v[206:207], v[238:239] op_sel:[0,1] op_sel_hi:[1,1]\n\t"
    "v_cvt_pk_bf16_f32 v176, v176, v177\n\t"
    "v_cvt_pk_bf16_f32 v177, v178, v179\n\t"
    "v_cvt_pk_bf16_f32 v178, v180, v181\n\t"
    "v_cvt_pk_bf16_f32 v179, v182, v183\n\t"
    "buffer_store_dwordx4 a[140:143], v228, s[48:51], s66 offen offset:3072 nt\n\t"
    "ds_write_b128 v230, v[176:179] offset:20480\n\t"
    "v_cvt_pk_bf16_f32 v184, v184, v185\n\t"
    "v_cvt_pk_bf16_f32 v185, v186, v187\n\t"
    "v_cvt_pk_bf16_f32 v186, v188, v189\n\t"
    "v_cvt_pk_bf16_f32 v187, v190, v191\n\t"
    "v_cvt_pk_bf16_f32 v192, v192, v193\n\t"
    "ds_write_b128 v231, v[184:187] offset:20480\n\t"
    "v_cvt_pk_bf16_f32 v193, v194, v195\n\t"
    "v_cvt_pk_bf16_f32 v194, v196, v197\n\t"
    "v_cvt_pk_bf16_f32 v195, v198, v199\n\t"
    "v_cvt_pk_bf16_f32 v200, v200, v201\n\t"
    "v_cvt_pk_bf16_f32 v201, v202, v203\n\t"
    "ds_write_b128 v230, v[192:195] offset:20608\n\t"
    "v_cvt_pk_bf16_f32 v202, v204, v205\n\t"
    "v_cvt_pk_bf16_f32 v203, v206, v207\n\t"
    "ds_write_b128 v231, v[200:203] offset:20608\n\t"
    "ds_read_b128 a[144:147], v232 offset:20480\n\t"
    "ds_read_b128 a[148:151], v232 offset:21504\n\t"
    "ds_read_b128 a[152:155], v232 offset:22528\n\t"
    "ds_read_b128 a[156:159], v232 offset:23552\n\t"
    "s_waitcnt lgkmcnt(0)\n\t"
    "s_add_u32 s66, s54, 20480\n\t"
    "buffer_store_dwordx4 a[144:147], v227, s[48:51], s66 offen offset:0 nt\n\t"
    "buffer_store_dwordx4 a[148:151], v228, s[48:51], s66 offen offset:1024 nt\n\t"
    "buffer_store_dwordx4 a[152:155], v227, s[48:51], s66 offen offset:2048 nt\n\t"
    "buffer_store_dwordx4 a[156:159], v228, s[48:51], s66 offen offset:3072 nt\n\t"
    "s_cmp_lg_u64 s[58:59], 0\n\t"
    "s_cselect_b32 s66, s55, 0\n\t"
    "s_or_b32 %0, s56, s66\n\t"
    "s_branch .Lend%=\n\t"
    ".Lend%=:\n\t"
    : "=&s"(fail)
    : "s"(Q), "s"(K), "s"(V), "s"(O), "s"(ldsb + (w >> 1) * 4096u + (w & 1u) * 1024u),
      "s"(kv[0]), "s"(kv[1]), "s"(kv[2]), "s"(kv[3]), "s"(qo[0]), "s"(qo[1]), "s"(qo[2]), "s"(qo[3]),
      "s"(ldsb), "s"(ldsb + 65536u + w * 24576u), "s"(w * 8192u + ((c >> 1) & 3u))
    : "v0", "v1", "v2", "v3", "v4", "v5", "v6", "v7", "v8", "v9", "v10", "v11", "v12", "v13", "v14", "v15", "v16", "v17", "v18", "v19", "v20", "v21", "v22", "v23", "v24", "v25", "v26", "v27", "v28", "v29", "v30", "v31", "v32", "v33", "v34", "v35", "v36", "v37", "v38", "v39", "v40", "v41", "v42", "v43", "v44", "v45", "v46", "v47", "v48", "v49", "v50", "v51", "v52", "v53", "v54", "v55", "v56", "v57", "v58", "v59", "v60", "v61", "v62", "v63", "v64", "v65", "v66", "v67", "v68", "v69", "v70", "v71", "v72", "v73", "v74", "v75", "v76", "v77", "v78", "v79", "v80", "v81", "v82", "v83", "v84", "v85", "v86", "v87", "v88", "v89", "v90", "v91", "v92", "v93", "v94", "v95", "v96", "v97", "v98", "v99", "v100", "v101", "v102", "v103", "v104", "v105", "v106", "v107", "v108", "v109", "v110", "v111", "v112", "v113", "v114", "v115", "v116", "v117", "v118", "v119", "v120", "v121", "v122", "v123", "v124", "v125", "v126", "v127", "v128", "v129", "v130", "v131", "v132", "v133", "v134", "v135", "v136", "v137", "v138", "v139", "v140", "v141", "v142", "v143", "v144", "v145", "v146", "v147", "v148", "v149", "v150", "v151", "v152", "v153", "v154", "v155", "v156", "v157", "v158", "v159", "v160", "v161", "v162", "v163", "v164", "v165", "v166", "v167", "v168", "v169", "v170", "v171", "v172", "v173", "v174", "v175", "v176", "v177", "v178", "v179", "v180", "v181", "v182", "v183", "v184", "v185", "v186", "v187", "v188", "v189", "v190", "v191", "v192", "v193", "v194", "v195", "v196", "v197", "v198", "v199", "v200", "v201", "v202", "v203", "v204", "v205", "v206", "v207", "v208", "v209", "v210", "v211", "v212", "v213", "v214", "v215", "v216", "v217", "v218", "v219", "v220", "v221", "v222", "v223", "v224", "v225", "v226", "v227", "v228", "v229", "v230", "v231", "v232", "v233", "v234", "v235", "v236", "v237", "v238", "v239", "v240", "v241", "v242", "v243", "v244", "v245", "v246", "v247", "v248", "v249", "v250", "v251", "v252", "v253", "v254", "v255", "a0", "a1", "a2", "a3", "a4", "a5", "a6", "a7", "a8", "a9", "a10", "a11", "a12", "a13", "a14", "a15", "a16", "a17", "a18", "a19", "a20", "a21", "a22", "a23", "a24", "a25", "a26", "a27", "a28", "a29", "a30", "a31", "a32", "a33", "a34", "a35", "a36", "a37", "a38", "a39", "a40", "a41", "a42", "a43", "a44", "a45", "a46", "a47", "a48", "a49", "a50", "a51", "a52", "a53", "a54", "a55", "a56", "a57", "a58", "a59", "a60", "a61", "a62", "a63", "a64", "a65", "a66", "a67", "a68", "a69", "a70", "a71", "a72", "a73", "a74", "a75", "a76", "a77", "a78", "a79", "a80", "a81", "a82", "a83", "a84", "a85", "a86", "a87", "a88", "a89", "a90", "a91", "a92", "a93", "a94", "a95", "a96", "a97", "a98", "a99", "a100", "a101", "a102", "a103", "a104", "a105", "a106", "a107", "a108", "a109", "a110", "a111", "a112", "a113", "a114", "a115", "a116", "a117", "a118", "a119", "a120", "a121", "a122", "a123", "a124", "a125", "a126", "a127", "a128", "a129", "a130", "a131", "a132", "a133", "a134", "a135", "a136", "a137", "a138", "a139", "a140", "a141", "a142", "a143", "a144", "a145", "a146", "a147", "a148", "a149", "a150", "a151", "a152", "a153", "a154", "a155", "a156", "a157", "a158", "a159", "a160", "a161", "a162", "a163", "a164", "a165", "a166", "a167", "a168", "a169", "a170", "a171", "a172", "a173", "a174", "a175", "a176", "a177", "a178", "a179", "a180", "a181", "a182", "a183", "a184", "a185", "a186", "a187", "a188", "a189", "a190", "a191", "a192", "a193", "a194", "a195", "a196", "a197", "a198", "a199", "a200", "a201", "a202", "a203", "a204", "a205", "a206", "a207", "a208", "a209", "a210", "a211", "a212", "a213", "a214", "a215", "a216", "a217", "a218", "a219", "a220", "a221", "a222", "a223", "a224", "a225", "a226", "a227", "a228", "a229", "a230", "a231", "a232", "a233", "a234", "a235", "a236", "a237", "a238", "a239", "a240", "a241", "a242", "a243", "a244", "a245", "a246", "a247", "a248", "a249", "a250", "a251", "a252", "a253", "a254", "a255", "s36", "s37", "s38", "s39", "s40", "s41", "s42", "s43", "s44", "s45", "s46", "s47", "s48", "s49", "s50", "s51", "s52", "s53", "s54", "s55", "s56", "s57", "s58", "s59", "s60", "s61", "s62", "s63", "s64", "s65", "s66", "s67", "s68", "s69", "s70", "s71", "s72", "s73", "s74", "s75", "s76", "s77", "s78", "s79", "s80", "s81", "s82", "s83", "s84", "s85", "s86", "s87", "s88", "s89", "s90", "s91", "s92", "s93", "s94", "s95", "s96", "s97", "s98", "s99", "vcc", "scc", "memory");
  if (fail) {
    asm volatile("s_waitcnt vmcnt(0)" ::: "memory");
#pragma unroll 1
    for (unsigned r = 0; r < 4; ++r) {
      if ((fail >> r) & 1u) {
        const unsigned j = 32u * r + c, hd = (j >> 1) * 8u + xcd;
        const size_t q0 = (size_t)(hd * 768u + (j & 1u) * 384u + w * 96u) * 256u, k0 = (size_t)hd * 131072u;
        slow_rows(Q + q0, K + k0, V + k0, O + q0);
      }
    }
  }
}

extern "C" void kernel_launch(void* const* d_in, const int* in_sizes, int n_in, void* d_out, int out_size, void* d_ws, size_t ws_size, hipStream_t stream) {
  (void)in_sizes; (void)n_in; (void)out_size; (void)d_ws; (void)ws_size;
  (void)hipFuncSetAttribute((const void*)mha_fwd_kernel, hipFuncAttributeMaxDynamicSharedMemorySize, 163840);
  hipLaunchKernelGGL(mha_fwd_kernel, dim3(256), dim3(256), 163840, stream, (const char*)d_in[0], (const char*)d_in[1], (const char*)d_in[2], (char*)d_out);
}
```
